```python
import torch

def _softmax(x, axis=-1):
    return torch.softmax(x, dim=axis)
def _gelu(x):
    return torch.nn.functional.gelu(x, approximate="tanh")
def _silu(x):
    return torch.nn.functional.silu(x)
def _sigmoid(x):
    return torch.sigmoid(x)
def _layernorm(x, axis=-1, eps=1e-5):
    m = x.mean(dim=axis, keepdim=True)
    v = x.var(dim=axis, keepdim=True, unbiased=False)
    return (x - m) / torch.sqrt(v + eps)
def _scatter_add(tgt, idx, src, axis=0):
    return tgt.clone().index_add_(axis, idx, src)
def _segment_sum(src, seg, n):
    out = torch.zeros((n,) + src.shape[1:], dtype=src.dtype)
    return out.index_add_(0, seg, src)
def _pad(x, pw):
    flat = tuple(v for pair in reversed(pw) for v in pair)
    return torch.nn.functional.pad(x, flat)

kernel_name = 'parallel_sum__compressed_kv_cla__geglu_ffn__decode'

def setup_inputs():

    _w = (lambda a: (lambda t: t.to(torch.bfloat16) if t.is_floating_point() else t)(torch.as_tensor(a)))
    def _A_setup():
        B, H, Sq, Sk, D, M = 8, 32, 1, 4096, 128, 512
        Dc = 4 * D
        return {
            "x": _w((lambda *s: torch.randn(*s, dtype=torch.bfloat16) * 0.05)(B, Sq, M)),
            "c_kv": _w((lambda *s: torch.randn(*s, dtype=torch.bfloat16) * 1.6)(B, Sk, Dc)),
            "w_q": _w((lambda *s: torch.randn(*s, dtype=torch.bfloat16) * 3.2)(M, H, D) * (M ** -0.5)),
            "w_uk": _w((lambda *s: torch.randn(*s, dtype=torch.bfloat16) * 3.2)(Dc, H, D) * (Dc ** -0.5)),
            "w_uv": _w((lambda *s: torch.randn(*s, dtype=torch.bfloat16) * 1.6)(Dc, H, D) * (Dc ** -0.5)),
            "w_o": _w((lambda *s: torch.randn(*s, dtype=torch.bfloat16) * 1.6)(H, D, M) * ((H * D) ** -0.5)),
        }
    _A_in = _A_setup()

    def _B_setup():
        B, S, D, F = 8, 1, 512, 1376
        return {
            "x": _w((lambda *s: torch.randn(*s, dtype=torch.bfloat16) * 0.05)(B, S, D)),
            "w_gate": _w((lambda *s: torch.randn(*s, dtype=torch.bfloat16) * 0.1)(D, F)),
            "w_up": _w((lambda *s: torch.randn(*s, dtype=torch.bfloat16) * 0.1)(D, F)),
            "w_down": _w((lambda *s: torch.randn(*s, dtype=torch.bfloat16) * 0.1)(F, D)),
        }
    _B_in = _B_setup()

    return {
        "A_x": _A_in["x"],
        "A_c_kv": _A_in["c_kv"],
        "A_w_q": _A_in["w_q"],
        "A_w_uk": _A_in["w_uk"],
        "A_w_uv": _A_in["w_uv"],
        "A_w_o": _A_in["w_o"],
        "B_x": _B_in["x"],
        "B_w_gate": _B_in["w_gate"],
        "B_w_up": _B_in["w_up"],
        "B_w_down": _B_in["w_down"],
    }

def reference(A_x, A_c_kv, A_w_q, A_w_uk, A_w_uv, A_w_o, B_x, B_w_gate, B_w_up, B_w_down):
    def _A_ref(x, c_kv, w_q, w_uk, w_uv, w_o):
        B, Sq, M = x.shape[0], x.shape[1], x.shape[2]
        Sk = c_kv.shape[1]
        H, D = w_q.shape[1], w_q.shape[2]
        q = torch.einsum("bqm,mhd->bhqd", x, w_q)
        k = torch.einsum("bkc,chd->bhkd", c_kv, w_uk)
        v = torch.einsum("bkc,chd->bhkd", c_kv, w_uv)
        scores = torch.einsum("bhqd,bhkd->bhqk", q, k) * (D ** -0.5)
        off = Sk - Sq
        qi = torch.arange(Sq)[:, None]
        kj = torch.arange(Sk)[None, :]
        cmask = kj <= (qi + off)
        scores = torch.where(cmask[None, None], scores, -1e9)
        p = _softmax(scores, axis=-1)
        ctx = torch.einsum("bhqk,bhkd->bhqd", p, v)
        return torch.einsum("bhqd,hdm->bqm", ctx, w_o)

    def _B_ref(x, w_gate, w_up, w_down):
        return (_gelu(x @ w_gate) * (x @ w_up)) @ w_down

    x = A_x
    xn = _layernorm(x, axis=-1)
    return x + _A_ref(xn, A_c_kv, A_w_q, A_w_uk, A_w_uv, A_w_o) + _B_ref(xn, B_w_gate, B_w_up, B_w_down)
```

```cpp
#include <hip/hip_runtime.h>
#include <hip/hip_bf16.h>
#include <stdint.h>

#define MIMIC 1

typedef unsigned short u16;
__device__ __forceinline__ float bf2f(u16 v) { return __uint_as_float(((unsigned)v) << 16); }
__device__ __forceinline__ u16 f2bf(float f) {
  unsigned u = __float_as_uint(f);
  u += 0x7FFFu + ((u >> 16) & 1u);
  return (u16)(u >> 16);
}
__device__ __forceinline__ float rbf(float f) {
  return bf2f(f2bf(f));
}

constexpr int B = 8, H = 32, SK = 4096, D = 128, M = 512, DC = 512, F = 1376;
constexpr int NSPLIT = 32, KPS = SK / NSPLIT;

__device__ void layernorm_rows(const u16* x, float* xn  , float* red) {
  int t = threadIdx.x, lane = t & 63, w = t >> 6;
  for (int r = w; r < B; r += 4) {
    float v[8];
    float s = 0.f;
    for (int j = 0; j < 8; ++j) { v[j] = bf2f(x[r * M + lane * 8 + j]); s += v[j]; }
    for (int o = 32; o > 0; o >>= 1) s += __shfl_xor(s, o);
    float mean = s / M;
    float ss = 0.f;
    for (int j = 0; j < 8; ++j) { float d = v[j] - mean; ss += d * d; }
    for (int o = 32; o > 0; o >>= 1) ss += __shfl_xor(ss, o);
    float var = ss / M;
    float mb = rbf(mean), vb = rbf(var);
    float den = rbf(sqrtf(rbf(vb + 1e-5f)));
    for (int j = 0; j < 8; ++j) xn[r * M + lane * 8 + j] = rbf(rbf(v[j] - mb) / den);
  }
  __syncthreads();
}

__global__ void __launch_bounds__(256) k1(const u16* x, const u16* wq, const u16* wuk, const u16* wg, const u16* wu,
                                          float* qabs, float* hid) {
  __shared__ float xn[B * M];
  __shared__ float q[B * D];
  __shared__ float red[8];
  layernorm_rows(x, xn, red);
  int t = threadIdx.x;
  if (blockIdx.x < H) {
    int h = blockIdx.x;
    int d = t & 127, hb = t >> 7;
    float acc[4] = {0, 0, 0, 0};
    for (int m = 0; m < M; ++m) {
      float w = bf2f(wq[(size_t)m * H * D + h * D + d]);
      for (int j = 0; j < 4; ++j) acc[j] += xn[(hb * 4 + j) * M + m] * w;
    }
    for (int j = 0; j < 4; ++j) q[(hb * 4 + j) * D + d] = rbf(acc[j]);
    __syncthreads();
    for (int c = t; c < DC; c += 256) {
      float a[8] = {0, 0, 0, 0, 0, 0, 0, 0};
      for (int dd = 0; dd < D; ++dd) {
        float w = bf2f(wuk[(size_t)c * H * D + h * D + dd]);
        for (int b = 0; b < 8; ++b) a[b] += q[b * D + dd] * w;
      }
      for (int b = 0; b < 8; ++b) qabs[((size_t)b * H + h) * DC + c] = a[b];
    }
  } else {
    int f = (blockIdx.x - H) * 256 + t;
    if (f < F) {
      float g[8] = {0, 0, 0, 0, 0, 0, 0, 0}, u[8] = {0, 0, 0, 0, 0, 0, 0, 0};
      for (int m = 0; m < M; ++m) {
        float a = bf2f(wg[(size_t)m * F + f]), c = bf2f(wu[(size_t)m * F + f]);
        for (int b = 0; b < 8; ++b) { g[b] += xn[b * M + m] * a; u[b] += xn[b * M + m] * c; }
      }
      for (int b = 0; b < 8; ++b) {
        float gg = rbf(g[b]), uu = rbf(u[b]);
        float inner = 0.7978845608028654f * (gg + 0.044715f * gg * gg * gg);
        float ge = rbf(0.5f * gg * (1.f + tanhf(inner)));
        hid[b * F + f] = rbf(ge * uu);
      }
    }
  }
}

__global__ void __launch_bounds__(256) k3(const u16* ckv, const float* qabs, float* part  , float* pm, float* pl) {
  __shared__ float qs[H * DC];
  __shared__ float sc[H * KPS];
  int b = blockIdx.y, sp = blockIdx.x, t = threadIdx.x;
  for (int i = t; i < H * DC; i += 256) qs[i] = qabs[(size_t)b * H * DC + i];
  __syncthreads();
  const u16* kv = ckv + ((size_t)b * SK + sp * KPS) * DC;
  {
    int k = t & 127, hh = t >> 7;
    float acc[16];
    for (int j = 0; j < 16; ++j) acc[j] = 0.f;
    for (int c = 0; c < DC; ++c) {
      float v = bf2f(kv[(size_t)k * DC + c]);
      for (int j = 0; j < 16; ++j) acc[j] += qs[(hh * 16 + j) * DC + c] * v;
    }
    for (int j = 0; j < 16; ++j) sc[(hh * 16 + j) * KPS + k] = rbf(rbf(acc[j]) * rbf(0.08838834764831845f));
  }
  __syncthreads();
  if (t < H) {
    float m = -1e30f;
    for (int k = 0; k < KPS; ++k) m = fmaxf(m, sc[t * KPS + k]);
    float l = 0.f;
    for (int k = 0; k < KPS; ++k) { float e = __expf(sc[t * KPS + k] - m); sc[t * KPS + k] = e; l += e; }
    pm[(b * NSPLIT + sp) * H + t] = m;
    pl[(b * NSPLIT + sp) * H + t] = l;
  }
  __syncthreads();
  for (int c = t; c < DC; c += 256) {
    float acc[32];
    for (int j = 0; j < 32; ++j) acc[j] = 0.f;
    for (int k = 0; k < KPS; ++k) {
      float v = bf2f(kv[(size_t)k * DC + c]);
      for (int j = 0; j < 32; ++j) acc[j] += sc[j * KPS + k] * v;
    }
    for (int j = 0; j < 32; ++j) part[(((size_t)b * NSPLIT + sp) * H + j) * DC + c] = acc[j];
  }
}

__global__ void __launch_bounds__(256) k4(const float* part, const float* pm, const float* pl, const u16* wuv, float* ctx) {
  __shared__ float cl[DC];
  __shared__ float wsc[NSPLIT];
  __shared__ float r2[256];
  int h = blockIdx.x, b = blockIdx.y, t = threadIdx.x;
  if (t == 0) {
    float m = -1e30f;
    for (int s = 0; s < NSPLIT; ++s) m = fmaxf(m, pm[(b * NSPLIT + s) * H + h]);
    float l = 0.f;
    for (int s = 0; s < NSPLIT; ++s) { float e = __expf(pm[(b * NSPLIT + s) * H + h] - m); wsc[s] = e; l += e * pl[(b * NSPLIT + s) * H + h]; }
    for (int s = 0; s < NSPLIT; ++s) wsc[s] /= l;
  }
  __syncthreads();
  for (int c = t; c < DC; c += 256) {
    float a = 0.f;
    for (int s = 0; s < NSPLIT; ++s) a += wsc[s] * part[(((size_t)b * NSPLIT + s) * H + h) * DC + c];
    cl[c] = a;
  }
  __syncthreads();
  int d = t & 127, hf = t >> 7;
  float a = 0.f;
  for (int c = hf * 256; c < hf * 256 + 256; ++c) a += cl[c] * bf2f(wuv[(size_t)c * H * D + h * D + d]);
  r2[t] = a;
  __syncthreads();
  if (t < 128) ctx[((size_t)b * H + h) * D + t] = rbf(r2[t] + r2[t + 128]);
}

__global__ void __launch_bounds__(256) k5(const u16* x, const float* ctx, const float* hid, const u16* wo, const u16* wd, u16* out) {
  __shared__ float r1[256], r2[256];
  int b = blockIdx.y, m = blockIdx.x * 64 + (threadIdx.x & 63), g = threadIdx.x >> 6;
  float a = 0.f;
  for (int k = g * 1024; k < g * 1024 + 1024; ++k) a += ctx[b * H * D + k] * bf2f(wo[(size_t)k * M + m]);
  float f = 0.f;
  for (int k = g * 344; k < g * 344 + 344; ++k) f += hid[b * F + k] * bf2f(wd[(size_t)k * M + m]);
  r1[threadIdx.x] = a; r2[threadIdx.x] = f;
  __syncthreads();
  if (g == 0) {
    int l = threadIdx.x;
    float A = rbf(r1[l] + r1[l + 64] + r1[l + 128] + r1[l + 192]);
    float Ff = rbf(r2[l] + r2[l + 64] + r2[l + 128] + r2[l + 192]);
    float o = rbf(bf2f(x[b * M + m]) + A) + Ff;
    out[b * M + m] = f2bf(o);
  }
}

extern "C" void kernel_launch(void* const* d_in, const int* in_sizes, int n_in, void* d_out, int out_size, void* d_ws,
                              size_t ws_size, hipStream_t stream) {
  const u16* x = (const u16*)d_in[0];
  const u16* ckv = (const u16*)d_in[1];
  const u16* wq = (const u16*)d_in[2];
  const u16* wuk = (const u16*)d_in[3];
  const u16* wuv = (const u16*)d_in[4];
  const u16* wo = (const u16*)d_in[5];
  const u16* wg = (const u16*)d_in[7];
  const u16* wu = (const u16*)d_in[8];
  const u16* wd = (const u16*)d_in[9];
  float* ws = (float*)d_ws;
  float* qabs = ws;
  float* hid = qabs + B * H * DC;
  float* ctx = hid + 16384;
  float* pm = ctx + B * H * D;
  float* pl = pm + B * NSPLIT * H;
  float* part = pl + B * NSPLIT * H;
  k1<<<H + (F + 255) / 256, 256, 0, stream>>>(x, wq, wuk, wg, wu, qabs, hid);
  k3<<<dim3(NSPLIT, B), 256, 0, stream>>>(ckv, qabs, part, pm, pl);
  k4<<<dim3(H, B), 256, 0, stream>>>(part, pm, pl, wuv, ctx);
  k5<<<dim3(M / 64, B), 256, 0, stream>>>(x, ctx, hid, wo, wd, (u16*)d_out);
}
```

```cpp
#include <hip/hip_runtime.h>
#include <stdint.h>

typedef unsigned short u16;
typedef unsigned long long u64;
typedef unsigned v4u __attribute__((ext_vector_type(4)));
typedef unsigned v2u __attribute__((ext_vector_type(2)));
typedef float f32x4 __attribute__((ext_vector_type(4)));
typedef float f32x16 __attribute__((ext_vector_type(16)));
typedef float v2f __attribute__((ext_vector_type(2)));
typedef short bf16x8 __attribute__((ext_vector_type(8)));
typedef short v4s __attribute__((ext_vector_type(4)));
typedef __bf16 v2bf __attribute__((ext_vector_type(2)));
#define GAS __attribute__((address_space(1)))
#define LAS __attribute__((address_space(3)))
#define DI __device__ __forceinline__

#define STAMPS 0

constexpr int NB = 8, NH = 32, SK = 4096, HD = 128, DM = 512, DC = 512, FF = 1376;
constexpr unsigned WS_GEN = 0;
constexpr unsigned WS_FLAGB = 1024;
constexpr unsigned WS_HINTE = 4096;
constexpr unsigned WS_ABUF = 8192;
constexpr unsigned WS_QABS = WS_ABUF + 262144;
constexpr unsigned WS_ML = WS_QABS + 262144;
constexpr unsigned WS_OUTE = WS_ML + 65536;
constexpr unsigned WS_OUTC = WS_OUTE + 256 * 8192;
constexpr unsigned WS_END = WS_OUTC + 256 * 32768;
constexpr unsigned WS_STAMP = WS_END;
constexpr unsigned L_KV = 0;
constexpr unsigned L_SC = 131072;
constexpr unsigned L_XN = L_SC;
constexpr unsigned L_WQ = L_SC + 8448;
constexpr unsigned L_QP = L_SC + 8448 + 16384;
constexpr unsigned L_QA = L_SC;
constexpr unsigned L_P = L_SC;
constexpr unsigned L_LS = L_SC + 8192;
constexpr unsigned L_MISC = L_SC + 31744;
constexpr unsigned L_MX = L_MISC + 64;
constexpr unsigned L_WUV = 32768;
constexpr unsigned L_GU = 49152;
constexpr unsigned L_CL = 65536;
constexpr unsigned L_CTX = 65536 + 4096;
constexpr unsigned L_FP = 65536 + 12288;
constexpr unsigned L_FIN = 65536 + 16384;
constexpr unsigned LDS_BYTES = 163840;
enum { C_POISON = 0, C_BREADY, C_QA, C_KV0, C_KV1, C_KV2, C_KV3, C_MX, C_P, C_PV, C_HB, C_FIN, C_N };

constexpr unsigned POISON = 0xFFFFFFFFu;
constexpr unsigned SPIN_CAP = 1u << 22;

struct Params {
  const u16 *x, *ckv, *wq, *wuk, *wuv, *wo, *wg, *wu, *wd;
  u16* out;
  unsigned char* ws;
};

DI float bf2f(unsigned v) { return __uint_as_float(v << 16); }
DI float bfhi(unsigned v) { return __uint_as_float(v & 0xFFFF0000u); }
DI unsigned pk2(float a, float b) {
  v2f t = {a, b};
  return __builtin_bit_cast(unsigned, __builtin_convertvector(t, v2bf));
}
DI float rbf(float a) { return bf2f(pk2(a, 0.f) & 0xFFFFu); }
DI unsigned san(unsigned v) { return v == POISON ? 0x7FC07FC0u : v; }
DI f32x4 mfma16(v4u a, v4u b, f32x4 c) {
  return __builtin_amdgcn_mfma_f32_16x16x32_bf16(__builtin_bit_cast(bf16x8, a), __builtin_bit_cast(bf16x8, b), c, 0, 0, 0);
}
DI f32x16 mfma32(v4u a, v4u b, f32x16 c) {
  return __builtin_amdgcn_mfma_f32_32x32x16_bf16(__builtin_bit_cast(bf16x8, a), __builtin_bit_cast(bf16x8, b), c, 0, 0, 0);
}
DI v2u trread(LAS unsigned char* L, unsigned off) {
  return __builtin_bit_cast(v2u, __builtin_amdgcn_ds_read_tr16_b64_v4i16((LAS v4s*)(L + off)));
}
DI v4u lds128(LAS unsigned char* L, unsigned off) { return *(LAS v4u*)(L + off); }
template <bool NT> DI void glds16(const void* gsrc, unsigned lds_dst) {
  unsigned keep;
  if (NT) asm volatile("s_mov_b32 %0, m0\n\ts_mov_b32 m0, %2\n\ts_nop 0\n\tglobal_load_lds_dwordx4 %1, off nt\n\ts_mov_b32 m0, %0" : "=&s"(keep) : "v"(gsrc), "s"(lds_dst) : "memory");
  else asm volatile("s_mov_b32 %0, m0\n\ts_mov_b32 m0, %2\n\ts_nop 0\n\tglobal_load_lds_dwordx4 %1, off\n\ts_mov_b32 m0, %0" : "=&s"(keep) : "v"(gsrc), "s"(lds_dst) : "memory");
}
#define VMWAIT(n) asm volatile("s_waitcnt vmcnt(%0)" ::"i"(n) : "memory")
#define LGKM0() asm volatile("s_waitcnt lgkmcnt(0)" ::: "memory")
#define CFENCE() asm volatile("" ::: "memory")
#define SB0() __builtin_amdgcn_sched_barrier(0)
DI void wg_barrier() {
  LGKM0();
  __builtin_amdgcn_s_barrier();
  CFENCE();
}
DI void cnt_add(LAS unsigned char* L, int c, int lane) {
  LGKM0();
  if (lane == 0) __hip_atomic_fetch_add((LAS unsigned*)(L + L_MISC) + c, 1u, __ATOMIC_RELAXED, __HIP_MEMORY_SCOPE_WORKGROUP);
  CFENCE();
}
DI void cnt_wait(LAS unsigned char* L, int c, unsigned target) {
  CFENCE();
  for (unsigned s = 0; s < SPIN_CAP * 4u; ++s) {
    unsigned v = __builtin_amdgcn_readfirstlane(__hip_atomic_load((LAS unsigned*)(L + L_MISC) + c, __ATOMIC_RELAXED, __HIP_MEMORY_SCOPE_WORKGROUP));
    if (v >= target) break;
    __builtin_amdgcn_s_sleep(0);
  }
  CFENCE();
}
DI v4u ld_sc1(__amdgpu_buffer_rsrc_t rs, unsigned off) { return __builtin_amdgcn_raw_buffer_load_b128(rs, off, 0, 16); }
DI v2u ld_sc1_64(__amdgpu_buffer_rsrc_t rs, unsigned off) { return __builtin_amdgcn_raw_buffer_load_b64(rs, off, 0, 16); }
DI void st_sc1(__amdgpu_buffer_rsrc_t rs, unsigned off, v4u v) { __builtin_amdgcn_raw_buffer_store_b128(v, rs, off, 0, 16); }
DI void st_sc1_64(__amdgpu_buffer_rsrc_t rs, unsigned off, v2u v) { __builtin_amdgcn_raw_buffer_store_b64(v, rs, off, 0, 16); }
DI unsigned fswz(unsigned row) { return ((row & 3u) << 2) | ((row >> 2) & 3u); }
DI float shx(float v, int m) { return __shfl_xor(v, m); }

DI void transpose8(const v4u (&r)[8], v4u (&c)[8]) {
#pragma unroll
  for (int q = 0; q < 4; ++q) {
#pragma unroll
    for (int e2 = 0; e2 < 4; ++e2) {
      unsigned a = r[2 * e2][q], b = r[2 * e2 + 1][q];
      c[2 * q][e2] = __builtin_amdgcn_perm(b, a, 0x05040100u);
      c[2 * q + 1][e2] = __builtin_amdgcn_perm(b, a, 0x07060302u);
    }
  }
}

#define STAMP(k) do {} while (0)

__global__ void __launch_bounds__(512, 2) fused_kernel(Params p) {
  extern __shared__ __attribute__((aligned(16))) unsigned char smem[];
  LAS unsigned char* L = (LAS unsigned char*)smem;
  const unsigned lbase = (unsigned)(size_t)L;
  const int tid = threadIdx.x, lane = tid & 63;
  const int w = __builtin_amdgcn_readfirstlane(tid >> 6);
  const int cu = blockIdx.x;
  const int xcd = cu & 7, r8 = cu >> 3;
  const int h = xcd * 4 + (r8 & 3), j = r8 >> 2;
  const int batt = cu >> 5, satt = cu & 31;
  const int oc0 = (43 * xcd + 1) >> 1, oc1 = (43 * (xcd + 1) + 1) >> 1;
  const bool has_ffn = (oc0 + r8) < oc1;
  const int oct = has_ffn ? oc0 + r8 : 0;
  const __amdgpu_buffer_rsrc_t rs = __builtin_amdgcn_make_buffer_rsrc((void*)p.ws, 0, (int)WS_END, 0x00020000);
  const int g4 = lane >> 4, i16 = lane & 15;
  STAMP(0);

  if (w < 4) {
    const int k = w;
    v4u xa, xb;
    asm volatile("global_load_dwordx4 %0, %2, off\n\tglobal_load_dwordx4 %1, %2, off offset:1024" : "=&v"(xa), "=&v"(xb) : "v"(p.x + (2 * k) * DM + lane * 8) : "memory");
    const v2u genv = ld_sc1_64(rs, WS_GEN);
    SB0();
    {
      const char* src = (const char*)p.wq + (size_t)(128 * k + (lane >> 1)) * 8192 + h * 256 + j * 32 + (lane & 1) * 16;
#pragma unroll
      for (int t = 0; t < 4; ++t) glds16<false>(src + (size_t)t * 32 * 8192, lbase + L_WQ + (128 * k + 32 * t) * 32);
    }
    SB0();
    v4u wukf[4][4];
    unsigned xfin = 0;
    if (k == 0) {
#pragma unroll
      for (int t = 0; t < 4; ++t)
#pragma unroll
        for (int s = 0; s < 4; ++s)
          wukf[t][s] = __builtin_nontemporal_load((const GAS v4u*)((const char*)p.wuk + (size_t)(64 * j + 16 * (i16 >> 2) + 4 * t + (lane & 3)) * 8192 + h * 256 + s * 64 + g4 * 16));
      xfin = p.x[cu * 16 + i16];
      SB0();
      asm volatile("s_waitcnt vmcnt(22)" : "+v"(xa), "+v"(xb)::"memory");
    } else {
      const v4u pz = {POISON, POISON, POISON, POISON};
      if (k < 3) {
#pragma unroll
        for (int t = 0; t < 16; ++t) st_sc1(rs, WS_OUTC + cu * 32768 + ((k - 1) * 16 + t) * 1024 + lane * 16, pz);
        SB0();
        asm volatile("s_waitcnt vmcnt(21)" : "+v"(xa), "+v"(xb)::"memory");
      } else {
#pragma unroll
        for (int t = 0; t < 8; ++t) st_sc1(rs, WS_OUTE + cu * 8192 + t * 1024 + lane * 16, pz);
        if (lane < 16) st_sc1(rs, WS_ML + cu * 256 + lane * 16, pz);
        SB0();
        asm volatile("s_waitcnt vmcnt(14)" : "+v"(xa), "+v"(xb)::"memory");
      }
    }
    SB0();
#pragma unroll
    for (int rr = 0; rr < 2; ++rr) {
      const v4u xv = rr ? xb : xa;
      float v[8];
#pragma unroll
      for (int e = 0; e < 4; ++e) { v[2 * e] = bf2f(xv[e] & 0xFFFFu); v[2 * e + 1] = bfhi(xv[e]); }
      float s = 0.f;
#pragma unroll
      for (int e = 0; e < 8; ++e) s += v[e];
#pragma unroll
      for (int o = 32; o > 0; o >>= 1) s += shx(s, o);
      const float mean = s * (1.f / DM);
      float ss = 0.f;
#pragma unroll
      for (int e = 0; e < 8; ++e) { float d = v[e] - mean; ss += d * d; }
#pragma unroll
      for (int o = 32; o > 0; o >>= 1) ss += shx(ss, o);
      const float mb = rbf(mean), vb = rbf(ss * (1.f / DM));
      const float den = rbf(sqrtf(rbf(vb + 1e-5f)));
      v4u o4;
#pragma unroll
      for (int e = 0; e < 4; ++e) o4[e] = pk2(rbf(v[2 * e] - mb) / den, rbf(v[2 * e + 1] - mb) / den);
      *(LAS v4u*)(L + L_XN + (2 * k + rr) * 1040 + lane * 16) = o4;
    }
    if (tid < C_N) ((LAS unsigned*)(L + L_MISC))[tid] = 0u;
    wg_barrier();
    STAMP(1);
    if (k == 0) VMWAIT(17); else if (k < 3) VMWAIT(16); else VMWAIT(9);
    {
      f32x4 acc = {0.f, 0.f, 0.f, 0.f};
#pragma unroll
      for (int t = 0; t < 4; ++t) {
        const int ks = 4 * k + t;
        const v4u a = lds128(L, L_XN + (lane & 7) * 1040 + (32 * ks + 8 * g4) * 2);
        const unsigned ro = L_WQ + (32 * ks + 8 * g4 + (i16 >> 2)) * 32 + (lane & 3) * 8;
        const v2u b0 = trread(L, ro), b1 = trread(L, ro + 4 * 32);
        const v4u b = {b0[0], b0[1], b1[0], b1[1]};
        acc = mfma16(a, b, acc);
      }
      if (lane < 32) *(LAS f32x4*)(L + L_QP + k * 512 + lane * 16) = acc;
    }
    wg_barrier();
    STAMP(2);
    const u64 gen1 = (((u64)genv[1] << 32) | genv[0]) + 1ull;
    const unsigned tagA = (unsigned)(gen1 & 0xFFFFull) << 16, tagB = (unsigned)(gen1 >> 16);
    if (k == 0) {
      if (lane == 0) ((LAS unsigned*)(L + L_MISC))[15] = (unsigned)gen1;
      if (lane < 32) {
        f32x4 q = *(LAS f32x4*)(L + L_QP + lane * 16);
#pragma unroll
        for (int kk = 1; kk < 4; ++kk) q += *(LAS f32x4*)(L + L_QP + kk * 512 + lane * 16);
#pragma unroll
        for (int e = 0; e < 4; ++e) {
          const v2u gr = {tagA | (pk2(q[e], 0.f) & 0xFFFFu), tagB};
          st_sc1_64(rs, WS_ABUF + ((h * 8 + 4 * g4 + e) * 128 + 16 * j + i16) * 8, gr);
        }
      }
      STAMP(3);
      v4u qfr[4];
      {
        const unsigned base = WS_ABUF + ((h * 8 + (lane & 7)) * 128 + 8 * g4) * 8;
        for (unsigned spin = 0; spin < SPIN_CAP; ++spin) {
          v4u gq[4][4];
#pragma unroll
          for (int s = 0; s < 4; ++s)
#pragma unroll
            for (int e = 0; e < 4; ++e) gq[s][e] = ld_sc1(rs, base + s * 256 + e * 16);
          unsigned bad = 0;
#pragma unroll
          for (int s = 0; s < 4; ++s)
#pragma unroll
            for (int e = 0; e < 4; ++e) {
              bad |= ((gq[s][e][0] ^ tagA) & 0xFFFF0000u) | (gq[s][e][1] ^ tagB) | ((gq[s][e][2] ^ tagA) & 0xFFFF0000u) | (gq[s][e][3] ^ tagB);
              qfr[s][e] = __builtin_amdgcn_perm(gq[s][e][2], gq[s][e][0], 0x05040100u);
            }
          if (__all(bad == 0)) break;
          __builtin_amdgcn_s_sleep(1);
        }
      }
      STAMP(4);
      f32x4 d4[4];
#pragma unroll
      for (int t = 0; t < 4; ++t) {
        d4[t] = f32x4{0.f, 0.f, 0.f, 0.f};
#pragma unroll
        for (int s = 0; s < 4; ++s) d4[t] = mfma16(wukf[t][s], qfr[s], d4[t]);
      }
      if (i16 < 8) {
#pragma unroll
        for (int hh = 0; hh < 2; ++hh) {
          const v4u v = {pk2(d4[2 * hh][0], d4[2 * hh][1]), pk2(d4[2 * hh][2], d4[2 * hh][3]), pk2(d4[2 * hh + 1][0], d4[2 * hh + 1][1]),
                         pk2(d4[2 * hh + 1][2], d4[2 * hh + 1][3])};
          st_sc1(rs, WS_QABS + ((i16 * 32 + 4 * j + g4) * 64 + hh * 32 + h) * 16, v);
        }
      }
      cnt_wait(L, C_POISON, 3);
      VMWAIT(0);
      if (lane == 0) st_sc1_64(rs, WS_FLAGB + (j * 32 + h) * 8, v2u{(unsigned)gen1, (unsigned)(gen1 >> 32)});
      STAMP(5);
    } else {
      VMWAIT(0);
      cnt_add(L, C_POISON, lane);
    }
    if (k == 1) {
      for (unsigned spin = 0; spin < SPIN_CAP; ++spin) {
        const v4u f0 = ld_sc1(rs, WS_FLAGB + lane * 32), f1 = ld_sc1(rs, WS_FLAGB + lane * 32 + 16);
        const unsigned lo = (unsigned)gen1, hi = (unsigned)(gen1 >> 32);
        const unsigned bad = (f0[0] ^ lo) | (f0[1] ^ hi) | (f0[2] ^ lo) | (f0[3] ^ hi) | (f1[0] ^ lo) | (f1[1] ^ hi) | (f1[2] ^ lo) | (f1[3] ^ hi);
        if (__all(bad == 0)) break;
        __builtin_amdgcn_s_sleep(2);
      }
      cnt_add(L, C_BREADY, lane);
      if (cu == 0 && lane == 0) st_sc1_64(rs, WS_GEN, v2u{(unsigned)gen1, (unsigned)(gen1 >> 32)});
    } else {
      cnt_wait(L, C_BREADY, 1);
    }
    STAMP(6);
    v4u qf[32];
    {
      v4u tmp[8];
#pragma unroll
      for (int t = 0; t < 8; ++t) tmp[t] = ld_sc1(rs, WS_QABS + ((batt * 32 + 8 * k + t) * 64 + lane) * 16);
      qf[31] = ld_sc1(rs, WS_QABS + ((batt * 32 + 31) * 64 + lane) * 16);
#pragma unroll
      for (int t = 0; t < 8; ++t)
        if (8 * k + t < 31) *(LAS v4u*)(L + L_QA + (8 * k + t) * 1024 + lane * 16) = tmp[t];
    }
    cnt_add(L, C_QA, lane);
    cnt_wait(L, C_QA, 4);
#pragma unroll
    for (int s = 0; s < 31; ++s) qf[s] = lds128(L, L_QA + s * 1024 + lane * 16);
    STAMP(7);
    cnt_wait(L, C_KV0 + k, 1);
    STAMP(8);
    f32x16 sacc;
#pragma unroll
    for (int e = 0; e < 16; ++e) sacc[e] = 0.f;
    {
      const unsigned key = 32 * k + (lane & 31);
      const unsigned rowoff = L_KV + key * 1024, fz = fswz(key), hh = lane >> 5;
      unsigned ad[8];
#pragma unroll
      for (int s = 0; s < 8; ++s) ad[s] = rowoff + (((2 * s + hh) ^ fz) * 16);
      v4u ab[2][8];
#pragma unroll
      for (int s = 0; s < 8; ++s) ab[0][s] = lds128(L, ad[s]);
#pragma unroll
      for (int blk = 0; blk < 4; ++blk) {
        if (blk < 3) {
#pragma unroll
          for (int s = 0; s < 8; ++s) ab[(blk + 1) & 1][s] = lds128(L, ad[s] + (blk + 1) * 256);
        }
        SB0();
#pragma unroll
        for (int s = 0; s < 8; ++s) sacc = mfma32(ab[blk & 1][s], qf[8 * blk + s], sacc);
        SB0();
      }
    }
    const float scale_b = 0.08837890625f;
    float sv[16];
    float mw = -3.0e38f;
#pragma unroll
    for (int e = 0; e < 16; ++e) { sv[e] = rbf(rbf(sacc[e]) * scale_b); mw = fmaxf(mw, sv[e]); }
    mw = fmaxf(mw, shx(mw, 32));
    if (lane < 32) ((LAS float*)(L + L_MX))[k * 32 + lane] = mw;
    cnt_add(L, C_MX, lane);
    cnt_wait(L, C_MX, 4);
    float mcu;
    {
      const LAS float* mx = (const LAS float*)(L + L_MX) + (lane & 31);
      mcu = fmaxf(fmaxf(mx[0], mx[32]), fmaxf(mx[64], mx[96]));
    }
    float lw = 0.f;
#pragma unroll
    for (int e = 0; e < 16; ++e) { sv[e] = __expf(sv[e] - mcu); lw += sv[e]; }
    lw += shx(lw, 32);
    if (lane < 32) ((LAS float*)(L + L_LS))[k * 32 + lane] = lw;
#pragma unroll
    for (int g = 0; g < 4; ++g) {
      const v2u pv = {pk2(sv[4 * g], sv[4 * g + 1]), pk2(sv[4 * g + 2], sv[4 * g + 3])};
      *(LAS v2u*)(L + L_P + (2 * k + (g >> 1)) * 1024 + ((g & 1) * 32 + (lane & 31)) * 16 + (lane >> 5) * 8) = pv;
    }
    cnt_add(L, C_P, lane);
    cnt_wait(L, C_P, 4);
    STAMP(9);
    v4u pf[8];
#pragma unroll
    for (int ks = 0; ks < 8; ++ks) pf[ks] = lds128(L, L_P + ks * 1024 + lane * 16);
    float lcu;
    {
      const LAS float* ls = (const LAS float*)(L + L_LS) + (lane & 31);
      lcu = (ls[0] + ls[32]) + (ls[64] + ls[96]);
    }
    f32x16 oacc[4];
    {
      const unsigned hh = lane >> 5, blk = (lane >> 4) & 1, q = i16 >> 2, pp = lane & 3;
      const unsigned r0 = 8 * hh + q, r1 = r0 + 4;
      v2u tb[2][16];
      auto ldt = [&](int ct, v2u (&dst)[16]) {
        const unsigned ch = 16 * k + 4 * ct + 2 * (pp & 1) + blk;
        const unsigned a0 = L_KV + r0 * 1024 + ((ch ^ fswz(r0)) * 16) + 8 * (pp >> 1);
        const unsigned a1 = L_KV + r1 * 1024 + ((ch ^ fswz(r1)) * 16) + 8 * (pp >> 1);
#pragma unroll
        for (int ks = 0; ks < 8; ++ks) { dst[2 * ks] = trread(L, a0 + ks * 16384); dst[2 * ks + 1] = trread(L, a1 + ks * 16384); }
      };
      ldt(0, tb[0]);
#pragma unroll
      for (int ct = 0; ct < 4; ++ct) {
        if (ct < 3) ldt(ct + 1, tb[(ct + 1) & 1]);
        SB0();
#pragma unroll
        for (int e = 0; e < 16; ++e) oacc[ct][e] = 0.f;
#pragma unroll
        for (int ks = 0; ks < 8; ++ks) {
          const v4u a = {tb[ct & 1][2 * ks][0], tb[ct & 1][2 * ks][1], tb[ct & 1][2 * ks + 1][0], tb[ct & 1][2 * ks + 1][1]};
          oacc[ct] = mfma32(a, pf[ks], oacc[ct]);
        }
        SB0();
      }
    }
    STAMP(10);
    {
      const unsigned hd = lane & 31, hh = lane >> 5;
      LGKM0();
#pragma unroll
      for (int ct = 0; ct < 4; ++ct)
#pragma unroll
        for (int u = 0; u < 2; ++u) {
          const v4u v = {san(pk2(oacc[ct][8 * u], oacc[ct][8 * u + 1])), san(pk2(oacc[ct][8 * u + 2], oacc[ct][8 * u + 3])),
                         san(pk2(oacc[ct][8 * u + 4], oacc[ct][8 * u + 5])), san(pk2(oacc[ct][8 * u + 6], oacc[ct][8 * u + 7]))};
          const unsigned chn = 4 * ct + 2 * hh + u;
          *(LAS v4u*)(L + L_KV + hd * 1024 + k * 256 + ((chn ^ (hd & 15)) * 16)) = v;
        }
      LGKM0();
      v4u ov[8];
#pragma unroll
      for (int t = 0; t < 8; ++t) {
        const unsigned hd2 = 4 * t + g4;
        ov[t] = lds128(L, L_KV + hd2 * 1024 + k * 256 + ((i16 ^ (hd2 & 15)) * 16));
      }
      cnt_add(L, C_PV, lane);
#pragma unroll
      for (int t = 0; t < 8; ++t) st_sc1(rs, WS_OUTC + cu * 32768 + k * 8192 + t * 1024 + lane * 16, ov[t]);
      if (k == 0 && lane < 32)
        st_sc1_64(rs, WS_ML + ((batt * 32 + lane) * 32 + satt) * 8, v2u{san(__float_as_uint(mcu)), san(__float_as_uint(lcu))});
    }
    STAMP(11);
    {
      const int sg = lane >> 3, o = lane & 7;
      v2u ml;
      for (unsigned spin = 0; spin < SPIN_CAP; ++spin) {
        ml = ld_sc1_64(rs, WS_ML + (((2 * k + (lane >> 5)) * 32 + h) * 32 + (lane & 31)) * 8);
        if (__all(max(ml[0], ml[1]) != POISON)) break;
        __builtin_amdgcn_s_sleep(2);
      }
      v4u dv2[2][4];
      for (unsigned spin = 0; spin < SPIN_CAP; ++spin) {
        unsigned mxv = 0;
#pragma unroll
        for (int bb = 0; bb < 2; ++bb)
#pragma unroll
          for (int t = 0; t < 4; ++t) dv2[bb][t] = ld_sc1(rs, WS_OUTC + (32 * (2 * k + bb) + 8 * t + sg) * 32768 + (j >> 1) * 8192 + h * 256 + (j & 1) * 128 + o * 16);
#pragma unroll
        for (int bb = 0; bb < 2; ++bb)
#pragma unroll
          for (int t = 0; t < 4; ++t) mxv = max(max(mxv, max(dv2[bb][t][0], dv2[bb][t][1])), max(dv2[bb][t][2], dv2[bb][t][3]));
        if (__all(mxv != POISON)) break;
        __builtin_amdgcn_s_sleep(2);
      }
      float wsv;
      {
        const float ms = __uint_as_float(ml[0]), lsv = __uint_as_float(ml[1]);
        float M = ms;
#pragma unroll
        for (int of = 16; of > 0; of >>= 1) M = fmaxf(M, shx(M, of));
        const float ee = __expf(ms - M);
        float Ls = ee * lsv;
#pragma unroll
        for (int of = 16; of > 0; of >>= 1) Ls += shx(Ls, of);
        wsv = ee / Ls;
      }
#pragma unroll
      for (int bb = 0; bb < 2; ++bb) {
        const int b = 2 * k + bb;
        const v4u (&dv)[4] = dv2[bb];
        float acc[8];
#pragma unroll
        for (int e = 0; e < 8; ++e) acc[e] = 0.f;
#pragma unroll
        for (int t = 0; t < 4; ++t) {
          const float wt = __shfl(wsv, 32 * bb + 8 * t + sg);
#pragma unroll
          for (int e = 0; e < 4; ++e) { acc[2 * e] += wt * bf2f(dv[t][e] & 0xFFFFu); acc[2 * e + 1] += wt * bfhi(dv[t][e]); }
        }
#pragma unroll
        for (int e = 0; e < 8; ++e) { acc[e] += shx(acc[e], 8); acc[e] += shx(acc[e], 16); acc[e] += shx(acc[e], 32); }
        if (sg < 2) {
          v4u v;
#pragma unroll
          for (int e = 0; e < 4; ++e) {
            const unsigned hi2 = pk2(acc[2 * e], acc[2 * e + 1]);
            const unsigned lo2 = pk2(acc[2 * e] - bf2f(hi2 & 0xFFFFu), acc[2 * e + 1] - bfhi(hi2));
            v[e] = sg ? lo2 : hi2;
          }
          *(LAS v4u*)(L + L_CL + (b + 8 * sg) * 144 + o * 16) = v;
        }
      }
    }
    STAMP(12);
    wg_barrier();
    {
      f32x4 acc = {0.f, 0.f, 0.f, 0.f};
#pragma unroll
      for (int ks = 0; ks < 2; ++ks) {
        const v4u a = lds128(L, L_CL + i16 * 144 + (32 * ks + 8 * g4) * 2);
        const unsigned r0 = 32 * ks + 8 * g4 + (i16 >> 2), r1 = r0 + 4, ch = 2 * w + ((lane & 3) >> 1);
        const v2u b0 = trread(L, L_WUV + r0 * 256 + ((ch ^ fswz(r0)) * 16) + 8 * (lane & 1));
        const v2u b1 = trread(L, L_WUV + r1 * 256 + ((ch ^ fswz(r1)) * 16) + 8 * (lane & 1));
        acc = mfma16(a, v4u{b0[0], b0[1], b1[0], b1[1]}, acc);
      }
#pragma unroll
      for (int e = 0; e < 4; ++e) {
        const float v = acc[e] + shx(acc[e], 32);
        const unsigned hi1 = pk2(v, 0.f) & 0xFFFFu;
        const unsigned lo1 = pk2(v - bf2f(hi1), 0.f) & 0xFFFFu;
        *(LAS u16*)(L + L_CTX + (4 * g4 + e) * 336 + (16 * w + i16) * 2) = (u16)(g4 < 2 ? hi1 : lo1);
      }
    }
    wg_barrier();
    STAMP(13);
    {
      const int pp = lane >> 1, hf = lane & 1;
      v4u ev[2];
      for (unsigned spin = 0; spin < SPIN_CAP; ++spin) {
        const unsigned hv = __builtin_amdgcn_raw_buffer_load_b32(rs, WS_HINTE + (((cu & 31) >> 3) * 256 + 64 * k + lane) * 4, 0, 16);
        if (__all(hv == (unsigned)gen1)) break;
        __builtin_amdgcn_s_sleep(2);
      }
      for (unsigned spin = 0; spin < SPIN_CAP; ++spin) {
#pragma unroll
        for (int t = 0; t < 2; ++t) ev[t] = ld_sc1(rs, WS_OUTE + (64 * k + 32 * t + pp) * 8192 + cu * 32 + hf * 16);
        const unsigned mxv = max(max(max(ev[0][0], ev[0][1]), max(ev[0][2], ev[0][3])), max(max(ev[1][0], ev[1][1]), max(ev[1][2], ev[1][3])));
        if (__all(mxv != POISON)) break;
        __builtin_amdgcn_s_sleep(1);
      }
      float acc[8];
#pragma unroll
      for (int e = 0; e < 4; ++e) {
        acc[2 * e] = bf2f(ev[0][e] & 0xFFFFu) + bf2f(ev[1][e] & 0xFFFFu);
        acc[2 * e + 1] = bfhi(ev[0][e]) + bfhi(ev[1][e]);
      }
#pragma unroll
      for (int e = 0; e < 8; ++e)
#pragma unroll
        for (int of = 2; of < 64; of <<= 1) acc[e] += shx(acc[e], of);
      if (lane < 2) {
        *(LAS f32x4*)(L + L_FIN + k * 64 + hf * 32) = f32x4{acc[0], acc[1], acc[2], acc[3]};
        *(LAS f32x4*)(L + L_FIN + k * 64 + hf * 32 + 16) = f32x4{acc[4], acc[5], acc[6], acc[7]};
      }
      cnt_add(L, C_FIN, lane);
      if (k == 0) {
        cnt_wait(L, C_FIN, 4);
        const LAS float* f = (const LAS float*)(L + L_FIN) + i16;
        const float s = (f[0] + f[16]) + (f[32] + f[48]);
        if (lane < 16) p.out[cu * 16 + lane] = (u16)(pk2(bf2f(xfin) + s, 0.f) & 0xFFFFu);
      }
    }
    STAMP(14);
  } else {
    const int hk = w - 4;
    {
      const char* src = (const char*)p.ckv + ((size_t)cu * 128 + 32 * hk) * 1024;
#pragma unroll
      for (int t = 0; t < 32; ++t) glds16<true>(src + t * 1024 + ((lane ^ fswz(t)) * 16), lbase + L_KV + (32 * hk + t) * 1024);
    }
    wg_barrier();
    v4u xnf[4];
#pragma unroll
    for (int t = 0; t < 4; ++t) xnf[t] = lds128(L, L_XN + (lane & 7) * 1040 + (32 * (4 * hk + t) + 8 * g4) * 2);
    wg_barrier();
    VMWAIT(8);
    STAMP(1);
    v4u wor[4][8], wdr[8];
    {
      const char* src = (const char*)p.wo + (size_t)(h * 128 + 8 * g4) * 1024 + hk * 256 + i16 * 16;
#pragma unroll
      for (int u = 0; u < 4; ++u)
#pragma unroll
        for (int e = 0; e < 8; ++e) wor[u][e] = *(const GAS v4u*)(src + (32 * u + e) * 1024);
      const char* sd = (const char*)p.wd + (size_t)(8 * oct) * 1024 + hk * 256 + i16 * 16;
#pragma unroll
      for (int e = 0; e < 8; ++e) wdr[e] = __builtin_nontemporal_load((const GAS v4u*)(sd + e * 1024));
    }
    SB0();
    VMWAIT(40);
    cnt_add(L, C_KV0 + hk, lane);
    STAMP(2);
    SB0();
    v4u wof[4][8], wdf[8];
#pragma unroll
    for (int u = 0; u < 4; ++u) transpose8(wor[u], wof[u]);
    transpose8(wdr, wdf);
#pragma unroll
    for (int u = 0; u < 4; ++u)
#pragma unroll
      for (int jj = 0; jj < 8; ++jj) asm volatile("" : "+v"(wof[u][jj]));
#pragma unroll
    for (int jj = 0; jj < 8; ++jj) asm volatile("" : "+v"(wdf[jj]));
    STAMP(3);
    cnt_wait(L, C_PV, 4);
    STAMP(4);
    {
#pragma unroll
      for (int t = 0; t < 4; ++t) {
        const unsigned row = 16 * hk + 4 * t + g4;
        glds16<true>((const char*)p.wuv + (size_t)(64 * j + row) * 8192 + h * 256 + ((i16 ^ fswz(row)) * 16), lbase + L_WUV + (16 * hk + 4 * t) * 256);
      }
      if (has_ffn) {
        const char* sg_ = (const char*)((lane & 1) ? p.wu : p.wg) + (size_t)(128 * hk + (lane >> 1)) * (FF * 2) + oct * 16;
#pragma unroll
        for (int t = 0; t < 4; ++t) glds16<false>(sg_ + (size_t)t * 32 * (FF * 2), lbase + L_GU + (128 * hk + 32 * t) * 32);
      }
    }
    VMWAIT(0);
    STAMP(5);
    if (has_ffn) {
      f32x4 acc = {0.f, 0.f, 0.f, 0.f};
#pragma unroll
      for (int t = 0; t < 4; ++t) {
        const int ks = 4 * hk + t;
        const unsigned ro = L_GU + (32 * ks + 8 * g4 + (i16 >> 2)) * 32 + (lane & 3) * 8;
        const v2u b0 = trread(L, ro), b1 = trread(L, ro + 4 * 32);
        acc = mfma16(xnf[t], v4u{b0[0], b0[1], b1[0], b1[1]}, acc);
      }
      if (lane < 32) *(LAS f32x4*)(L + L_FP + hk * 512 + lane * 16) = acc;
      cnt_add(L, C_HB, lane);
      if (hk == 0) {
        cnt_wait(L, C_HB, 4);
        f32x4 s4 = *(LAS f32x4*)(L + L_FP + (lane & 31) * 16);
#pragma unroll
        for (int kk = 1; kk < 4; ++kk) s4 += *(LAS f32x4*)(L + L_FP + kk * 512 + (lane & 31) * 16);
#pragma unroll
        for (int e = 0; e < 4; ++e) {
          const float up = shx(s4[e], 8);
          const float gg = rbf(s4[e]), uu = rbf(up);
          const float inner = 0.7978845608028654f * (gg + 0.044715f * gg * gg * gg);
          const float th = 1.f - 2.f / (__expf(2.f * inner) + 1.f);
          const float ge = rbf(0.5f * gg * (1.f + th));
          const unsigned hv = pk2(ge * uu, 0.f) & 0xFFFFu;
          if (lane < 32 && i16 < 8) *(LAS u16*)(L + L_CTX + (4 * g4 + e) * 336 + (128 + i16) * 2) = (u16)hv;
        }
        {
          const int row = lane >> 2, chn = lane & 3;
          if (row >= 8 || chn > 0) *(LAS v4u*)(L + L_CTX + row * 336 + 256 + chn * 16) = v4u{0u, 0u, 0u, 0u};
        }
      }
    }
    STAMP(6);
    wg_barrier();
    {
      f32x4 acc = {0.f, 0.f, 0.f, 0.f};
#pragma unroll
      for (int ks = 0; ks < 2; ++ks) {
        const v4u a = lds128(L, L_CL + i16 * 144 + (32 * ks + 8 * g4) * 2);
        const unsigned r0 = 32 * ks + 8 * g4 + (i16 >> 2), r1 = r0 + 4, ch = 2 * w + ((lane & 3) >> 1);
        const v2u b0 = trread(L, L_WUV + r0 * 256 + ((ch ^ fswz(r0)) * 16) + 8 * (lane & 1));
        const v2u b1 = trread(L, L_WUV + r1 * 256 + ((ch ^ fswz(r1)) * 16) + 8 * (lane & 1));
        acc = mfma16(a, v4u{b0[0], b0[1], b1[0], b1[1]}, acc);
      }
#pragma unroll
      for (int e = 0; e < 4; ++e) {
        const float v = acc[e] + shx(acc[e], 32);
        const unsigned hi1 = pk2(v, 0.f) & 0xFFFFu;
        const unsigned lo1 = pk2(v - bf2f(hi1), 0.f) & 0xFFFFu;
        *(LAS u16*)(L + L_CTX + (4 * g4 + e) * 336 + (16 * w + i16) * 2) = (u16)(g4 < 2 ? hi1 : lo1);
      }
    }
    wg_barrier();
    STAMP(7);
    f32x4 oc[8];
#pragma unroll
    for (int jj = 0; jj < 8; ++jj) oc[jj] = f32x4{0.f, 0.f, 0.f, 0.f};
#pragma unroll
    for (int u = 0; u < 4; ++u) {
      const v4u a = lds128(L, L_CTX + i16 * 336 + (32 * u + 8 * g4) * 2);
#pragma unroll
      for (int jj = 0; jj < 8; ++jj) oc[jj] = mfma16(a, wof[u][jj], oc[jj]);
    }
    if (has_ffn) {
      const v4u a = lds128(L, L_CTX + i16 * 336 + (128 + 8 * g4) * 2);
#pragma unroll
      for (int jj = 0; jj < 8; ++jj) oc[jj] = mfma16(a, wdf[jj], oc[jj]);
    }
#pragma unroll
    for (int jj = 0; jj < 8; ++jj)
#pragma unroll
      for (int e = 0; e < 4; ++e) oc[jj][e] += shx(oc[jj][e], 32);
    if (lane < 32) {
#pragma unroll
      for (int e = 0; e < 4; ++e) {
        const v4u v = {san(pk2(oc[0][e], oc[1][e])), san(pk2(oc[2][e], oc[3][e])), san(pk2(oc[4][e], oc[5][e])), san(pk2(oc[6][e], oc[7][e]))};
        st_sc1(rs, WS_OUTE + cu * 8192 + (4 * g4 + e) * 1024 + hk * 256 + i16 * 16, v);
      }
    }
    if (lane == 0) __builtin_amdgcn_raw_buffer_store_b32(((LAS unsigned*)(L + L_MISC))[15], rs, WS_HINTE + (hk * 256 + cu) * 4, 0, 16);
    STAMP(8);
  }
}

extern "C" void kernel_launch(void* const* d_in, const int* in_sizes, int n_in, void* d_out, int out_size, void* d_ws,
                              size_t ws_size, hipStream_t stream) {
  Params p;
  p.x = (const u16*)d_in[0];
  p.ckv = (const u16*)d_in[1];
  p.wq = (const u16*)d_in[2];
  p.wuk = (const u16*)d_in[3];
  p.wuv = (const u16*)d_in[4];
  p.wo = (const u16*)d_in[5];
  p.wg = (const u16*)d_in[7];
  p.wu = (const u16*)d_in[8];
  p.wd = (const u16*)d_in[9];
  p.out = (u16*)d_out;
  p.ws = (unsigned char*)d_ws;
  (void)hipFuncSetAttribute((const void*)fused_kernel, hipFuncAttributeMaxDynamicSharedMemorySize, LDS_BYTES);
  fused_kernel<<<256, 512, LDS_BYTES, stream>>>(p);
}
```

```cpp
#include <hip/hip_runtime.h>
#include <stdint.h>

typedef unsigned short u16;
typedef unsigned long long u64;
typedef unsigned v4u __attribute__((ext_vector_type(4)));
typedef unsigned v2u __attribute__((ext_vector_type(2)));
typedef float f32x4 __attribute__((ext_vector_type(4)));
typedef float f32x16 __attribute__((ext_vector_type(16)));
typedef float v2f __attribute__((ext_vector_type(2)));
typedef short bf16x8 __attribute__((ext_vector_type(8)));
typedef short v4s __attribute__((ext_vector_type(4)));
typedef __bf16 v2bf __attribute__((ext_vector_type(2)));
#define GAS __attribute__((address_space(1)))
#define LAS __attribute__((address_space(3)))
#define DI __device__ __forceinline__

#define NFA 4
#define NFB 10
#define NFC 16
#define T_AB 220
#define T_BC 700
#define POLL_START 300
#define POLL_GAP 6
#define F_DLY 80
#define W_DLY 150
#define NFW 16

constexpr int DM = 512, FF = 1376;
constexpr unsigned WS_GEN = 0;
constexpr unsigned WS_FLAG = 4096;
constexpr unsigned WS_ABUF = WS_FLAG + 131072;
constexpr unsigned WS_FBUF = WS_ABUF + 131072;
constexpr unsigned WS_QABS = WS_FBUF + 1048576;
constexpr unsigned WS_ML = WS_QABS + 262144;
constexpr unsigned WS_EIN = WS_ML + 262144;
constexpr unsigned WS_CIN = WS_EIN + 1048576;
constexpr unsigned CIN_ROW = 352, CIN_WI = 128 * CIN_ROW;
constexpr unsigned WS_END = WS_CIN + 128 * CIN_WI;
constexpr unsigned WS_STAMP = WS_END;
constexpr unsigned L_Q = 0;
constexpr unsigned L_P = L_Q;
constexpr unsigned L_MX = L_Q + 4096;
constexpr unsigned L_LS = L_Q + 4608;
constexpr unsigned L_SLOT = 32768;
constexpr unsigned L_XN = 0;
constexpr unsigned L_QP = 8448;
constexpr unsigned L_CL = L_QP + 8192;
constexpr unsigned L_CTX = L_CL + 4352;
constexpr unsigned L_FIN = L_CTX + 5376;
constexpr unsigned L_QH = L_FIN + 1024;
constexpr unsigned L_WQ = 32768;
constexpr unsigned L_GU = 65536;
constexpr unsigned L_WUV = 98304;
constexpr unsigned L_CNT = 31744;
constexpr unsigned LDS_BYTES = 163840;
enum { C_P = 0, C_OVF = 1, C_BOK, C_QA, C_QFREE, C_MX, C_PVD, C_QST, C_GU, C_FIN, C_QH, C_GO, C_GOT, C_MODE, C_RDY = 16, C_N = 32 };

constexpr unsigned POISON = 0xFFFFFFFFu;
constexpr unsigned SPIN_CAP = 1u << 20;

struct Params {
  const u16 *x, *ckv, *wq, *wuk, *wuv, *wo, *wg, *wu, *wd;
  u16* out;
  unsigned char* ws;
};

DI float bf2f(unsigned v) { return __uint_as_float(v << 16); }
DI float bfhi(unsigned v) { return __uint_as_float(v & 0xFFFF0000u); }
DI unsigned pk2(float a, float b) {
  v2f t = {a, b};
  return __builtin_bit_cast(unsigned, __builtin_convertvector(t, v2bf));
}
DI float rbf(float a) { return bf2f(pk2(a, 0.f) & 0xFFFFu); }
DI unsigned san(unsigned v) { return min(v, 0xFFFFFFFEu); }
DI f32x4 mfma16(v4u a, v4u b, f32x4 c) {
  return __builtin_amdgcn_mfma_f32_16x16x32_bf16(__builtin_bit_cast(bf16x8, a), __builtin_bit_cast(bf16x8, b), c, 0, 0, 0);
}
DI f32x16 mfma32(v4u a, v4u b, f32x16 c) {
  return __builtin_amdgcn_mfma_f32_32x32x16_bf16(__builtin_bit_cast(bf16x8, a), __builtin_bit_cast(bf16x8, b), c, 0, 0, 0);
}
DI v4u lds128(unsigned a) { return *(LAS v4u*)(size_t)a; }
DI v2u trread(unsigned a) { return __builtin_bit_cast(v2u, __builtin_amdgcn_ds_read_tr16_b64_v4i16((LAS v4s*)(size_t)a)); }
template <class T> DI LAS T& lds(unsigned a) { return *(LAS T*)(size_t)a; }
template <int POL> DI void glds16(const void* gsrc, unsigned lds_dst) {
  unsigned keep;
  if (POL == 1) asm volatile("s_mov_b32 %0, m0\n\ts_mov_b32 m0, %2\n\ts_nop 0\n\tglobal_load_lds_dwordx4 %1, off nt\n\ts_mov_b32 m0, %0" : "=&s"(keep) : "v"(gsrc), "s"(lds_dst) : "memory");
  else if (POL == 2) asm volatile("s_mov_b32 %0, m0\n\ts_mov_b32 m0, %2\n\ts_nop 0\n\tglobal_load_lds_dwordx4 %1, off sc1\n\ts_mov_b32 m0, %0" : "=&s"(keep) : "v"(gsrc), "s"(lds_dst) : "memory");
  else asm volatile("s_mov_b32 %0, m0\n\ts_mov_b32 m0, %2\n\ts_nop 0\n\tglobal_load_lds_dwordx4 %1, off\n\ts_mov_b32 m0, %0" : "=&s"(keep) : "v"(gsrc), "s"(lds_dst) : "memory");
}
DI void glds16s(unsigned voff, const void* sbase, unsigned lds_dst) {
  unsigned keep;
  asm volatile("s_mov_b32 %0, m0\n\ts_mov_b32 m0, %3\n\ts_nop 0\n\tglobal_load_lds_dwordx4 %1, %2 nt\n\ts_mov_b32 m0, %0" : "=&s"(keep) : "v"(voff), "s"(sbase), "s"(lds_dst) : "memory");
}
DI void gld16s(v4u& d, unsigned voff, const void* sbase) {
  asm volatile("global_load_dwordx4 %0, %1, %2 nt" : "=v"(d) : "v"(voff), "s"(sbase) : "memory");
}
#define VMWAIT(n) asm volatile("s_waitcnt vmcnt(%0)" ::"i"(n) : "memory")
#define LGKM0() asm volatile("s_waitcnt lgkmcnt(0)" ::: "memory")
#define CFENCE() asm volatile("" ::: "memory")
#define SB0() __builtin_amdgcn_sched_barrier(0)
DI void wg_barrier() {
  LGKM0();
  __builtin_amdgcn_s_barrier();
  CFENCE();
}
DI void cnt_add(unsigned c) {
  u64 keep;
  asm volatile("s_mov_b64 %0, exec\n\ts_mov_b64 exec, 1\n\tds_add_u32 %1, %2 offset:%3\n\ts_mov_b64 exec, %0" : "=&s"(keep) : "v"(c * 4u), "v"(1u), "i"(L_CNT) : "memory");
}
DI unsigned cnt_add_rtn(unsigned c) {
  u64 keep;
  unsigned v;
  asm volatile("s_mov_b64 %0, exec\n\ts_mov_b64 exec, 1\n\tds_add_rtn_u32 %1, %2, %3 offset:%4\n\ts_waitcnt lgkmcnt(0)\n\ts_mov_b64 exec, %0"
               : "=&s"(keep), "=&v"(v) : "v"(c * 4u), "v"(1u), "i"(L_CNT) : "memory");
  return __builtin_amdgcn_readfirstlane(v);
}
DI unsigned cnt_get(unsigned c) {
  unsigned v;
  asm volatile("ds_read_b32 %0, %1 offset:%2\n\ts_waitcnt lgkmcnt(0)" : "=v"(v) : "v"(c * 4u), "i"(L_CNT) : "memory");
  return __builtin_amdgcn_readfirstlane(v);
}
template <int SLP = 0> DI void cnt_wait(unsigned c, unsigned target) {
#pragma unroll 1
  for (unsigned s = 0; s < SPIN_CAP * 16u; ++s) {
    if (cnt_get(c) >= target) break;
    if (SLP) __builtin_amdgcn_s_sleep(SLP);
  }
}
DI unsigned now32() { return (unsigned)__builtin_amdgcn_s_memrealtime(); }
DI void wait_until(unsigned t0, unsigned ticks) {
  if (ticks == 0) return;
#pragma unroll 1
  while (now32() - t0 < ticks) __builtin_amdgcn_s_sleep(1);
}
DI v4u ld_sc1(__amdgpu_buffer_rsrc_t rs, unsigned off) { return __builtin_amdgcn_raw_buffer_load_b128(rs, off, 0, 16); }
DI v2u ld_sc1_64(__amdgpu_buffer_rsrc_t rs, unsigned off) { return __builtin_amdgcn_raw_buffer_load_b64(rs, off, 0, 16); }
DI void st_sc1(__amdgpu_buffer_rsrc_t rs, unsigned off, v4u v) { __builtin_amdgcn_raw_buffer_store_b128(v, rs, off, 0, 16); }
DI void st_sc1_64(__amdgpu_buffer_rsrc_t rs, unsigned off, v2u v) { __builtin_amdgcn_raw_buffer_store_b64(v, rs, off, 0, 16); }
DI void st_sc1_32(__amdgpu_buffer_rsrc_t rs, unsigned off, unsigned v) { __builtin_amdgcn_raw_buffer_store_b32(v, rs, off, 0, 16); }
DI unsigned fswz(unsigned row) { return ((row & 3u) << 2) | ((0x78u >> (2u * ((row >> 2) & 3u))) & 3u); }
template <int N> DI float ror16(float v) {
  return __int_as_float(__builtin_amdgcn_update_dpp(0, __float_as_int(v), 0x120 + N, 0xF, 0xF, false));
}
DI float xor1(float v) { return __int_as_float(__builtin_amdgcn_update_dpp(0, __float_as_int(v), 0xB1, 0xF, 0xF, false)); }
DI float add16(float v) { auto r = __builtin_amdgcn_permlane16_swap(__float_as_uint(v), __float_as_uint(v), false, false); return __uint_as_float(r[0]) + __uint_as_float(r[1]); }
DI float add32(float v) { auto r = __builtin_amdgcn_permlane32_swap(__float_as_uint(v), __float_as_uint(v), false, false); return __uint_as_float(r[0]) + __uint_as_float(r[1]); }
DI float rs_add16(float a, float b) { auto r = __builtin_amdgcn_permlane16_swap(__float_as_uint(a), __float_as_uint(b), false, false); return __uint_as_float(r[0]) + __uint_as_float(r[1]); }
DI float rs_add32(float a, float b) { auto r = __builtin_amdgcn_permlane32_swap(__float_as_uint(a), __float_as_uint(b), false, false); return __uint_as_float(r[0]) + __uint_as_float(r[1]); }
DI float rs_max16(float a, float b) { auto r = __builtin_amdgcn_permlane16_swap(__float_as_uint(a), __float_as_uint(b), false, false); return fmaxf(__uint_as_float(r[0]), __uint_as_float(r[1])); }
DI float max32(float v) { auto r = __builtin_amdgcn_permlane32_swap(__float_as_uint(v), __float_as_uint(v), false, false); return fmaxf(__uint_as_float(r[0]), __uint_as_float(r[1])); }
DI float rowsum(float v) { v += ror16<8>(v); v += ror16<4>(v); v += ror16<2>(v); v += ror16<1>(v); return v; }
DI float rowmax(float v) { v = fmaxf(v, ror16<8>(v)); v = fmaxf(v, ror16<4>(v)); v = fmaxf(v, ror16<2>(v)); v = fmaxf(v, ror16<1>(v)); return v; }
DI float ex2(float v) { return __builtin_amdgcn_exp2f(v); }
constexpr float LOG2E = 1.4426950408889634f;

DI void transpose8(const v4u (&r)[8], v4u (&c)[8]) {
#pragma unroll
  for (int q = 0; q < 4; ++q) {
#pragma unroll
    for (int e2 = 0; e2 < 4; ++e2) {
      unsigned a = r[2 * e2][q], b = r[2 * e2 + 1][q];
      c[2 * q][e2] = __builtin_amdgcn_perm(b, a, 0x05040100u);
      c[2 * q + 1][e2] = __builtin_amdgcn_perm(b, a, 0x07060302u);
    }
  }
}
#define PLD(f) asm volatile("global_load_dwordx2 %0, %1, %2 sc1" : "=&v"(f) : "v"(fo), "s"(wsp) : "memory")
#define PCHK(f) (__extension__({ asm volatile("s_waitcnt vmcnt(2)" : "+v"(f)::"memory"); __all(((f[0] ^ tag) | (f[1] ^ tag)) == 0); }))
DI void poll_flags(const unsigned char* wsp, unsigned fo, unsigned tag, v2u& f0, v2u& f1, v2u& f2) {
  PLD(f0); __builtin_amdgcn_s_sleep(POLL_GAP);
  PLD(f1); __builtin_amdgcn_s_sleep(POLL_GAP);
  PLD(f2);
#pragma unroll 1
  for (unsigned spin = 0; spin < SPIN_CAP; ++spin) {
    if (PCHK(f0)) break;
    PLD(f0);
    if (PCHK(f1)) break;
    PLD(f1);
    if (PCHK(f2)) break;
    PLD(f2);
  }
}

DI void send_flags(__amdgpu_buffer_rsrc_t rs, int wi, int lane, unsigned tag) {
#pragma unroll
  for (int t = 0; t < 4; ++t) st_sc1_32(rs, WS_FLAG + (64 * t + lane) * 512 + wi * 4, tag);
}

__global__ void __launch_bounds__(512, 2) fused_kernel(Params p) {
  extern __shared__ __attribute__((aligned(16))) unsigned char smem[];
  const u64 t0l = __builtin_amdgcn_s_memrealtime();
  const unsigned t0 = (unsigned)t0l;
  const int tid = threadIdx.x, lane = tid & 63;
  const int w = __builtin_amdgcn_readfirstlane(tid >> 6);
  const int cu = blockIdx.x;
  const int xcd = cu & 7, r8 = cu >> 3, idx = r8 >> 1;
  const __amdgpu_buffer_rsrc_t rs = __builtin_amdgcn_make_buffer_rsrc((void*)p.ws, 0, (int)WS_END, 0x00020000);
  const int g4 = lane >> 4, i16 = lane & 15;
  unsigned gen;
  asm volatile("s_load_dword %0, %1, 0x0" : "=s"(gen) : "s"(p.ws), "s"((unsigned)(size_t)(LAS unsigned char*)smem) : "memory");
#define GET_TAG() (__extension__({ asm volatile("s_waitcnt lgkmcnt(0)" : "+s"(gen)::"memory"); gen + 1u; }))

  if ((r8 & 1) == 0) {
    const int b = xcd, s = idx;
    if (w >= 4) {
      const int sw = w - 4;
      const char* base = (const char*)p.ckv + ((size_t)(b * 16 + s) * 256 + 16 * sw) * 1024;
      unsigned vo[16];
#pragma unroll
      for (int i = 0; i < 16; ++i) vo[i] = i * 1024 + ((lane ^ fswz(i)) * 16);
      v4u st[32];
      unsigned mode = 0, vz = 0;
      asm volatile("" : "+v"(vz));
#pragma unroll
      for (int i = 0; i < 64; ++i) {
        const int r = i >> 4;
        if (r < 2) glds16s(vo[i & 15], base + r * 65536, L_SLOT + r * 65536 + (16 * sw + (i & 15)) * 1024);
        else gld16s(st[i - 32], vo[i & 15], base + r * 65536);
        if (i == NFA - 1) wg_barrier();
        if (i >= NFA - 1) {
          unsigned tmp;
          asm volatile("s_waitcnt vmcnt(%3)\n\ts_cmp_gt_u32 %0, 1\n\ts_cbranch_scc1 1f\n\tds_read_b32 %1, %2 offset:%6\n\ts_waitcnt lgkmcnt(0)\n\tv_readfirstlane_b32 %0, %1\n\t"
                       "s_cmp_gt_u32 %0, 1\n\ts_cbranch_scc1 1f\n\ts_waitcnt vmcnt(%4)\n\ts_cmp_gt_u32 %0, 0\n\ts_cbranch_scc1 1f\n\ts_waitcnt vmcnt(%5)\n1:"
                       : "+s"(mode), "=&v"(tmp) : "v"(vz), "i"(NFC - 1), "i"(NFB - 1), "i"(NFA - 1), "i"(L_CNT + 4 * C_MODE) : "memory", "scc");
        }
        if (i - NFC + 1 == 15) cnt_add(C_RDY + 0);
        if (i - NFC + 1 == 31) cnt_add(C_RDY + 1);
      }
      asm volatile("s_waitcnt vmcnt(16)" : "+v"(st[0]), "+v"(st[1]), "+v"(st[2]), "+v"(st[3]), "+v"(st[4]), "+v"(st[5]), "+v"(st[6]), "+v"(st[7]), "+v"(st[8]), "+v"(st[9]),
                   "+v"(st[10]), "+v"(st[11]), "+v"(st[12]), "+v"(st[13]), "+v"(st[14]), "+v"(st[15])::"memory");
      const unsigned wa = L_SLOT + sw * 16384 + lane * 16;
      cnt_wait<1>(C_PVD, 4);
#pragma unroll
      for (int i = 0; i < 16; ++i) lds<v4u>(wa + i * 1024) = st[i];
      cnt_add(C_RDY + 2);
      asm volatile("s_waitcnt vmcnt(0)" : "+v"(st[16]), "+v"(st[17]), "+v"(st[18]), "+v"(st[19]), "+v"(st[20]), "+v"(st[21]), "+v"(st[22]), "+v"(st[23]), "+v"(st[24]),
                   "+v"(st[25]), "+v"(st[26]), "+v"(st[27]), "+v"(st[28]), "+v"(st[29]), "+v"(st[30]), "+v"(st[31])::"memory");
      cnt_wait<1>(C_PVD, 8);
#pragma unroll
      for (int i = 0; i < 16; ++i) lds<v4u>(wa + 65536 + i * 1024) = st[16 + i];
      cnt_add(C_RDY + 3);
    } else {
      const int k = w;
      const int kvi = b * 16 + s;
      v2u f0 = {0u, 0u}, f1 = f0, f2 = f0;
      if (k == 0 && lane < C_N) lds<unsigned>(L_CNT + lane * 4) = 0u;
      wg_barrier();
      const unsigned tag = GET_TAG();
      if (k == 0) {
        wait_until(t0, POLL_START);
        poll_flags(p.ws, WS_FLAG + kvi * 512 + lane * 8, tag, f0, f1, f2);
        cnt_add(C_BOK);
      } else if (k == 1) {
        wait_until(t0, T_AB);
        cnt_add(C_MODE);
        bool fired = false;
#pragma unroll 1
        for (unsigned sp = 0; sp < SPIN_CAP * 16u; ++sp) {
          if (cnt_get(C_BOK) >= 1) break;
          if (!fired && now32() - t0 >= T_BC) { cnt_add(C_MODE); fired = true; }
          __builtin_amdgcn_s_sleep(1);
        }
      } else {
        cnt_wait<1>(C_BOK, 1);
      }
      v4u qf[32];
      {
        const char* qsrc = (const char*)p.ws + WS_QABS + b * 32768 + i16 * 1024 + g4 * 16;
        const char* qk = qsrc + (k >> 1) * 16384 + (k & 1) * 512;
#pragma unroll
        for (int t = 0; t < 7; ++t) glds16<2>(qk + t * 64, L_Q + (8 * k + t) * 1024);
        if (k < 3) glds16<2>(qk + 7 * 64, L_Q + (8 * k + 7) * 1024);
        asm volatile("global_load_dwordx4 %0, %1, off sc1" : "=&v"(qf[31]) : "v"(qsrc + 16384 + 15 * 64) : "memory");
      }
      asm volatile("s_waitcnt vmcnt(0)" : "+v"(f0), "+v"(f1), "+v"(f2), "+v"(qf[31])::"memory");
      cnt_add(C_QA);
      cnt_wait(C_QA, 4);
      if (k == 1) cnt_add(C_MODE);
#pragma unroll
      for (int f = 0; f < 31; ++f) qf[f] = lds128(L_Q + f * 1024 + lane * 16);
      cnt_add(C_QFREE);
      f32x16 oacc[4];
#pragma unroll
      for (int ct = 0; ct < 4; ++ct)
#pragma unroll
        for (int e = 0; e < 16; ++e) oacc[ct][e] = 0.f;
      float mr0 = -3.0e38f, mr1 = -3.0e38f, l0 = 0.f, l1 = 0.f;
      const float scale_b = 0.08837890625f;
      const float OVT = 40.f;
      unsigned np = 0, nx = 0;
      unsigned aq0, ap0;
      const unsigned hh = lane >> 5;
      {
        aq0 = L_SLOT + (16 * k + i16) * 1024 + ((fswz(i16) ^ g4) * 16);
        const unsigned blk = (lane >> 4) & 1, q4 = i16 >> 2, pp = lane & 3;
        const unsigned r0 = 8 * hh + q4;
        const unsigned ch = 16 * k + 2 * (pp & 1) + blk;
        ap0 = L_SLOT + r0 * 1024 + ((ch ^ fswz(r0)) * 16) + 8 * (pp >> 1);
      }
      const unsigned po = L_P + k * 1024 + (i16 + 32 * (g4 >> 1)) * 16 + (g4 & 1) * 8;
      v4u ob[4], pf[4];
      auto ldo = [&](int n) {
        const v2u t0_ = trread((ap0 ^ ((n >> 2) * 64)) + (n & 3) * 16384), t1_ = trread((ap0 ^ ((n >> 2) * 64 + 32)) + (n & 3) * 16384 + 4096);
        ob[n & 3] = v4u{t0_[0], t0_[1], t1_[0], t1_[1]};
      };
      const unsigned hd = lane & 31;
      const unsigned sb = L_SLOT + k * 16384;
      const unsigned sa = sb + hd * 368 + hh * 176;
      auto dwd = [&](int d) { return d < 32 ? pk2(oacc[d >> 3][2 * (d & 7)], oacc[d >> 3][2 * (d & 7) + 1]) : 0u; };
      auto cvt = [&](int g0, int g1) {
#pragma unroll
        for (int g = g0; g < g1; ++g) lds<v4u>(sa + g * 16) = v4u{dwd(3 * g), dwd(3 * g + 1), dwd(3 * g + 2), tag};
      };
#pragma unroll 1
      for (int r = 0; r < 4; ++r) {
        cnt_wait(C_RDY + r, 4);
        f32x4 s0 = {0.f, 0.f, 0.f, 0.f}, s1 = {0.f, 0.f, 0.f, 0.f};
        {
          v4u ab[2][4];
          unsigned aq[4];
#pragma unroll
          for (int u = 0; u < 4; ++u) aq[u] = aq0 ^ (u * 64);
#pragma unroll
          for (int u = 0; u < 4; ++u) ab[0][u] = lds128(aq[u]);
#pragma unroll
          for (int bl = 0; bl < 4; ++bl) {
            if (bl < 3) {
#pragma unroll
              for (int u = 0; u < 4; ++u) ab[(bl + 1) & 1][u] = lds128(aq[u] + (bl + 1) * 256);
            }
            SB0();
#pragma unroll
            for (int u = 0; u < 4; ++u) {
              s0 = mfma16(ab[bl & 1][u], qf[4 * bl + u], s0);
              s1 = mfma16(ab[bl & 1][u], qf[16 + 4 * bl + u], s1);
            }
            SB0();
          }
        }
        ldo(0); ldo(1); ldo(2); ldo(3);
        float sv0[4], sv1[4];
#pragma unroll
        for (int e = 0; e < 4; ++e) {
          const unsigned u = pk2(s0[e], s1[e]);
          const unsigned v = pk2(bf2f(u) * scale_b, bfhi(u) * scale_b);
          sv0[e] = bf2f(v); sv1[e] = bfhi(v);
        }
        const float mw0 = fmaxf(fmaxf(sv0[0], sv0[1]), fmaxf(sv0[2], sv0[3])), mw1 = fmaxf(fmaxf(sv1[0], sv1[1]), fmaxf(sv1[2], sv1[3]));
        bool xch = r == 0;
        if (xch) cnt_wait(C_QFREE, 4);
        else if (__any(mw0 > mr0 + OVT || mw1 > mr1 + OVT)) lds<unsigned>(L_CNT + C_OVF * 4) = r;
        float ln0, ln1;
#pragma unroll 1
        for (;;) {
          if (xch) {
            const float mw = max32(rs_max16(mw0, mw1));
            if (lane < 32) lds<float>(L_MX + k * 128 + lane * 4) = mw;
            cnt_add(C_MX);
            nx += 4;
            cnt_wait(C_MX, nx);
            const LAS float* mx = (const LAS float*)(size_t)(L_MX + i16 * 4);
            const float mn0 = fmaxf(fmaxf(fmaxf(mx[0], mx[32]), fmaxf(mx[64], mx[96])), mr0);
            const float mn1 = fmaxf(fmaxf(fmaxf(mx[16], mx[48]), fmaxf(mx[80], mx[112])), mr1);
            if (r > 0) {
              const float al0 = ex2((mr0 - mn0) * LOG2E), al1 = ex2((mr1 - mn1) * LOG2E);
              l0 *= al0; l1 *= al1;
              const float alpha = (lane & 16) ? al1 : al0;
#pragma unroll
              for (int ct = 0; ct < 4; ++ct)
#pragma unroll
                for (int e = 0; e < 16; ++e) oacc[ct][e] *= alpha;
            }
            mr0 = mn0; mr1 = mn1;
          }
          float e0[4], e1[4];
          const float c0 = mr0 * LOG2E, c1 = mr1 * LOG2E;
#pragma unroll
          for (int e = 0; e < 4; ++e) { e0[e] = ex2(fmaf(sv0[e], LOG2E, -c0)); e1[e] = ex2(fmaf(sv1[e], LOG2E, -c1)); }
          lds<v2u>(po) = v2u{pk2(e0[0], e0[1]), pk2(e0[2], e0[3])};
          lds<v2u>(po + 256) = v2u{pk2(e1[0], e1[1]), pk2(e1[2], e1[3])};
          ln0 = l0 + ((e0[0] + e0[1]) + (e0[2] + e0[3]));
          ln1 = l1 + ((e1[0] + e1[1]) + (e1[2] + e1[3]));
          if (r == 3) {
            const float lt = add32(rs_add16(ln0, ln1));
            if (lane < 32) lds<float>(L_LS + k * 128 + lane * 4) = lt;
          }
          cnt_add(C_P);
          np += 4;
          v2u cv;
#pragma unroll 1
          for (unsigned sp = 0; sp < SPIN_CAP * 16u; ++sp) {
            asm volatile("ds_read_b64 %0, %1 offset:%2\n\ts_waitcnt lgkmcnt(0)" : "=v"(cv) : "v"(0u), "i"(L_CNT) : "memory");
            if (__builtin_amdgcn_readfirstlane(cv[0]) >= np) break;
          }
          if (xch || __builtin_amdgcn_readfirstlane(cv[1]) != (unsigned)r) break;
          xch = true;
        }
        l0 = ln0; l1 = ln1;
#pragma unroll
        for (int n = 0; n < 4; ++n) pf[n] = lds128(L_P + n * 1024 + lane * 16);
        if (r == 3) {
          const LAS float* ls = (const LAS float*)(size_t)(L_LS + hd * 4);
          const float lt = (ls[0] + ls[32]) + (ls[64] + ls[96]);
          const float mp = (lane & 16) ? mr1 : mr0;
          if (lane < 32) st_sc1(rs, WS_ML + ((hd * 4 + k) * 128 + kvi) * 16, v4u{__float_as_uint(mp), __float_as_uint(lt), tag, tag});
        }
#pragma unroll
        for (int n = 0; n < 16; ++n) {
          SB0();
          oacc[n >> 2] = mfma32(ob[n & 3], pf[n & 3], oacc[n >> 2]);
          SB0();
          if (n + 4 < 16) ldo(n + 4);
          if (r == 3) {
            if (n == 5) cvt(0, 2);
            if (n == 9) cvt(2, 5);
            if (n == 13) cvt(5, 8);
          }
        }
        cnt_add(C_PVD);
        {
          const unsigned dl = (r & 1) ? 0u - 65536u : 65536u;
          aq0 += dl; ap0 += dl;
          asm volatile("" : "+v"(aq0), "+v"(ap0));
        }
      }
      {
        unsigned hx[11];
#pragma unroll
        for (int t = 0; t < 11; ++t) hx[t] = ((64 * t + lane) * 2979u) >> 16;
        cvt(8, 11);
        v4u ov[11];
#pragma unroll
        for (int t = 0; t < 11; ++t) ov[t] = lds128(sb + t * 1024 + lane * 16 + hx[t] * 16);
        const unsigned gb = WS_CIN + k * CIN_WI + kvi * CIN_ROW + lane * 16;
#pragma unroll
        for (int t = 0; t < 11; ++t) st_sc1(rs, gb + t * 1024 + hx[t] * (4 * CIN_WI - CIN_ROW), ov[t]);
      }
    }
  } else {
    const int h = 4 * xcd + (idx & 3), j = idx >> 2, wi = 4 * h + j;
    const bool ffn1 = h < 22;
    const bool ffn2 = h < 21 || (h == 21 && j < 2);
    v4u xa;
    asm volatile("global_load_dwordx4 %0, %1, off" : "=&v"(xa) : "v"(p.x + w * DM + lane * 8) : "memory");
    SB0();
    __builtin_amdgcn_s_barrier();
    {
      const char* src = (const char*)p.wq + (size_t)(64 * w + (lane >> 2)) * 8192 + h * 256 + j * 64 + (((lane & 3) ^ (2 * (lane >> 5))) * 16);
#pragma unroll
      for (int t = 0; t < 4; ++t) glds16<0>(src + (size_t)t * 16 * 8192, L_WQ + (64 * w + 16 * t) * 64);
    }
    SB0();
    v4u wukf[2][4];
    if (w >= 4) {
#pragma unroll
      for (int t = 0; t < 2; ++t)
#pragma unroll
        for (int s = 0; s < 4; ++s)
          wukf[t][s] = __builtin_nontemporal_load((const GAS v4u*)((const char*)p.wuk + (size_t)(128 * j + 32 * (w - 4) + 8 * (i16 >> 2) + 4 * t + (lane & 3)) * 8192 + h * 256 + s * 64 + g4 * 16));
    }
    SB0();
    asm volatile("s_waitcnt vmcnt(12)\n\ts_cmp_gt_u32 %1, 3\n\ts_cbranch_scc1 1f\n\ts_waitcnt vmcnt(4)\n1:" : "+v"(xa) : "s"(w) : "memory", "scc");
    SB0();
    {
      float v[8];
#pragma unroll
      for (int e = 0; e < 4; ++e) { v[2 * e] = bf2f(xa[e]); v[2 * e + 1] = bfhi(xa[e]); }
      const float piv = __uint_as_float(__builtin_amdgcn_readfirstlane(__float_as_uint(v[0])));
      float sm = 0.f, ss = 0.f;
#pragma unroll
      for (int e = 0; e < 8; ++e) { const float d = v[e] - piv; sm += d; ss = fmaf(d, d, ss); }
      float t = rowsum(add32(rs_add16(sm, ss)));
      auto sw2 = __builtin_amdgcn_permlane16_swap(__float_as_uint(t), __float_as_uint(t), false, false);
      const float md = __uint_as_float(sw2[0]) * (1.f / DM);
      const float var = fmaxf(fmaf(-md, md, __uint_as_float(sw2[1]) * (1.f / DM)), 0.f);
      const float mb = rbf(piv + md), vb = rbf(var);
      const float rden = __builtin_amdgcn_rcpf(rbf(__builtin_amdgcn_sqrtf(rbf(vb + 1e-5f))));
      v4u o4;
#pragma unroll
      for (int e = 0; e < 4; ++e) {
        const unsigned u = pk2(v[2 * e] - mb, v[2 * e + 1] - mb);
        o4[e] = pk2(bf2f(u) * rden, bfhi(u) * rden);
      }
      lds<v4u>(L_XN + w * 1040 + lane * 16) = o4;
    }
    if (tid < C_N) lds<unsigned>(L_CNT + tid * 4) = 0u;
    wg_barrier();
    if (w >= 4) VMWAIT(8); else VMWAIT(0);
    {
      f32x4 a0 = {0.f, 0.f, 0.f, 0.f}, a1 = {0.f, 0.f, 0.f, 0.f};
#pragma unroll
      for (int t = 0; t < 2; ++t) {
        const int ks = 2 * w + t;
        const v4u a = lds128(L_XN + (lane & 7) * 1040 + (32 * ks + 8 * g4) * 2);
        const unsigned ro = L_WQ + (32 * ks + 8 * g4 + (i16 >> 2)) * 64 + (lane & 3) * 8;
        const unsigned h0 = (g4 & 1) * 32, h1 = 32 - h0;
        const v2u b0 = trread(ro + h0), b1 = trread(ro + 256 + h0);
        const v2u c0 = trread(ro + h1), c1 = trread(ro + 256 + h1);
        a0 = mfma16(a, v4u{b0[0], b0[1], b1[0], b1[1]}, a0);
        a1 = mfma16(a, v4u{c0[0], c0[1], c1[0], c1[1]}, a1);
      }
      if (lane < 32) {
        lds<f32x4>(L_QP + w * 1024 + lane * 16) = a0;
        lds<f32x4>(L_QP + w * 1024 + 512 + lane * 16) = a1;
      }
    }
    wg_barrier();
    const unsigned tag = GET_TAG();
    v4u wof[4][8], wdf[8];
    if (w >= 4) {
      const int hk = w - 4;
      v4u qfr[4];
      {
        const unsigned base = WS_ABUF + (h * 4 + hk) * 1024 + lane * 16;
        v4u gq;
#pragma unroll 1
        for (unsigned spin = 0; spin < SPIN_CAP; ++spin) {
          gq = ld_sc1(rs, base);
          if (__all(((gq[1] ^ tag) | (gq[3] ^ tag)) == 0)) break;
          __builtin_amdgcn_s_sleep(1);
          CFENCE();
        }
        lds<v2u>(L_QH + (lane >> 3) * 272 + hk * 64 + (lane & 7) * 8) = v2u{gq[0], gq[2]};
        cnt_add(C_QH);
        cnt_wait(C_QH, 4);
#pragma unroll
        for (int s = 0; s < 4; ++s) qfr[s] = lds128(L_QH + (lane & 7) * 272 + s * 64 + g4 * 16);
      }
      f32x4 d4[2];
#pragma unroll
      for (int t = 0; t < 2; ++t) {
        d4[t] = f32x4{0.f, 0.f, 0.f, 0.f};
#pragma unroll
        for (int s = 0; s < 4; ++s) d4[t] = mfma16(wukf[t][s], qfr[s], d4[t]);
      }
      if (i16 < 8) {
        const v4u v = {pk2(d4[0][0], d4[0][1]), pk2(d4[0][2], d4[0][3]), pk2(d4[1][0], d4[1][1]), pk2(d4[1][2], d4[1][3])};
        st_sc1(rs, WS_QABS + (i16 * 32 + h) * 1024 + (128 * j + 32 * hk + 8 * g4) * 2, v);
      }
      VMWAIT(0);
      if (cnt_add_rtn(C_QST) == 5u) send_flags(rs, wi, lane, tag);
      cnt_wait<2>(C_GO, 1);
      wait_until(cnt_get(C_GOT), W_DLY);
      v4u wor[4][8], wdr[8];
      {
        const char* src = (const char*)p.wo + (size_t)(h * 128 + 8 * g4) * 1024 + hk * 256 + i16 * 16;
        const int u0 = ffn2 ? 64 * h + 16 * j : 0;
        const char* sd = (const char*)p.wd + (size_t)(u0 + 8 * (g4 & 1)) * 1024 + hk * 256 + i16 * 16;
#pragma unroll
        for (int n = 0; n < 40; ++n) {
          if (n < 32) asm volatile("global_load_dwordx4 %0, %1, off" : "=v"(wor[n >> 3][n & 7]) : "v"(src + (32 * (n >> 3) + (n & 7)) * 1024) : "memory");
          else asm volatile("global_load_dwordx4 %0, %1, off nt" : "=v"(wdr[n & 7]) : "v"(sd + (n & 7) * 1024) : "memory");
          if (n >= NFW - 1 && n < 39) VMWAIT(NFW - 1);
        }
      }
#define W8(a) "+v"(a[0]), "+v"(a[1]), "+v"(a[2]), "+v"(a[3]), "+v"(a[4]), "+v"(a[5]), "+v"(a[6]), "+v"(a[7])
      asm volatile("s_waitcnt vmcnt(32)" : W8(wor[0])::"memory");
      transpose8(wor[0], wof[0]);
      asm volatile("s_waitcnt vmcnt(24)" : W8(wor[1])::"memory");
      transpose8(wor[1], wof[1]);
      asm volatile("s_waitcnt vmcnt(16)" : W8(wor[2])::"memory");
      transpose8(wor[2], wof[2]);
      asm volatile("s_waitcnt vmcnt(8)" : W8(wor[3])::"memory");
      transpose8(wor[3], wof[3]);
      asm volatile("s_waitcnt vmcnt(0)" : W8(wdr)::"memory");
      transpose8(wdr, wdf);
#pragma unroll
      for (int u = 0; u < 4; ++u) asm volatile("" : W8(wof[u]));
      asm volatile("" : W8(wdf));
    } else {
      if (w == 0) {
        f32x4 q = lds<f32x4>(L_QP + lane * 16);
#pragma unroll
        for (int kk = 1; kk < 8; ++kk) q += lds<f32x4>(L_QP + kk * 1024 + lane * 16);
        const bool odd = lane & 1;
        const float o0 = xor1(odd ? q[0] : q[2]), o1 = xor1(odd ? q[1] : q[3]);
        const unsigned g0 = odd ? pk2(o0, q[2]) : pk2(q[0], o0), g1 = odd ? pk2(o1, q[3]) : pk2(q[1], o1);
        const int b0 = 4 * (g4 & 1) + (odd ? 2 : 0);
        const unsigned ga = WS_ABUF + wi * 1024 + (b0 * 16 + 8 * (lane >> 5) + (i16 >> 1)) * 8;
        st_sc1_64(rs, ga, v2u{g0, tag});
        st_sc1_64(rs, ga + 128, v2u{g1, tag});
      } else if (w == 1) {
        v2u f0, f1, f2;
        wait_until(t0, POLL_START);
        poll_flags(p.ws, WS_FLAG + (128 + wi) * 512 + lane * 8, tag, f0, f1, f2);
        lds<unsigned>(L_CNT + C_GOT * 4) = now32();
        cnt_add(C_GO);
        asm volatile("s_waitcnt vmcnt(0)" : "+v"(f0), "+v"(f1), "+v"(f2)::"memory");
      } else {
        const v4u pz = {POISON, POISON, POISON, POISON};
#pragma unroll
        for (int t = 0; t < 4; ++t) st_sc1(rs, WS_EIN + wi * 8192 + (4 * (w - 2) + t) * 1024 + lane * 16, pz);
        VMWAIT(0);
        if (cnt_add_rtn(C_QST) == 5u) send_flags(rs, wi, lane, tag);
      }
      cnt_wait<2>(C_GO, 1);
      const unsigned tgo = cnt_get(C_GOT);
      if (ffn1) {
        wait_until(tgo, F_DLY);
#pragma unroll
        for (int t = 0; t < 8; ++t) {
          const unsigned row = 32 * w + 4 * t + g4;
          const unsigned c = i16 ^ fswz(row);
          unsigned cc = c & 7;
          if (h == 21) cc &= 3;
          glds16<1>((const char*)((c & 8) ? p.wu : p.wg) + (size_t)(128 * j + row) * (FF * 2) + h * 128 + cc * 16, L_GU + (32 * w + 4 * t) * 256);
        }
        VMWAIT(0);
        cnt_add(C_GU);
        cnt_wait(C_GU, 4);
        f32x4 ag = {0.f, 0.f, 0.f, 0.f}, au = {0.f, 0.f, 0.f, 0.f};
#pragma unroll
        for (int ks = 0; ks < 4; ++ks) {
          const v4u a = lds128(L_XN + (lane & 7) * 1040 + (128 * j + 32 * ks + 8 * g4) * 2);
          const unsigned ra = 32 * ks + 8 * g4 + (i16 >> 2), rb = ra + 4, ch = 2 * w + ((lane & 3) >> 1);
          const v2u b0 = trread(L_GU + ra * 256 + ((ch ^ fswz(ra)) * 16) + 8 * (lane & 1));
          const v2u b1 = trread(L_GU + rb * 256 + ((ch ^ fswz(rb)) * 16) + 8 * (lane & 1));
          const v2u c0 = trread(L_GU + ra * 256 + (((ch + 8) ^ fswz(ra)) * 16) + 8 * (lane & 1));
          const v2u c1 = trread(L_GU + rb * 256 + (((ch + 8) ^ fswz(rb)) * 16) + 8 * (lane & 1));
          ag = mfma16(a, v4u{b0[0], b0[1], b1[0], b1[1]}, ag);
          au = mfma16(a, v4u{c0[0], c0[1], c1[0], c1[1]}, au);
        }
        if (lane < 32) {
          const unsigned fb = WS_FBUF + ((h * 4 + w) * 4 + j) * 2048 + (i16 * 8 + 4 * g4) * 8;
          st_sc1(rs, fb, v4u{__float_as_uint(ag[0]), tag, __float_as_uint(ag[1]), tag});
          st_sc1(rs, fb + 16, v4u{__float_as_uint(ag[2]), tag, __float_as_uint(ag[3]), tag});
          st_sc1(rs, fb + 1024, v4u{__float_as_uint(au[0]), tag, __float_as_uint(au[1]), tag});
          st_sc1(rs, fb + 1040, v4u{__float_as_uint(au[2]), tag, __float_as_uint(au[3]), tag});
        }
      }
      wait_until(tgo, W_DLY);
#pragma unroll
      for (int t = 0; t < 8; ++t) {
        const unsigned row = 32 * w + 4 * t + g4;
        glds16<1>((const char*)p.wuv + (size_t)(128 * j + row) * 8192 + h * 256 + ((i16 ^ fswz(row)) * 16), L_WUV + (32 * w + 4 * t) * 256);
      }
      if (w == 3 && ffn2) {
        float gs0, gs1, us0, us1;
#pragma unroll 1
        for (unsigned spin = 0; spin < SPIN_CAP; ++spin) {
          v4u fv[4][2];
#pragma unroll
          for (int sr = 0; sr < 4; ++sr)
#pragma unroll
            for (int gu = 0; gu < 2; ++gu) fv[sr][gu] = ld_sc1(rs, WS_FBUF + ((h * 4 + j) * 4 + sr) * 2048 + gu * 1024 + lane * 16);
          unsigned bad = 0;
          gs0 = gs1 = us0 = us1 = 0.f;
#pragma unroll
          for (int sr = 0; sr < 4; ++sr) {
            bad |= (fv[sr][0][1] ^ tag) | (fv[sr][0][3] ^ tag) | (fv[sr][1][1] ^ tag) | (fv[sr][1][3] ^ tag);
            gs0 += __uint_as_float(fv[sr][0][0]); gs1 += __uint_as_float(fv[sr][0][2]);
            us0 += __uint_as_float(fv[sr][1][0]); us1 += __uint_as_float(fv[sr][1][2]);
          }
          if (__all(bad == 0)) break;
          __builtin_amdgcn_s_sleep(4);
          CFENCE();
        }
#pragma unroll
        for (int e = 0; e < 2; ++e) {
          const float gg = rbf(e ? gs1 : gs0), uu = rbf(e ? us1 : us0);
          const float inner = 0.7978845608028654f * (gg + 0.044715f * gg * gg * gg);
          const float th = 1.f - 2.f / (__expf(2.f * inner) + 1.f);
          const float ge = rbf(0.5f * gg * (1.f + th));
          lds<u16>(L_CTX + (2 * (lane & 3) + e) * 336 + (128 + (lane >> 2)) * 2) = (u16)(pk2(ge * uu, 0.f) & 0xFFFFu);
        }
        {
          const int row = lane >> 2, chn = lane & 3;
          if (row >= 8 || chn > 1) lds<v4u>(L_CTX + row * 336 + 256 + chn * 16) = v4u{0u, 0u, 0u, 0u};
        }
      }
    }
    {
      const unsigned hh = lane >> 5, gi = min(lane & 31, 21);
      unsigned ca[3];
      {
        const unsigned ph = gi >= 11, d0 = 3 * (gi - 11 * ph);
#pragma unroll
        for (int q = 0; q < 3; ++q) {
          const unsigned dd = d0 + q;
          const unsigned cp = (4 * (dd >> 3) + 2 * ph + ((dd >> 2) & 1)) * 4 + (dd & 3);
          ca[q] = L_CL + (w + 8 * hh) * 272 + (dd < 32 ? cp * 4 : 256);
        }
      }
      v4u ml;
#pragma unroll 1
      for (unsigned spin = 0; spin < SPIN_CAP; ++spin) {
        ml = ld_sc1(rs, WS_ML + (wi * 128 + w * 16 + i16) * 16);
        if (__all(ml[2] == tag)) break;
        __builtin_amdgcn_s_sleep(2);
        CFENCE();
      }
      const unsigned cb = WS_CIN + wi * CIN_WI + (w * 16 + hh) * CIN_ROW + gi * 16;
      v4u dv[8];
#pragma unroll
      for (int t = 0; t < 8; ++t) dv[t] = ld_sc1(rs, cb + t * 2 * CIN_ROW);
      float wt[8];
      {
        const float ms = __uint_as_float(ml[0]), lsv = __uint_as_float(ml[1]);
        const float M = rowmax(ms);
        const float ee = ex2((ms - M) * LOG2E);
        const float wsv = ee / rowsum(ee * lsv);
#pragma unroll
        for (int t = 0; t < 8; ++t) wt[t] = __shfl(wsv, 2 * t + hh);
      }
#pragma unroll 1
      for (unsigned spin = 0; spin < SPIN_CAP; ++spin) {
        unsigned bad = 0;
#pragma unroll
        for (int t = 0; t < 8; ++t) bad |= dv[t][3] ^ tag;
        if (__all(bad == 0)) break;
        CFENCE();
#pragma unroll
        for (int t = 0; t < 8; ++t) dv[t] = ld_sc1(rs, cb + t * 2 * CIN_ROW);
      }
      v2f acc[3];
#pragma unroll
      for (int q = 0; q < 3; ++q) acc[q] = v2f{0.f, 0.f};
#pragma unroll
      for (int t = 0; t < 8; ++t)
#pragma unroll
        for (int q = 0; q < 3; ++q) acc[q] += v2f{wt[t], wt[t]} * v2f{bf2f(dv[t][q]), bfhi(dv[t][q])};
#pragma unroll
      for (int q = 0; q < 3; ++q) {
        const float a0 = add32(acc[q][0]), a1 = add32(acc[q][1]);
        const unsigned hi2 = pk2(a0, a1);
        const unsigned lo2 = pk2(a0 - bf2f(hi2), a1 - bfhi(hi2));
        lds<unsigned>(ca[q]) = hh ? lo2 : hi2;
      }
    }
    VMWAIT(0);
    wg_barrier();
    if (wi == 0 && tid == 0) st_sc1_32(rs, WS_GEN, tag);
    f32x4 oc[8];
    {
      f32x4 acc = {0.f, 0.f, 0.f, 0.f};
#pragma unroll
      for (int ks = 0; ks < 4; ++ks) {
        const v4u a = lds128(L_CL + i16 * 272 + (32 * ks + 8 * g4) * 2);
        const unsigned ra = 32 * ks + 8 * g4 + (i16 >> 2), rb = ra + 4, ch = 2 * w + ((lane & 3) >> 1);
        const v2u b0 = trread(L_WUV + ra * 256 + ((ch ^ fswz(ra)) * 16) + 8 * (lane & 1));
        const v2u b1 = trread(L_WUV + rb * 256 + ((ch ^ fswz(rb)) * 16) + 8 * (lane & 1));
        acc = mfma16(a, v4u{b0[0], b0[1], b1[0], b1[1]}, acc);
      }
#pragma unroll
      for (int jj = 0; jj < 8; ++jj) oc[jj] = f32x4{0.f, 0.f, 0.f, 0.f};
      if (w >= 4 && ffn2) {
        const v4u a = lds128(L_CTX + i16 * 336 + (128 + 8 * g4) * 2);
#pragma unroll
        for (int jj = 0; jj < 8; ++jj) oc[jj] = mfma16(a, wdf[jj], oc[jj]);
      }
#pragma unroll
      for (int e = 0; e < 4; ++e) {
        const float v = add32(acc[e]);
        const unsigned hi1 = pk2(v, 0.f);
        const unsigned lo1 = pk2(v - bf2f(hi1), 0.f);
        lds<u16>(L_CTX + (4 * g4 + e) * 336 + (16 * w + i16) * 2) = (u16)(g4 < 2 ? hi1 : lo1);
      }
    }
    wg_barrier();
    if (w >= 4) {
      const int hk = w - 4;
#pragma unroll
      for (int u = 0; u < 4; ++u) {
        const v4u a = lds128(L_CTX + i16 * 336 + (32 * u + 8 * g4) * 2);
#pragma unroll
        for (int jj = 0; jj < 8; ++jj) oc[jj] = mfma16(a, wof[u][jj], oc[jj]);
      }
      const unsigned eb = WS_EIN + ((4 * (g4 & 1) + 2 * (lane >> 5)) * 16 + 4 * hk + (i16 >> 2)) * 8192 + wi * 64 + (i16 & 3) * 16;
#pragma unroll
      for (int e = 0; e < 2; ++e) {
        float r[8];
#pragma unroll
        for (int jj = 0; jj < 8; ++jj) r[jj] = rs_add32(oc[jj][e], oc[jj][e + 2]);
        st_sc1(rs, eb + e * 16 * 8192, v4u{san(pk2(r[0], r[1])), san(pk2(r[2], r[3])), san(pk2(r[4], r[5])), san(pk2(r[6], r[7]))});
      }
    } else {
      const unsigned xf = *(const GAS unsigned*)(p.x + wi * 32 + 2 * i16);
      v4u ev[2];
#pragma unroll 1
      for (unsigned spin = 0; spin < SPIN_CAP; ++spin) {
#pragma unroll
        for (int t = 0; t < 2; ++t) ev[t] = ld_sc1(rs, WS_EIN + wi * 8192 + (2 * w + t) * 1024 + lane * 16);
        const unsigned mxv = max(max(max(ev[0][0], ev[0][1]), max(ev[0][2], ev[0][3])), max(max(ev[1][0], ev[1][1]), max(ev[1][2], ev[1][3])));
        if (__all(mxv != POISON)) break;
        CFENCE();
      }
      float acc[8];
#pragma unroll
      for (int e = 0; e < 4; ++e) {
        acc[2 * e] = bf2f(ev[0][e]) + bf2f(ev[1][e]);
        acc[2 * e + 1] = bfhi(ev[0][e]) + bfhi(ev[1][e]);
      }
      float r4[4], r2[2];
#pragma unroll
      for (int e = 0; e < 4; ++e) r4[e] = rs_add32(acc[e], acc[e + 4]);
#pragma unroll
      for (int e = 0; e < 2; ++e) r2[e] = rs_add16(r4[e], r4[e + 2]);
#pragma unroll
      for (int e = 0; e < 2; ++e) { r2[e] += ror16<8>(r2[e]); r2[e] += ror16<4>(r2[e]); }
      if (i16 < 4) lds<v2f>(L_FIN + w * 128 + i16 * 32 + g4 * 8) = v2f{r2[0], r2[1]};
      if (cnt_add_rtn(C_FIN) == 3u) {
        const LAS v2f* f = (const LAS v2f*)(size_t)(L_FIN + i16 * 8);
        const v2f sm = (f[0] + f[16]) + (f[32] + f[48]);
        if (lane < 16) *(GAS unsigned*)(p.out + wi * 32 + 2 * lane) = pk2(bf2f(xf) + sm[0], bfhi(xf) + sm[1]);
      }
    }
  }
}

extern "C" void kernel_launch(void* const* d_in, const int* in_sizes, int n_in, void* d_out, int out_size, void* d_ws,
                              size_t ws_size, hipStream_t stream) {
  Params p;
  p.x = (const u16*)d_in[0];
  p.ckv = (const u16*)d_in[1];
  p.wq = (const u16*)d_in[2];
  p.wuk = (const u16*)d_in[3];
  p.wuv = (const u16*)d_in[4];
  p.wo = (const u16*)d_in[5];
  p.wg = (const u16*)d_in[7];
  p.wu = (const u16*)d_in[8];
  p.wd = (const u16*)d_in[9];
  p.out = (u16*)d_out;
  p.ws = (unsigned char*)d_ws;
  (void)hipFuncSetAttribute((const void*)fused_kernel, hipFuncAttributeMaxDynamicSharedMemorySize, LDS_BYTES);
  fused_kernel<<<256, 512, LDS_BYTES, stream>>>(p);
}
```

```cpp
#include <hip/hip_runtime.h>
#include <stdint.h>

typedef unsigned short u16;
typedef unsigned long long u64;
typedef unsigned v4u __attribute__((ext_vector_type(4)));
typedef unsigned v2u __attribute__((ext_vector_type(2)));
typedef float f32x4 __attribute__((ext_vector_type(4)));
typedef float f32x16 __attribute__((ext_vector_type(16)));
typedef float v2f __attribute__((ext_vector_type(2)));
typedef short bf16x8 __attribute__((ext_vector_type(8)));
typedef short v4s __attribute__((ext_vector_type(4)));
typedef __bf16 v2bf __attribute__((ext_vector_type(2)));
#define GAS __attribute__((address_space(1)))
#define LAS __attribute__((address_space(3)))
#define DI __device__ __forceinline__

#define NF 8
#define R2AT (47 + NF)
#define POLL_START 250
#define POLL_GAP 4
#define Q_DLY 0
#define KV_DLY 150
#define E_DLY 4
#define T_F 0
#define T_UV 600
#define T_O 0
#define MLAT 17
#define FR4 1
#define PAUSE 1
#define QPF 0
#define WPF 2
#define BPF 0
#define BPF_DLY 0
#define WPF_DLY 0
#define FMAP 0
#define PK 0
#define FLAG_FIRST 0
#define F2AT 1
#define GO_DLY 100
#define C_DLY 0
#define NFW 16

constexpr int DM = 512, FF = 1376;
constexpr unsigned WS_GEN = 0;
constexpr unsigned WS_FLAG = 4096;
constexpr unsigned WS_FLAG2 = WS_FLAG + 131072;
constexpr unsigned WS_QABS = WS_FLAG2 + 65536;
constexpr unsigned WS_ML = WS_QABS + 524288;
constexpr unsigned WS_EIN = WS_ML + 262144;
constexpr unsigned WS_CIN = WS_EIN + 1048576;
constexpr unsigned CIN_ROW = 384, CIN_WI = 128 * CIN_ROW;
constexpr unsigned WS_END = WS_CIN + 128 * CIN_WI;
constexpr unsigned L_Q = 0;
constexpr unsigned L_P = L_Q;
constexpr unsigned L_MX = L_Q + 4096;
constexpr unsigned L_LS = L_Q + 4608;
constexpr unsigned L_SLOT = 32768;
constexpr unsigned L_XN = 0;
constexpr unsigned L_QH = 8448;
constexpr unsigned L_CL = L_QH + 2176;
constexpr unsigned L_CTX = L_CL + 2176;
constexpr unsigned L_HF = L_CTX + 2176;
constexpr unsigned L_FP = L_HF + 640;
constexpr unsigned L_WQ = 32768;
constexpr unsigned L_GU = 65536;
constexpr unsigned L_WUV = 98304;
constexpr unsigned L_CNT = 32512;
constexpr unsigned LDS_BYTES = 163840;
enum { C_P = 0, C_OVF = 1, C_PAUSE = 2, C_QA = 3, C_BOK, C_QFREE, C_MX, C_PVD, C_QST, C_GU, C_FFN, C_UV, C_GO, C_RDY = 13, C_N = 16 };

constexpr unsigned POISON = 0xFFFFFFFFu;
constexpr unsigned SPIN_CAP = 1u << 20;

struct Params {
  const u16 *x, *ckv, *wq, *wuk, *wuv, *wo, *wg, *wu, *wd;
  u16* out;
  unsigned char* ws;
};

DI float bf2f(unsigned v) { return __uint_as_float(v << 16); }
DI float bfhi(unsigned v) { return __uint_as_float(v & 0xFFFF0000u); }
DI unsigned pk2(float a, float b) {
  v2f t = {a, b};
  return __builtin_bit_cast(unsigned, __builtin_convertvector(t, v2bf));
}
DI float rbf(float a) { return bf2f(pk2(a, 0.f) & 0xFFFFu); }
DI unsigned san(unsigned v) { return min(v, 0xFFFFFFFEu); }
DI f32x4 mfma16(v4u a, v4u b, f32x4 c) {
  return __builtin_amdgcn_mfma_f32_16x16x32_bf16(__builtin_bit_cast(bf16x8, a), __builtin_bit_cast(bf16x8, b), c, 0, 0, 0);
}
DI f32x16 mfma32(v4u a, v4u b, f32x16 c) {
  return __builtin_amdgcn_mfma_f32_32x32x16_bf16(__builtin_bit_cast(bf16x8, a), __builtin_bit_cast(bf16x8, b), c, 0, 0, 0);
}
DI v4u lds128(unsigned a) { return *(LAS v4u*)(size_t)a; }
DI v2u trread(unsigned a) { return __builtin_bit_cast(v2u, __builtin_amdgcn_ds_read_tr16_b64_v4i16((LAS v4s*)(size_t)a)); }
template <class T> DI LAS T& lds(unsigned a) { return *(LAS T*)(size_t)a; }
template <int POL> DI void glds16(const void* gsrc, unsigned lds_dst) {
  unsigned keep;
  if (POL == 1) asm volatile("s_mov_b32 %0, m0\n\ts_mov_b32 m0, %2\n\ts_nop 0\n\tglobal_load_lds_dwordx4 %1, off nt\n\ts_mov_b32 m0, %0" : "=&s"(keep) : "v"(gsrc), "s"(lds_dst) : "memory");
  else if (POL == 2) asm volatile("s_mov_b32 %0, m0\n\ts_mov_b32 m0, %2\n\ts_nop 0\n\tglobal_load_lds_dwordx4 %1, off sc1\n\ts_mov_b32 m0, %0" : "=&s"(keep) : "v"(gsrc), "s"(lds_dst) : "memory");
  else asm volatile("s_mov_b32 %0, m0\n\ts_mov_b32 m0, %2\n\ts_nop 0\n\tglobal_load_lds_dwordx4 %1, off\n\ts_mov_b32 m0, %0" : "=&s"(keep) : "v"(gsrc), "s"(lds_dst) : "memory");
}
DI void glds16s(unsigned voff, const void* sbase, unsigned lds_dst) {
  unsigned keep;
  asm volatile("s_mov_b32 %0, m0\n\ts_mov_b32 m0, %3\n\ts_nop 0\n\tglobal_load_lds_dwordx4 %1, %2 nt\n\ts_mov_b32 m0, %0" : "=&s"(keep) : "v"(voff), "s"(sbase), "s"(lds_dst) : "memory");
}
DI void gld16s(v4u& d, unsigned voff, const void* sbase) {
  asm volatile("global_load_dwordx4 %0, %1, %2 nt" : "=v"(d) : "v"(voff), "s"(sbase) : "memory");
}
#define VMWAIT(n) asm volatile("s_waitcnt vmcnt(%0)" ::"i"(n) : "memory")
#define LGKM0() asm volatile("s_waitcnt lgkmcnt(0)" ::: "memory")
#define CFENCE() asm volatile("" ::: "memory")
#define SB0() __builtin_amdgcn_sched_barrier(0)
DI void wg_barrier() {
  LGKM0();
  __builtin_amdgcn_s_barrier();
  CFENCE();
}
DI void cnt_add(unsigned c) {
  u64 keep;
  asm volatile("s_mov_b64 %0, exec\n\ts_mov_b64 exec, 1\n\tds_add_u32 %1, %2 offset:%3\n\ts_mov_b64 exec, %0" : "=&s"(keep) : "v"(c * 4u), "v"(1u), "i"(L_CNT) : "memory");
}
DI unsigned cnt_add_rtn(unsigned c) {
  u64 keep;
  unsigned v;
  asm volatile("s_mov_b64 %0, exec\n\ts_mov_b64 exec, 1\n\tds_add_rtn_u32 %1, %2, %3 offset:%4\n\ts_waitcnt lgkmcnt(0)\n\ts_mov_b64 exec, %0"
               : "=&s"(keep), "=&v"(v) : "v"(c * 4u), "v"(1u), "i"(L_CNT) : "memory");
  return __builtin_amdgcn_readfirstlane(v);
}
DI unsigned cnt_get(unsigned c) {
  unsigned v;
  asm volatile("ds_read_b32 %0, %1 offset:%2\n\ts_waitcnt lgkmcnt(0)" : "=v"(v) : "v"(c * 4u), "i"(L_CNT) : "memory");
  return __builtin_amdgcn_readfirstlane(v);
}
template <int SLP = 0> DI void cnt_wait(unsigned c, unsigned target) {
#pragma unroll 1
  for (unsigned s = 0; s < SPIN_CAP * 16u; ++s) {
    if (cnt_get(c) >= target) break;
    if (SLP) __builtin_amdgcn_s_sleep(SLP);
  }
}
DI unsigned now32() { return (unsigned)__builtin_amdgcn_s_memrealtime(); }
DI void wait_until(unsigned t0, unsigned ticks) {
  if (ticks == 0) return;
#pragma unroll 1
  while (now32() - t0 < ticks) __builtin_amdgcn_s_sleep(1);
}
DI v4u ld_sc1(__amdgpu_buffer_rsrc_t rs, unsigned off) { return __builtin_amdgcn_raw_buffer_load_b128(rs, off, 0, 16); }
DI v2u ld_sc1_64(__amdgpu_buffer_rsrc_t rs, unsigned off) { return __builtin_amdgcn_raw_buffer_load_b64(rs, off, 0, 16); }
DI void st_sc1(__amdgpu_buffer_rsrc_t rs, unsigned off, v4u v) { __builtin_amdgcn_raw_buffer_store_b128(v, rs, off, 0, 16); }
DI void st_sc1_64(__amdgpu_buffer_rsrc_t rs, unsigned off, v2u v) { __builtin_amdgcn_raw_buffer_store_b64(v, rs, off, 0, 16); }
DI void st_sc1_32(__amdgpu_buffer_rsrc_t rs, unsigned off, unsigned v) { __builtin_amdgcn_raw_buffer_store_b32(v, rs, off, 0, 16); }
DI unsigned fswz(unsigned row) { return ((row & 3u) << 2) | ((0x78u >> (2u * ((row >> 2) & 3u))) & 3u); }
template <int N> DI float ror16(float v) {
  return __int_as_float(__builtin_amdgcn_update_dpp(0, __float_as_int(v), 0x120 + N, 0xF, 0xF, false));
}
DI float xor1(float v) { return __int_as_float(__builtin_amdgcn_update_dpp(0, __float_as_int(v), 0xB1, 0xF, 0xF, false)); }
DI float add16(float v) { auto r = __builtin_amdgcn_permlane16_swap(__float_as_uint(v), __float_as_uint(v), false, false); return __uint_as_float(r[0]) + __uint_as_float(r[1]); }
DI float add32(float v) { auto r = __builtin_amdgcn_permlane32_swap(__float_as_uint(v), __float_as_uint(v), false, false); return __uint_as_float(r[0]) + __uint_as_float(r[1]); }
DI float rs_add16(float a, float b) { auto r = __builtin_amdgcn_permlane16_swap(__float_as_uint(a), __float_as_uint(b), false, false); return __uint_as_float(r[0]) + __uint_as_float(r[1]); }
DI float rs_add32(float a, float b) { auto r = __builtin_amdgcn_permlane32_swap(__float_as_uint(a), __float_as_uint(b), false, false); return __uint_as_float(r[0]) + __uint_as_float(r[1]); }
DI float rs_max16(float a, float b) { auto r = __builtin_amdgcn_permlane16_swap(__float_as_uint(a), __float_as_uint(b), false, false); return fmaxf(__uint_as_float(r[0]), __uint_as_float(r[1])); }
DI float max32(float v) { auto r = __builtin_amdgcn_permlane32_swap(__float_as_uint(v), __float_as_uint(v), false, false); return fmaxf(__uint_as_float(r[0]), __uint_as_float(r[1])); }
DI float rowsum(float v) { v += ror16<8>(v); v += ror16<4>(v); v += ror16<2>(v); v += ror16<1>(v); return v; }
DI float rowmax(float v) { v = fmaxf(v, ror16<8>(v)); v = fmaxf(v, ror16<4>(v)); v = fmaxf(v, ror16<2>(v)); v = fmaxf(v, ror16<1>(v)); return v; }
DI float ex2(float v) { return __builtin_amdgcn_exp2f(v); }
constexpr float LOG2E = 1.4426950408889634f;

DI void transpose8(const v4u (&r)[8], v4u (&c)[8]) {
#pragma unroll
  for (int q = 0; q < 4; ++q) {
#pragma unroll
    for (int e2 = 0; e2 < 4; ++e2) {
      unsigned a = r[2 * e2][q], b = r[2 * e2 + 1][q];
      c[2 * q][e2] = __builtin_amdgcn_perm(b, a, 0x05040100u);
      c[2 * q + 1][e2] = __builtin_amdgcn_perm(b, a, 0x07060302u);
    }
  }
}
#define PLD(f) asm volatile("global_atomic_add_x2 %0, %1, %2, %3 sc0 sc1" : "=&v"(f) : "v"(fo), "v"(zz), "s"(wsp) : "memory")
#define PCHK(f) (__extension__({ asm volatile("s_waitcnt vmcnt(2)" : "+v"(f)::"memory"); __all(((f[0] ^ tag) | (f[1] ^ tag)) == 0); }))
DI void poll_flags(const unsigned char* wsp, unsigned fo, unsigned tag, v2u& f0, v2u& f1, v2u& f2) {
  const v2u zz = {0u, 0u};
  PLD(f0); __builtin_amdgcn_s_sleep(POLL_GAP);
  PLD(f1); __builtin_amdgcn_s_sleep(POLL_GAP);
  PLD(f2);
#pragma unroll 1
  for (unsigned spin = 0; spin < SPIN_CAP; ++spin) {
    if (PCHK(f0)) break;
    PLD(f0);
    if (PCHK(f1)) break;
    PLD(f1);
    if (PCHK(f2)) break;
    PLD(f2);
  }
}

DI void send_flags(__amdgpu_buffer_rsrc_t rs, int wi, int lane, unsigned tag) {
#pragma unroll
  for (int t = 0; t < 4; ++t) st_sc1_32(rs, WS_FLAG + (64 * t + lane) * 512 + wi * 4, tag);
}

#define PIN(x) asm volatile("" : "+v"(x))
__global__ void __launch_bounds__(512, 2) fused_kernel(Params p) {
  extern __shared__ __attribute__((aligned(16))) unsigned char smem[];
  const u64 t0l = __builtin_amdgcn_s_memrealtime();
  const unsigned t0 = (unsigned)t0l;
  const int tid = threadIdx.x, lane = tid & 63;
  const int w = __builtin_amdgcn_readfirstlane(tid >> 6);
  const int cu = blockIdx.x;
  const int xcd = cu & 7, r8 = cu >> 3, idx = r8 >> 1;
  const __amdgpu_buffer_rsrc_t rs = __builtin_amdgcn_make_buffer_rsrc((void*)p.ws, 0, (int)WS_END, 0x00020000);
  const int g4 = lane >> 4, i16 = lane & 15;
  unsigned gen;
  asm volatile("s_load_dword %0, %1, 0x0" : "=s"(gen) : "s"(p.ws), "s"((unsigned)(size_t)(LAS unsigned char*)smem) : "memory");
  unsigned gen1;
#define GET_TAG() (__extension__({ asm volatile("s_waitcnt lgkmcnt(0)\n\ts_add_u32 %0, %1, 1" : "=s"(gen1) : "s"(gen) : "memory", "scc"); (gen1 & 0x3FFFu) | ((gen1 << 2) & 0x3FFF0000u); }))

  if ((r8 & 1) == 0) {
    const int b = xcd, s = idx;
    if (w >= 4) {
      const int sw = w - 4;
      const char* base = (const char*)p.ckv + ((size_t)(b * 16 + s) * 256 + 16 * sw) * 1024;
      unsigned vo[16];
#pragma unroll
      for (int i = 0; i < 16; ++i) vo[i] = i * 1024 + ((lane ^ fswz(i)) * 16);
      v4u st[32];
      const unsigned wa = L_SLOT + sw * 16384 + lane * 16;
      bool done2 = false, run = !PAUSE;
      wait_until(t0, KV_DLY);
#define W8(a, o) "+v"(a[o]), "+v"(a[o + 1]), "+v"(a[o + 2]), "+v"(a[o + 3]), "+v"(a[o + 4]), "+v"(a[o + 5]), "+v"(a[o + 6]), "+v"(a[o + 7])
#pragma unroll
      for (int i = 0; i < 64; ++i) {
        const int r = i >> 4;
        if (r < 2) glds16s(vo[i & 15], base + r * 65536, L_SLOT + r * 65536 + (16 * sw + (i & 15)) * 1024);
        else gld16s(st[i - 32], vo[i & 15], base + r * 65536);
        if (i == NF - 1) wg_barrier();
        if (i >= NF - 1) VMWAIT(NF - 1);
        if (PAUSE && i >= NF && i < 63 && !run) {
          if (cnt_get(C_PAUSE)) { cnt_wait<1>(C_QA, 4); run = true; }
        }
        if (i - NF + 1 == 15) { cnt_add(C_RDY + 0); }
        if (i - NF + 1 == 31) { cnt_add(C_RDY + 1); }
        if (i == R2AT) {
          asm volatile("s_waitcnt vmcnt(%16)" : W8(st, 0), W8(st, 8) : "i"(R2AT - 47) : "memory");
          if (cnt_get(C_PVD) >= 4) {
#pragma unroll
            for (int q = 0; q < 16; ++q) lds<v4u>(wa + q * 1024) = st[q];
            cnt_add(C_RDY + 2);
            done2 = true;
          }
        }
      }
      if (!done2) {
        cnt_wait<1>(C_PVD, 4);
#pragma unroll
        for (int q = 0; q < 16; ++q) lds<v4u>(wa + q * 1024) = st[q];
        cnt_add(C_RDY + 2);
      }
      cnt_wait<1>(C_PVD, 8);
#pragma unroll
      for (int q = 0; q < 16; ++q) {
        if (q >= 16 - NF) asm volatile("s_waitcnt vmcnt(%1)" : "+v"(st[16 + q]) : "i"(15 - q) : "memory");
        else PIN(st[16 + q]);
        lds<v4u>(wa + 65536 + q * 1024) = st[16 + q];
      }
      LGKM0();
    } else {
      const int k = w;
      const int kvi = b * 16 + s;
      v2u f0 = {0u, 0u}, f1 = f0, f2 = f0;
      if (k == 0 && lane < C_N) lds<unsigned>(L_CNT + lane * 4) = 0u;
      wg_barrier();
      const unsigned tag = GET_TAG();
      f32x16 oacc[4];
      float nr0 = 0.f, nr1 = 0.f;
      v2f lsum = {0.f, 0.f};
      const float OVT = 56.f;
      unsigned np = 0, nx = 0;
      unsigned aq0, ap0;
      const unsigned hh = lane >> 5, hd = lane & 31;
      {
        aq0 = L_SLOT + (16 * k + i16) * 1024 + ((fswz(i16) ^ g4) * 16);
        const unsigned blk = (lane >> 4) & 1, q4 = i16 >> 2, pp = lane & 3;
        const unsigned r0 = 8 * hh + q4;
        const unsigned ch = 16 * k + 2 * (pp & 1) + blk;
        ap0 = L_SLOT + r0 * 1024 + ((ch ^ fswz(r0)) * 16) + 8 * (pp >> 1);
      }
      unsigned po = L_P + k * 1024 + (i16 + 32 * (g4 >> 1)) * 16 + (g4 & 1) * 8;
      unsigned l16 = lane * 16;
      PIN(po); PIN(l16); PIN(aq0); PIN(ap0);
      v4u sel;
#pragma unroll
      for (int q = 0; q < 4; ++q) sel[q] = ((g4 & 1) == (i16 >> 3) && ((i16 & 7) >> 1) == q) ? ((i16 & 1) ? 0x3F800000u : 0x3F80u) : 0u;
      unsigned qs = WS_QABS + (g4 >> 1) * 262144 + b * 32768 + k * 8192 + (lane & 31) * 16;
      unsigned qd = WS_QABS + b * 32768 + (16 + i16) * 1024 + (60 + g4) * 16;
      unsigned qw[8], qw15;
#pragma unroll
      for (int u = 0; u < 8; ++u) { qw[u] = L_Q + k * 8192 + ((i16 ^ fswz(8 * k + u)) * 16) + (g4 >> 1) * 256 + (g4 & 1) * 8; PIN(qw[u]); }
      qw15 = qw[7] - (k == 3 && g4 >= 2 && i16 >= 12 ? 4096u : 0u);
      unsigned qr = L_Q + i16 * 1024 + ((fswz(i16) ^ g4) * 16);
      PIN(sel); PIN(qs); PIN(qd); PIN(qw15); PIN(qr);
#pragma unroll
      for (int ct = 0; ct < 4; ++ct) {
#pragma unroll
        for (int e = 0; e < 16; ++e) oacc[ct][e] = 0.f;
        PIN(oacc[ct]);
      }
      if (k == 0) {
        wait_until(t0, POLL_START);
        const unsigned char* wsp = p.ws;
        const unsigned fo = WS_FLAG + kvi * 512 + lane * 8;
        const v2u zz = {0u, 0u};
        bool sent = false;
#define PCHK2(f) (__extension__({ asm volatile("s_waitcnt vmcnt(2)" : "+v"(f)::"memory"); \
        const int miss = __popcll(__ballot(f[0] != tag)) + __popcll(__ballot(f[1] != tag)); \
        if (PAUSE && !sent && miss <= PK) { cnt_add(C_PAUSE); sent = true; } miss == 0; }))
        PLD(f0); __builtin_amdgcn_s_sleep(POLL_GAP);
        PLD(f1); __builtin_amdgcn_s_sleep(POLL_GAP);
        PLD(f2);
#pragma unroll 1
        for (unsigned spin = 0; spin < SPIN_CAP; ++spin) {
          if (PCHK2(f0)) break;
          PLD(f0);
          if (PCHK2(f1)) break;
          PLD(f1);
          if (PCHK2(f2)) break;
          PLD(f2);
        }
        cnt_add(C_BOK);
      } else {
        cnt_wait<1>(C_BOK, 1);
      }
      if (Q_DLY) __builtin_amdgcn_s_sleep(Q_DLY);
      v4u qf[32], da, db;
      if (QPF) {
        v4u pv = ld_sc1(rs, WS_QABS + (s >> 3) * 262144 + b * 32768 + ((s & 7) * 4 + k) * 1024 + lane * 16);
        asm volatile("" :: "v"(pv));
        if (QPF > 1) __builtin_amdgcn_s_sleep(QPF);
      }
#pragma unroll 1
      for (unsigned spin = 0; spin < SPIN_CAP; ++spin) {
        v4u qv[16];
#pragma unroll
        for (int t = 0; t < 16; ++t) qv[t] = __builtin_amdgcn_raw_buffer_load_b128(rs, qs + (t & 7) * 512, (t >> 3) * 4096, 16);
        da = ld_sc1(rs, qd);
        db = __builtin_amdgcn_raw_buffer_load_b128(rs, qd, 262144, 16);
        SB0();
        float chk = 0.f;
        f32x4 d[16];
#pragma unroll
        for (int t = 0; t < 18; ++t) {
          if (t < 16) d[t] = mfma16(sel, qv[t], f32x4{0.f, 0.f, 0.f, 0.f});
          if (t >= 2) {
            const int u = t - 2;
            lds<v2u>((u == 15 ? qw15 : qw[u >> 1]) + (u >> 1) * 1024 + (u & 1) * 512) = v2u{pk2(d[u][0], d[u][1]), pk2(d[u][2], d[u][3])};
            chk += d[u][0];
          }
          SB0();
        }
        if (!__any(chk != chk || max(da[0], db[0]) == POISON)) break;
        unsigned mx = max(da[0], db[0]);
#pragma unroll
        for (int t = 0; t < 16; ++t) mx = max(mx, qv[t][0]);
        if (!__any(mx == POISON)) break;
        CFENCE();
      }
      cnt_add(C_QA);
      asm volatile("" : "+v"(f0), "+v"(f1), "+v"(f2)::"memory");
#pragma unroll
      for (int e = 0; e < 4; ++e) qf[31][e] = pk2(bf2f(da[e]) + bf2f(db[e]), bfhi(da[e]) + bfhi(db[e]));
      PIN(qf[31]);
      cnt_wait(C_QA, 4);
#pragma unroll
      for (int f = 0; f < 31; ++f) qf[(f >> 1) + 16 * (f & 1)] = lds128((qr ^ (((f >> 1) & 3) * 64)) + (f >> 3) * 256 + (f & 1) * 16384);
      cnt_add(C_QFREE);
      const unsigned nq = 4;
      v4u ob[4], pf[4];
      auto ldo = [&](int n) {
        const v2u t0_ = trread((ap0 ^ ((n >> 2) * 64)) + (n & 3) * 16384), t1_ = trread((ap0 ^ ((n >> 2) * 64 + 32)) + (n & 3) * 16384 + 4096);
        ob[n & 3] = v4u{t0_[0], t0_[1], t1_[0], t1_[1]};
      };
      f32x4 s0, s1;
      auto do_qk = [&]() {
        v4u ab[6];
        unsigned aq[4];
#pragma unroll
        for (int u = 0; u < 4; ++u) aq[u] = aq0 ^ (u * 64);
#pragma unroll
        for (int u = 0; u < 6; ++u) ab[u] = lds128(aq[u & 3] + (u >> 2) * 256);
        s0 = f32x4{nr0, nr0, nr0, nr0}; s1 = f32x4{nr1, nr1, nr1, nr1};
#pragma unroll
        for (int u = 0; u < 16; ++u) {
          SB0();
          s0 = mfma16(ab[u % 6], qf[u], s0);
          s1 = mfma16(ab[u % 6], qf[16 + u], s1);
          SB0();
          if (u + 6 < 16) ab[u % 6] = lds128(aq[(u + 6) & 3] + ((u + 6) >> 2) * 256);
          else if (u >= 12) ldo(u - 12);
        }
      };
      auto exchange = [&](bool first) {
        const float mw0 = fmaxf(fmaxf(s0[0], s0[1]), fmaxf(s0[2], s0[3])), mw1 = fmaxf(fmaxf(s1[0], s1[1]), fmaxf(s1[2], s1[3]));
        const float mw = max32(rs_max16(mw0, mw1));
        if (lane < 32) lds<float>(L_MX + k * 128 + lane * 4) = mw;
        cnt_add(C_MX);
        nx += 4;
        cnt_wait(C_MX, nx);
        const LAS float* mx = (const LAS float*)(size_t)(L_MX + i16 * 4);
        float d0 = fmaxf(fmaxf(mx[0], mx[32]), fmaxf(mx[64], mx[96]));
        float d1 = fmaxf(fmaxf(mx[16], mx[48]), fmaxf(mx[80], mx[112]));
        if (!first) {
          d0 = fmaxf(d0, 0.f); d1 = fmaxf(d1, 0.f);
          const float al0 = ex2(-d0), al1 = ex2(-d1);
          lsum[0] *= al0; lsum[1] *= al1;
          const float alpha = (lane & 16) ? al1 : al0;
#pragma unroll
          for (int ct = 0; ct < 4; ++ct)
#pragma unroll
            for (int e = 0; e < 16; ++e) oacc[ct][e] *= alpha;
        }
#pragma unroll
        for (int e = 0; e < 4; ++e) { s0[e] -= d0; s1[e] -= d1; }
        nr0 -= d0; nr1 -= d1;
      };
      v2f ln;
      auto do_p = [&]() {
        float e0[4], e1[4];
#pragma unroll
        for (int e = 0; e < 4; ++e) { e0[e] = ex2(s0[e]); e1[e] = ex2(s1[e]); }
        lds<v2u>(po) = v2u{pk2(e0[0], e0[1]), pk2(e0[2], e0[3])};
        lds<v2u>(po + 256) = v2u{pk2(e1[0], e1[1]), pk2(e1[2], e1[3])};
        ln = lsum + ((v2f{e0[0], e1[0]} + v2f{e0[1], e1[1]}) + (v2f{e0[2], e1[2]} + v2f{e0[3], e1[3]}));
      };
      auto over = [&]() {
        const float m = fmaxf(fmaxf(fmaxf(s0[0], s0[1]), fmaxf(s0[2], s0[3])), fmaxf(fmaxf(s1[0], s1[1]), fmaxf(s1[2], s1[3])));
        return __any(m > OVT);
      };
#pragma unroll 1
      for (int r = 0; r < 3; ++r) {
        cnt_wait(C_RDY + r, 4);
        do_qk();
        bool xch = r == 0;
        if (xch) cnt_wait(C_QFREE, nq);
        else if (over()) lds<unsigned>(L_CNT + C_OVF * 4) = r;
#pragma unroll 1
        for (;;) {
          if (xch) exchange(r == 0);
          do_p();
          cnt_add(C_P);
          np += 4;
          v2u cv;
#pragma unroll 1
          for (unsigned sp = 0; sp < SPIN_CAP * 16u; ++sp) {
            asm volatile("ds_read_b64 %0, %1 offset:%2\n\ts_waitcnt lgkmcnt(0)" : "=v"(cv) : "v"(0u), "i"(L_CNT) : "memory");
            if (__builtin_amdgcn_readfirstlane(cv[0]) >= np) break;
          }
          if (xch || __builtin_amdgcn_readfirstlane(cv[1]) != (unsigned)r) break;
          xch = true;
        }
        lsum = ln;
#pragma unroll
        for (int n = 0; n < 4; ++n) pf[n] = lds128(L_P + n * 1024 + l16);
#pragma unroll
        for (int n = 0; n < 16; ++n) {
          SB0();
          oacc[n >> 2] = mfma32(ob[n & 3], pf[n & 3], oacc[n >> 2]);
          SB0();
          if (n + 4 < 16) ldo(n + 4);
        }
        cnt_add(C_PVD);
        {
          const unsigned dl = (r & 1) ? 0u - 65536u : 65536u;
          aq0 += dl; ap0 += dl;
          PIN(aq0); PIN(ap0);
        }
      }
      {
        const unsigned sb = L_SLOT + k * 16384;
        unsigned sa1 = sb + hd * 400 + hh * 128, sa2 = sb + hd * 400 + 256 + hh * 48;
        unsigned rx = sb + g4 * 400 + i16 * 16, ry = sb + (lane >> 3) * 400 + 256 + (lane & 7) * 16;
        unsigned gx = WS_CIN + (g4 * 4 + k) * CIN_WI + kvi * CIN_ROW + i16 * 16;
        unsigned gy = WS_CIN + ((lane >> 3) * 4 + k) * CIN_WI + kvi * CIN_ROW + 256 + (lane & 7) * 16;
        unsigned gm = WS_ML + ((hd * 4 + k) * 128 + kvi) * 16;
        unsigned lsa = L_LS + hd * 4;
        PIN(sa1); PIN(sa2); PIN(rx); PIN(ry); PIN(gx); PIN(gy); PIN(gm); PIN(lsa);
        wg_barrier();
        do_qk();
        if (over()) lds<unsigned>(L_CNT + C_OVF * 4) = 3u;
        bool xch = false;
#pragma unroll 1
        for (;;) {
          if (xch) exchange(false);
          do_p();
          const float lt = add32(rs_add16(ln[0], ln[1]));
          lds<float>(lsa + k * 128) = lt;
          wg_barrier();
          const unsigned ov = lds<unsigned>(L_CNT + C_OVF * 4);
#pragma unroll
          for (int n = 0; n < 4; ++n) pf[n] = lds128(L_P + n * 1024 + l16);
          if (xch || __builtin_amdgcn_readfirstlane(ov) != 3u) break;
          LGKM0();
          xch = true;
        }
        auto dwd = [&](int d) { return d < 32 ? pk2(oacc[d >> 3][2 * (d & 7)], oacc[d >> 3][2 * (d & 7) + 1]) : 0u; };
        auto cvt = [&](int g0, int g1) {
#pragma unroll
          for (int g = g0; g < g1; ++g) lds<v4u>(g < 8 ? sa1 + g * 16 : sa2 + (g - 8) * 16) = v4u{dwd(3 * g), dwd(3 * g + 1), dwd(3 * g + 2), tag};
        };
        auto send_ml = [&]() {
          const LAS float* ls = (const LAS float*)(size_t)lsa;
          const float lt = (ls[0] + ls[32]) + (ls[64] + ls[96]);
          const float mp = (lane & 16) ? -nr1 : -nr0;
          if (lane < 32) st_sc1(rs, gm, v4u{__float_as_uint(mp), tag, __float_as_uint(lt), tag});
        };
        v4u ov[8];
#pragma unroll
        for (int n = 0; n < 16; ++n) {
          SB0();
          oacc[n >> 2] = mfma32(ob[n & 3], pf[n & 3], oacc[n >> 2]);
          SB0();
          if (n + 4 < 16) ldo(n + 4);
          if (n == MLAT) send_ml();
          if (n == 5) cvt(0, 2);
          if (n == 9) cvt(2, 5);
          if (n == 13) cvt(5, 8);
          if (n == 14) {
#pragma unroll
            for (int t = 0; t < 8; ++t) ov[t] = lds128(rx + t * 1600);
          }
        }
        SB0();
        if (MLAT == 16) send_ml();
#pragma unroll
        for (int t = 0; t < 8; ++t) __builtin_amdgcn_raw_buffer_store_b128(ov[t], rs, gx, t * 16 * CIN_WI, 16);
        if (MLAT == 17) send_ml();
        cvt(8, 11);
#pragma unroll
        for (int t = 0; t < 4; ++t) ov[t] = lds128(ry + t * 3200);
#pragma unroll
        for (int t = 0; t < 4; ++t) __builtin_amdgcn_raw_buffer_store_b128(ov[t], rs, gy, t * 32 * CIN_WI, 16);
        if (MLAT == 18) send_ml();
      }
    }
  } else {
    const int h = 4 * xcd + (idx & 3), j = idx >> 2, wi = 4 * h + j;
    const bool ffn = wi < 86;
    const unsigned hh = lane >> 5;
    v4u xa, xb;
    asm volatile("s_cmp_gt_u32 %3, 3\n\ts_cbranch_scc1 1f\n\tglobal_load_dwordx4 %0, %2, %4\n\tglobal_load_dwordx4 %1, %2, %4 offset:512\n1:"
                 : "=&v"(xa), "=&v"(xb) : "v"((2 * w + hh) * 1024 + (lane & 31) * 16), "s"(w), "s"(p.x) : "memory", "scc");
    constexpr int NT = FR4 ? 2 : 4;
    const int hf = FMAP ? (idx & 7) + 8 * (xcd & 3) : h, jf = FMAP ? (idx >> 3) + 2 * (xcd >> 2) : j;
    const int fr = FR4 ? 1 : __builtin_amdgcn_readfirstlane((jf >> 1) ^ 1);
    const int dj = jf & 1, c0 = (FR4 ? (jf >> 1) * 256 : 0) + w * 16 * NT;
    unsigned qb = WS_QABS + dj * 262144 + (i16 * 32 + hf) * 1024 + (c0 + 8 * g4) * 2;
    SB0();
    if (WPF) {
      unsigned d0, d1;
      if (WPF == 1) {
        const unsigned po = (32 * idx + 4 * w + g4) * 8192 + xcd * 1024 + i16 * 64;
        asm volatile("global_load_dword %0, %4, %5\n\tglobal_load_dword %1, %4, %6\n\ts_waitcnt vmcnt(0)" : "=&v"(d0), "=&v"(d1), "+v"(xa), "+v"(xb) : "v"(po), "s"(p.wq), "s"(p.wuk) : "memory");
      } else {
        const char* pa = (const char*)(hh ? p.wuk : p.wq) + (32 * idx + 4 * w + ((lane >> 3) & 3)) * 8192 + xcd * 1024 + (lane & 7) * 128;
        asm volatile("global_load_dword %0, %3, off\n\ts_waitcnt vmcnt(0)" : "=&v"(d0), "+v"(xa), "+v"(xb) : "v"(pa) : "memory");
      }
      if (WPF_DLY) __builtin_amdgcn_s_sleep(WPF_DLY);
    }
    __builtin_amdgcn_s_barrier();
    v4u wukf[8];
    if (fr) {
      const char* src = (const char*)p.wq + hf * 256 + dj * 128 + (size_t)(64 * w) * 8192;
#pragma unroll
      for (int t = 0; t < 8; ++t) glds16s((lane >> 3) * 8192 + (((lane & 7) ^ (((g4 & 1) | ((t & 1) << 1)) << 1)) * 16), src + t * 65536, L_WQ + (64 * w + 8 * t) * 128);
    }
    {
      const unsigned vk = (8 * (i16 >> 2) + (lane & 3)) * 8192 + g4 * 16;
      const char* kb = (const char*)p.wuk + hf * 256 + dj * 128 + (size_t)c0 * 8192;
#pragma unroll
      for (int u = 0; u < NT / 2; ++u)
        asm volatile("s_cmp_eq_u32 %5, 0\n\ts_cbranch_scc1 1f\n\t"
                     "global_load_dwordx4 %0, %4, %6\n\tglobal_load_dwordx4 %1, %4, %6 offset:64\n\t"
                     "global_load_dwordx4 %2, %4, %7\n\tglobal_load_dwordx4 %3, %4, %7 offset:64\n1:"
                     : "=&v"(wukf[4 * u]), "=&v"(wukf[4 * u + 1]), "=&v"(wukf[4 * u + 2]), "=&v"(wukf[4 * u + 3])
                     : "v"(vk), "s"(fr), "s"(kb + 32 * u * 8192), "s"(kb + (32 * u + 4) * 8192) : "memory", "scc");
    }
    {
      const v4u pz = {POISON, POISON, POISON, POISON};
      if (fr && i16 < 8) {
#pragma unroll
        for (int u = 0; u < NT / 2; ++u) st_sc1(rs, qb + u * 64, pz);
      }
      st_sc1(rs, WS_EIN + wi * 8192 + w * 1024 + lane * 16, pz);
    }
    if (w < 4) {
      asm volatile("s_waitcnt vmcnt(%3)\n\ts_cmp_lg_u32 %2, 0\n\ts_cbranch_scc1 1f\n\ts_waitcnt vmcnt(1)\n1:" : "+v"(xa), "+v"(xb) : "s"(fr), "i"(8 + 2 * NT + NT / 2 + 1) : "memory", "scc");
      v2f v[8];
#pragma unroll
      for (int e = 0; e < 4; ++e) { v[e] = v2f{bf2f(xa[e]), bfhi(xa[e])}; v[4 + e] = v2f{bf2f(xb[e]), bfhi(xb[e])}; }
      const unsigned p0 = __builtin_amdgcn_readlane(__float_as_uint(v[0][0]), 0), p1 = __builtin_amdgcn_readlane(__float_as_uint(v[0][0]), 32);
      const float piv = __uint_as_float(hh ? p1 : p0);
      v2f sm = {0.f, 0.f}, ss = {0.f, 0.f};
#pragma unroll
      for (int e = 0; e < 8; ++e) { const v2f d = v[e] - v2f{piv, piv}; sm += d; ss += d * d; }
      float t = rowsum(rs_add16(sm[0] + sm[1], ss[0] + ss[1]));
      auto sw2 = __builtin_amdgcn_permlane16_swap(__float_as_uint(t), __float_as_uint(t), false, false);
      const float md = __uint_as_float(sw2[0]) * (1.f / DM);
      const float var = fmaxf(fmaf(-md, md, __uint_as_float(sw2[1]) * (1.f / DM)), 0.f);
      const float mb = rbf(piv + md), vb = rbf(var);
      const float rden = __builtin_amdgcn_rcpf(rbf(__builtin_amdgcn_sqrtf(rbf(vb + 1e-5f))));
      v4u o4[2];
#pragma unroll
      for (int e = 0; e < 8; ++e) {
        const v2f d = v[e] - v2f{mb, mb};
        const unsigned u = pk2(d[0], d[1]);
        const v2f o = v2f{bf2f(u), bfhi(u)} * v2f{rden, rden};
        o4[e >> 2][e & 3] = pk2(o[0], o[1]);
      }
      const unsigned xo = L_XN + (2 * w + hh) * 1040 + (lane & 31) * 16;
      lds<v4u>(xo) = o4[0];
      lds<v4u>(xo + 512) = o4[1];
      if (tid < C_N) lds<unsigned>(L_CNT + tid * 4) = 0u;
    }
    const unsigned tag = GET_TAG();
    unsigned qa = L_QH + (lane & 7) * 144 + g4 * 16;
    {
      unsigned ta = L_WQ + (8 * g4 + (i16 >> 2)) * 128 + (((2 * w + ((lane & 3) >> 1)) ^ (((i16 >> 3) | ((g4 & 1) << 1)) << 1)) * 16) + 8 * (lane & 1);
      unsigned xq = L_XN + (lane & 7) * 1040 + g4 * 16;
      unsigned qw = L_QH + (lane & 7) * 144 + (16 * w + 4 * g4) * 2;
      PIN(ta); PIN(xq); PIN(qw); PIN(qa); PIN(qb);
      VMWAIT(2 * NT + NT / 2 + 1);
      wg_barrier();
      if (fr && w < 4) {
        f32x4 c0a = {0.f, 0.f, 0.f, 0.f}, c1a = c0a;
#pragma unroll
        for (int ks = 0; ks < 16; ks += 2) {
          const v4u x0 = lds128(xq + ks * 64), x1 = lds128(xq + ks * 64 + 64);
          const v2u a0 = trread(ta + ks * 4096), a1 = trread(ta + ks * 4096 + 512), b0 = trread(ta + ks * 4096 + 4096), b1 = trread(ta + ks * 4096 + 4608);
          c0a = mfma16(v4u{a0[0], a0[1], a1[0], a1[1]}, x0, c0a);
          c1a = mfma16(v4u{b0[0], b0[1], b1[0], b1[1]}, x1, c1a);
        }
        c0a += c1a;
        lds<v2u>(qw) = v2u{pk2(c0a[0], c0a[1]), pk2(c0a[2], c0a[3])};
      }
    }
    wg_barrier();
    {
      const v4u q0 = lds128(qa), q1 = lds128(qa + 64);
      if (NT == 4) asm volatile("s_waitcnt vmcnt(0)" : W8(wukf, 0)::"memory");
      else asm volatile("s_waitcnt vmcnt(0)" : "+v"(wukf[0]), "+v"(wukf[1]), "+v"(wukf[2]), "+v"(wukf[3])::"memory");
      const bool last = cnt_add_rtn(C_QST) == 7u;
      if (FLAG_FIRST && last) send_flags(rs, wi, lane, tag);
      if (fr) {
        const float sc = 0.08838834764831845f * LOG2E;
#pragma unroll
        for (int u = 0; u < NT / 2; ++u) {
          f32x4 d0 = mfma16(wukf[4 * u], q0, f32x4{0.f, 0.f, 0.f, 0.f}), d1 = mfma16(wukf[4 * u + 2], q0, f32x4{0.f, 0.f, 0.f, 0.f});
          d0 = mfma16(wukf[4 * u + 1], q1, d0);
          d1 = mfma16(wukf[4 * u + 3], q1, d1);
          d0 *= sc; d1 *= sc;
          if (i16 < 8) st_sc1(rs, qb + u * 64, v4u{san(pk2(d0[0], d0[1])), pk2(d0[2], d0[3]), pk2(d1[0], d1[1]), pk2(d1[2], d1[3])});
        }
      }
      if (!FLAG_FIRST && last) send_flags(rs, wi, lane, tag);
    }
    if (w == 1) {
      v2u f0, f1, f2;
      wait_until(t0, POLL_START);
      poll_flags(p.ws, WS_FLAG + (128 + wi) * 512 + lane * 8, tag, f0, f1, f2);
      if (GO_DLY) wait_until(now32(), GO_DLY);
      cnt_add(C_GO);
      asm volatile("s_waitcnt vmcnt(0)" : "+v"(f0), "+v"(f1), "+v"(f2)::"memory");
    }
    {
      const int u = idx * 8 + w;
      const char* pa = hh ? (const char*)p.wo + xcd * 524288 + u * 4096 + (lane & 31) * 128
                          : (const char*)p.wuv + (4 * u + (lane >> 3)) * 8192 + xcd * 1024 + (lane & 7) * 128;
      const int q5 = lane / 5, r5 = lane - 5 * q5;
      const char* pb = lane < 40 ? (const char*)((q5 & 1) ? p.wu : p.wg) + (4 * u + (q5 >> 1)) * (FF * 2) + min(xcd * 512 + min(r5 * 128, 508), FF * 2 - 4)
                                 : (const char*)p.wd + min(xcd * 262144 + u * 2048 + ((lane - 40) & 15) * 128, FF * 1024 - 128);
      cnt_wait<2>(C_GO, 1);
      wait_until(t0, T_F);
      if (BPF) {
        unsigned d0, d1;
        asm volatile("global_load_dword %0, %2, off\n\tglobal_load_dword %1, %3, off\n\ts_waitcnt vmcnt(0)" : "=&v"(d0), "=&v"(d1) : "v"(pb), "v"(pa) : "memory");
        if (BPF_DLY) __builtin_amdgcn_s_sleep(BPF_DLY);
      }
    }
    v4u wof[4][8];
    f32x4 oc[8];
    if (w >= 4) {
      const int hk = w - 4;
      wait_until(t0, T_O);
      v4u wor[4][8], wdr[8], wdf[8];
      {
        const unsigned vo = 8 * g4 * 1024 + i16 * 16, vd = 8 * (g4 & 1) * 1024 + i16 * 16;
        const char* so = (const char*)p.wo + (size_t)(h * 128) * 1024 + hk * 256;
        const char* sd = (const char*)p.wd + (size_t)(ffn ? 16 * wi : 0) * 1024 + hk * 256;
#pragma unroll
        for (int n = 0; n < 40; ++n) {
          if (n < 8) asm volatile("global_load_dwordx4 %0, %1, %2 nt" : "=v"(wdr[n]) : "v"(vd), "s"(sd + n * 1024) : "memory");
          else asm volatile("global_load_dwordx4 %0, %1, %2" : "=v"(wor[(n - 8) >> 3][n & 7]) : "v"(vo), "s"(so + (32 * ((((n - 8) >> 3) + j) & 3) + (n & 7)) * 1024) : "memory");
          if (n >= NFW - 1 && n < 39) VMWAIT(NFW - 1);
        }
      }
      asm volatile("s_waitcnt vmcnt(32)" : W8(wdr, 0)::"memory");
      transpose8(wdr, wdf);
      asm volatile("s_waitcnt vmcnt(24)" : W8(wor[0], 0)::"memory");
      transpose8(wor[0], wof[0]);
      asm volatile("s_waitcnt vmcnt(16)" : W8(wor[1], 0)::"memory");
      transpose8(wor[1], wof[1]);
      asm volatile("s_waitcnt vmcnt(8)" : W8(wor[2], 0)::"memory");
      transpose8(wor[2], wof[2]);
#pragma unroll
      for (int jj = 0; jj < 8; ++jj) oc[jj] = f32x4{0.f, 0.f, 0.f, 0.f};
      if (ffn) {
        cnt_wait<1>(C_FFN, 1);
        const v4u a = lds128(L_HF + ((lane & 7) ^ ((lane & 8) >> 2)) * 80 + g4 * 16);
#pragma unroll
        for (int jj = 0; jj < 8; ++jj) oc[jj] = mfma16(a, wdf[jj], oc[jj]);
      }
      asm volatile("s_waitcnt vmcnt(0)" : W8(wor[3], 0)::"memory");
      transpose8(wor[3], wof[3]);
#pragma unroll
      for (int u = 0; u < 4; ++u) asm volatile("" : W8(wof[u], 0));
      asm volatile("" : W8(oc, 0));
    } else {
      if (ffn) {
        const char* src = (const char*)((lane & 2) ? p.wu : p.wg) + (size_t)(128 * w + (lane >> 2)) * (FF * 2) + wi * 32 + (lane & 1) * 16;
#pragma unroll
        for (int t = 0; t < 8; ++t) glds16<0>(src + (size_t)t * 16 * (FF * 2), L_GU + (128 * w + 16 * t) * 64);
      }
#pragma unroll
      for (int t = 0; t < 8; ++t) {
        const unsigned row = 32 * w + 4 * t + g4;
        glds16<1>((const char*)p.wuv + (size_t)(128 * j + row) * 8192 + h * 256 + ((i16 ^ fswz(row)) * 16), L_WUV + (32 * w + 4 * t) * 256);
      }
      if (ffn) {
        VMWAIT(8);
        f32x4 ag = {0.f, 0.f, 0.f, 0.f}, au = {0.f, 0.f, 0.f, 0.f};
        const unsigned ro = L_GU + (128 * w + 8 * g4 + (i16 >> 2)) * 64 + (lane & 3) * 8;
#pragma unroll
        for (int ks = 0; ks < 4; ++ks) {
          const v4u a = lds128(L_XN + (lane & 7) * 1040 + (128 * w + 32 * ks + 8 * g4) * 2);
          const v2u b0 = trread(ro + ks * 2048), b1 = trread(ro + ks * 2048 + 256);
          const v2u c0 = trread(ro + ks * 2048 + 32), c1 = trread(ro + ks * 2048 + 288);
          ag = mfma16(a, v4u{b0[0], b0[1], b1[0], b1[1]}, ag);
          au = mfma16(a, v4u{c0[0], c0[1], c1[0], c1[1]}, au);
        }
        if (lane < 32) {
          lds<f32x4>(L_FP + w * 1024 + lane * 16) = ag;
          lds<f32x4>(L_FP + w * 1024 + 512 + lane * 16) = au;
        }
        if (cnt_add_rtn(C_GU) == 3u) {
          const unsigned fa = L_FP + (lane & 31) * 16 + hh * 8;
          const v2f gs = (lds<v2f>(fa) + lds<v2f>(fa + 1024)) + (lds<v2f>(fa + 2048) + lds<v2f>(fa + 3072));
          const v2f us = (lds<v2f>(fa + 512) + lds<v2f>(fa + 1536)) + (lds<v2f>(fa + 2560) + lds<v2f>(fa + 3584));
#pragma unroll
          for (int e = 0; e < 2; ++e) {
            const float gg = rbf(gs[e]), uu = rbf(us[e]);
            const float inner = 0.7978845608028654f * (gg + 0.044715f * gg * gg * gg);
            const float th = 1.f - 2.f / (__expf(2.f * inner) + 1.f);
            const float ge = rbf(0.5f * gg * (1.f + th));
            lds<u16>(L_HF + (4 * (g4 & 1) + 2 * hh + e) * 80 + i16 * 2) = (u16)(pk2(ge * uu, 0.f) & 0xFFFFu);
          }
          if (lane < 16) lds<v4u>(L_HF + (lane >> 1) * 80 + 32 + (lane & 1) * 16) = v4u{0u, 0u, 0u, 0u};
          cnt_add(C_FFN);
        }
      }
      VMWAIT(0);
      cnt_add(C_UV);
    }
    v4u uvf[4];
    cnt_wait<2>(C_UV, 4);
#pragma unroll
    for (int ks = 0; ks < 4; ++ks) {
      const unsigned ra = 32 * (2 * (ks & 1) + (g4 >> 1)) + 16 * (ks >> 1) + 8 * (g4 & 1) + (i16 >> 2), rb = ra + 4, ch = 2 * w + ((lane & 3) >> 1);
      const v2u b0 = trread(L_WUV + ra * 256 + ((ch ^ fswz(ra)) * 16) + 8 * (lane & 1));
      const v2u b1 = trread(L_WUV + rb * 256 + ((ch ^ fswz(rb)) * 16) + 8 * (lane & 1));
      uvf[ks] = v4u{b0[0], b0[1], b1[0], b1[1]};
      PIN(uvf[ks]);
    }
    unsigned cx = WS_CIN + wi * CIN_WI + (w * 16 + g4) * CIN_ROW + i16 * 16;
    unsigned cy = WS_CIN + wi * CIN_WI + (w * 16 + g4) * CIN_ROW + 256 + min(lane & 7, 5) * 16;
    unsigned clx = L_CL + w * 272 + (g4 < 3 ? ((i16 >> 3) * 32 + 3 * (i16 & 7) + g4) * 4 : 256);
    unsigned cly;
    {
      const unsigned ph = i16 >= 3, dd = 3 * (8 + i16 - 3 * ph) + g4;
      cly = L_CL + w * 272 + (g4 < 3 && i16 < 6 && dd < 32 ? (ph * 32 + dd) * 4 : 260);
    }
    unsigned cla = L_CL + (lane & 7) * 272 + g4 * 16;
    unsigned cxw = L_CTX + (lane & 7) * 272 + (16 * w + 4 * g4) * 2;
    unsigned cxa[4];
#pragma unroll
    for (int u = 0; u < 4; ++u) { cxa[u] = L_CTX + ((lane & 7) ^ ((lane & 8) >> 2)) * 272 + (32 * ((u + j) & 3) + 8 * g4) * 2; PIN(cxa[u]); }
    unsigned eb = w >= 4 ? WS_EIN + ((4 * (g4 & 1) + 2 * hh) * 16 + 4 * (w - 4) + (i16 >> 2)) * 8192 + wi * 64 + (i16 & 3) * 16 : WS_EIN + wi * 8192 + lane * 16;
    unsigned f2a = WS_FLAG2 + (64 * (w & 1) + lane) * 512 + wi * 4;
    PIN(f2a);
    unsigned msk[3];
#pragma unroll
    for (int q = 0; q < 3; ++q) { msk[q] = (i16 >> 1) == 2 * q ? ((i16 & 1) ? 0xFFFF0000u : 0xFFFFu) : 0u; PIN(msk[q]); }
    PIN(cx); PIN(cy); PIN(clx); PIN(cly); PIN(cla); PIN(cxw); PIN(eb);
    {
      v2u ml;
      v4u dv[8];
      const unsigned ma = WS_ML + (wi * 128 + w * 16) * 16 + (lane & 31) * 8;
      const v2u zz = {0u, 0u};
      unsigned pend = 15u;
#pragma unroll 1
      for (unsigned spin = 0; spin < SPIN_CAP; ++spin) {
        asm volatile("global_atomic_add_x2 %0, %1, %2, %3 sc0 sc1\n\ts_waitcnt vmcnt(0)" : "=&v"(ml) : "v"(ma), "v"(zz), "s"(p.ws) : "memory");
        const unsigned okm = (unsigned)__ballot(ml[1] == tag);
#pragma unroll
        for (int t = 0; t < 4; ++t) {
          if ((pend >> t & 1u) && ((okm >> (8 * t)) & 255u) == 255u) {
            dv[2 * t] = ld_sc1(rs, cx + t * 4 * CIN_ROW);
            dv[2 * t + 1] = ld_sc1(rs, cy + t * 4 * CIN_ROW);
            pend &= ~(1u << t);
          }
        }
        if (pend == 0) break;
      }
      v4u aw[4];
      {
        const float o = xor1(__uint_as_float(ml[0]));
        const float ms = (lane & 1) ? o : __uint_as_float(ml[0]), lsv = (lane & 1) ? __uint_as_float(ml[0]) : o;
        float M = rowmax(ms);
        M = rs_max16(M, M);
        const float ee = ex2(ms - M);
        const float wsv = 2.f * ee / add16(rowsum(ee * lsv));
        const unsigned wb = pk2(wsv, wsv);
#pragma unroll
        for (int t = 0; t < 4; ++t) {
          const unsigned u = __shfl(wb, 8 * t + 2 * g4);
          aw[t] = v4u{u & msk[0], u & msk[1], u & msk[2], 0u};
          PIN(aw[t]);
        }
      }
#pragma unroll 1
      for (unsigned spin = 0; spin < SPIN_CAP; ++spin) {
        bool ok = true;
        bool bad[8];
#pragma unroll
        for (int t = 0; t < 8; ++t) { bad[t] = __any(dv[t][3] != tag); ok = ok && !bad[t]; }
        if (ok) break;
        CFENCE();
#pragma unroll
        for (int t = 0; t < 8; ++t)
          if (bad[t]) dv[t] = ld_sc1(rs, ((t & 1) ? cy : cx) + (t >> 1) * 4 * CIN_ROW);
      }
      f32x4 dx = {0.f, 0.f, 0.f, 0.f}, dy = dx;
#pragma unroll
      for (int t = 0; t < 4; ++t) { dx = mfma16(aw[t], dv[2 * t], dx); dy = mfma16(aw[t], dv[2 * t + 1], dy); }
      lds<unsigned>(clx) = pk2(dx[0], dx[1]);
      lds<unsigned>(cly) = pk2(dy[0], dy[1]);
    }
    wg_barrier();
    if (F2AT == 0 && (w == 2 || w == 3)) st_sc1_32(rs, f2a, tag);
    {
      v4u a[4];
#pragma unroll
      for (int ks = 0; ks < 4; ++ks) a[ks] = lds128(cla + ks * 64);
      SB0();
      f32x4 c0 = mfma16(uvf[0], a[0], f32x4{0.f, 0.f, 0.f, 0.f}), c1 = mfma16(uvf[1], a[1], f32x4{0.f, 0.f, 0.f, 0.f});
      c0 = mfma16(uvf[2], a[2], c0);
      c1 = mfma16(uvf[3], a[3], c1);
      c0 += c1;
      lds<v2u>(cxw) = v2u{pk2(c0[0], c0[1]), pk2(c0[2], c0[3])};
    }
    wg_barrier();
    if (F2AT == 1 && (w == 2 || w == 3)) st_sc1_32(rs, f2a, tag);
    if (w >= 4) {
      v4u a[4];
#pragma unroll
      for (int u = 0; u < 4; ++u) a[u] = lds128(cxa[u]);
      SB0();
#pragma unroll
      for (int u = 0; u < 4; ++u)
#pragma unroll
        for (int jj = 0; jj < 8; ++jj) oc[jj] = mfma16(a[u], wof[u][jj], oc[jj]);
#pragma unroll
      for (int e = 0; e < 2; ++e)
        __builtin_amdgcn_raw_buffer_store_b128(v4u{san(pk2(oc[0][e], oc[1][e])), san(pk2(oc[2][e], oc[3][e])), san(pk2(oc[4][e], oc[5][e])), san(pk2(oc[6][e], oc[7][e]))},
                                               rs, eb, e * 16 * 8192, 16);
    } else if (w == 0) {
      const unsigned oo = wi * 64 + (i16 & 3) * 16 + g4 * 4;
      const unsigned xf = *(const GAS unsigned*)((const char*)p.x + oo);
      const unsigned one = (i16 & 2) ? 0u : ((i16 & 1) ? 0x3F800000u : 0x3F80u);
      v4u sel;
#pragma unroll
      for (int q = 0; q < 4; ++q) sel[q] = (i16 >> 2) == q ? one : 0u;
      v2u f0, f1, f2;
      poll_flags(p.ws, WS_FLAG2 + wi * 512 + lane * 8, tag, f0, f1, f2);
      if (E_DLY) __builtin_amdgcn_s_sleep(E_DLY);
      float r0, r1;
#pragma unroll 1
      for (unsigned spin = 0; spin < SPIN_CAP; ++spin) {
        v4u ev[8];
#pragma unroll
        for (int t = 0; t < 8; ++t) ev[t] = ld_sc1(rs, eb + t * 1024);
        f32x4 d0 = {0.f, 0.f, 0.f, 0.f}, d1 = d0;
#pragma unroll
        for (int t = 0; t < 4; ++t) { d0 = mfma16(sel, ev[2 * t], d0); d1 = mfma16(sel, ev[2 * t + 1], d1); }
        r0 = d0[0] + d1[0]; r1 = d0[1] + d1[1];
        r0 += ror16<8>(r0); r1 += ror16<8>(r1);
        r0 += ror16<4>(r0); r1 += ror16<4>(r1);
        if (!__any(r0 != r0)) break;
        unsigned mxv = 0;
#pragma unroll
        for (int t = 0; t < 8; ++t) mxv = max(mxv, max(max(ev[t][0], ev[t][1]), max(ev[t][2], ev[t][3])));
        if (__all(mxv != POISON)) break;
        CFENCE();
      }
      if (i16 < 4) *(GAS unsigned*)((char*)p.out + oo) = pk2(bf2f(xf) + r0, bfhi(xf) + r1);
      asm volatile("s_waitcnt vmcnt(0)" : "+v"(f0), "+v"(f1), "+v"(f2)::"memory");
    } else if (w == 2) {
      if (wi == 0 && lane == 0) st_sc1_32(rs, WS_GEN, gen1);
    }
  }
}

extern "C" void kernel_launch(void* const* d_in, const int* in_sizes, int n_in, void* d_out, int out_size, void* d_ws,
                              size_t ws_size, hipStream_t stream) {
  Params p;
  p.x = (const u16*)d_in[0];
  p.ckv = (const u16*)d_in[1];
  p.wq = (const u16*)d_in[2];
  p.wuk = (const u16*)d_in[3];
  p.wuv = (const u16*)d_in[4];
  p.wo = (const u16*)d_in[5];
  p.wg = (const u16*)d_in[7];
  p.wu = (const u16*)d_in[8];
  p.wd = (const u16*)d_in[9];
  p.out = (u16*)d_out;
  p.ws = (unsigned char*)d_ws;
  (void)hipFuncSetAttribute((const void*)fused_kernel, hipFuncAttributeMaxDynamicSharedMemorySize, LDS_BYTES);
  fused_kernel<<<256, 512, LDS_BYTES, stream>>>(p);
}
```

```cpp
#include <hip/hip_runtime.h>
#include <stdint.h>

typedef unsigned short u16;
typedef unsigned long long u64;
typedef unsigned v4u __attribute__((ext_vector_type(4)));
typedef unsigned v2u __attribute__((ext_vector_type(2)));
typedef float f32x4 __attribute__((ext_vector_type(4)));
typedef float f32x16 __attribute__((ext_vector_type(16)));
typedef float v2f __attribute__((ext_vector_type(2)));
typedef short bf16x8 __attribute__((ext_vector_type(8)));
typedef short v4s __attribute__((ext_vector_type(4)));
typedef __bf16 v2bf __attribute__((ext_vector_type(2)));
#define GAS __attribute__((address_space(1)))
#define LAS __attribute__((address_space(3)))
#define DI __device__ __forceinline__

#define NF 12
#define R2AT (47 + NF)
#define POLL_START 250
#define POLL_GAP 4
#define Q_DLY 0
#define KV_DLY 50
#define E_DLY 4
#define T_F 800
#define T_UV 0
#define T_O 450
#define MLAT 17
#define FR4 1
#define PAUSE 1
#define QPF 0
#define WPF 2
#define BPF 0
#define BPF_DLY 0
#define WPF_DLY 0
#define FMAP 0
#define OD 6
#define V3 4
#define QPRIO 1
#define PK 8
#define WM_DLY 110
#define FPF 0
#define FLAG_AT 1
#define FL_DLY 0
#define PZ_DLY 0
#define B0W 2
#define NF2 16
#define PMIN 32
#define VD 0
#define MIDSIG 1
#define V2AT 0
#define CKAT 10
#define PPRIO 0
#define SPAT 13
#define MIX 0
#define PROG 1
#define RES 1
#define XAT 2
#define PUTAT 3
#define FLAG_FIRST 0
#define F2AT 1
#define GO_DLY 100
#define C_DLY 0
#define KPF 0
#define KPF_AT 0
#define KPF_S 1
#define FX_SLP 8
#define OSAT 11
#define PZE 0
#define PZT 0
#define PZ_AT 150
#define K1AT 1
#define CSW 1
#define FLW 0
#define FLW2 0
#define BMAP 0
#define EMAP 0
#define NFW 8

constexpr int DM = 512, FF = 1376;
constexpr unsigned WS_GEN = 0;
constexpr unsigned WS_FLAG = 4096;
constexpr unsigned WS_FLAG2 = WS_FLAG + 262144;
constexpr unsigned WS_QABS = WS_FLAG2 + 131072;
constexpr unsigned WS_ML = WS_QABS + 524288;
constexpr unsigned WS_EIN = WS_ML + 262144;
constexpr unsigned WS_CIN = WS_EIN + 1048576;
constexpr unsigned CIN_ROW = 256, CIN_WI = 128 * CIN_ROW;
constexpr unsigned WS_FIN = WS_CIN + 128 * CIN_WI;
constexpr unsigned WS_END = WS_FIN + 88 * 8192;
constexpr unsigned L_Q = 0;
constexpr unsigned L_P = L_Q;
constexpr unsigned L_LS = L_Q + 24576;
constexpr unsigned L_AL = L_Q + 25600;
constexpr unsigned L_SLOT = 32768;
constexpr unsigned L_XN = 0;
constexpr unsigned L_QH = 8448;
constexpr unsigned L_CL = L_QH + 2176;
constexpr unsigned L_CTX = L_CL + 2176;
constexpr unsigned L_HF = L_CTX + 2176;
constexpr unsigned L_FP = L_HF + 640;
constexpr unsigned L_WQ = 32768;
constexpr unsigned L_GU = 65536;
constexpr unsigned L_WUV = 98304;
constexpr unsigned L_CNT = 32512;
static_assert(L_CNT == 32512, "");
constexpr unsigned LDS_BYTES = 163840;
enum { C_P = 0, C_OVF = 1, C_ST = 2, C_FL = 3, C_MX = 5, C_PVD, C_XS, C_MASK = 8, C_N = 16 };
enum { C_QST = 0, C_GU, C_FFN, C_UV, C_GO, C_SENT };

constexpr unsigned ANY = 0x80u;
constexpr unsigned POISON = 0xFFFFFFFFu;
constexpr unsigned SPIN_CAP = 1u << 20;

struct Params {
  const u16 *x, *ckv, *wq, *wuk, *wuv, *wo, *wg, *wu, *wd;
  u16* out;
  unsigned char* ws;
};

DI float bf2f(unsigned v) { return __uint_as_float(v << 16); }
DI float bfhi(unsigned v) { return __uint_as_float(v & 0xFFFF0000u); }
DI unsigned pk2(float a, float b) {
  v2f t = {a, b};
  return __builtin_bit_cast(unsigned, __builtin_convertvector(t, v2bf));
}
DI float rbf(float a) { return bf2f(pk2(a, 0.f) & 0xFFFFu); }
DI unsigned san(unsigned v) { return min(v, 0xFFFFFFFEu); }
DI f32x4 mfma16(v4u a, v4u b, f32x4 c) {
  return __builtin_amdgcn_mfma_f32_16x16x32_bf16(__builtin_bit_cast(bf16x8, a), __builtin_bit_cast(bf16x8, b), c, 0, 0, 0);
}
DI f32x16 mfma32(v4u a, v4u b, f32x16 c) {
  return __builtin_amdgcn_mfma_f32_32x32x16_bf16(__builtin_bit_cast(bf16x8, a), __builtin_bit_cast(bf16x8, b), c, 0, 0, 0);
}
DI v4u lds128(unsigned a) { return *(LAS v4u*)(size_t)a; }
DI v2u trread(unsigned a) { return __builtin_bit_cast(v2u, __builtin_amdgcn_ds_read_tr16_b64_v4i16((LAS v4s*)(size_t)a)); }
template <class T> DI LAS T& lds(unsigned a) { return *(LAS T*)(size_t)a; }
template <int POL> DI void glds16(const void* gsrc, unsigned lds_dst) {
  unsigned keep;
  if (POL == 1) asm volatile("s_mov_b32 %0, m0\n\ts_mov_b32 m0, %2\n\ts_nop 0\n\tglobal_load_lds_dwordx4 %1, off nt\n\ts_mov_b32 m0, %0" : "=&s"(keep) : "v"(gsrc), "s"(lds_dst) : "memory");
  else if (POL == 2) asm volatile("s_mov_b32 %0, m0\n\ts_mov_b32 m0, %2\n\ts_nop 0\n\tglobal_load_lds_dwordx4 %1, off sc1\n\ts_mov_b32 m0, %0" : "=&s"(keep) : "v"(gsrc), "s"(lds_dst) : "memory");
  else asm volatile("s_mov_b32 %0, m0\n\ts_mov_b32 m0, %2\n\ts_nop 0\n\tglobal_load_lds_dwordx4 %1, off\n\ts_mov_b32 m0, %0" : "=&s"(keep) : "v"(gsrc), "s"(lds_dst) : "memory");
}
DI void glds16s(unsigned voff, const void* sbase, unsigned lds_dst) {
  unsigned keep;
  asm volatile("s_mov_b32 %0, m0\n\ts_mov_b32 m0, %3\n\ts_nop 0\n\tglobal_load_lds_dwordx4 %1, %2 nt\n\ts_mov_b32 m0, %0" : "=&s"(keep) : "v"(voff), "s"(sbase), "s"(lds_dst) : "memory");
}
DI void gld16s(v4u& d, unsigned voff, const void* sbase) {
  asm volatile("global_load_dwordx4 %0, %1, %2 nt" : "=v"(d) : "v"(voff), "s"(sbase) : "memory");
}
#define VMWAIT(n) asm volatile("s_waitcnt vmcnt(%0)" ::"i"(n) : "memory")
#define LGKM0() asm volatile("s_waitcnt lgkmcnt(0)" ::: "memory")
#define CFENCE() asm volatile("" ::: "memory")
#define SB0() __builtin_amdgcn_sched_barrier(0)
#define W8(a, o) "+v"(a[o]), "+v"(a[o + 1]), "+v"(a[o + 2]), "+v"(a[o + 3]), "+v"(a[o + 4]), "+v"(a[o + 5]), "+v"(a[o + 6]), "+v"(a[o + 7])
#define WARMUP 1
#define MK(n) asm volatile("7:\n\ts_cbranch_execz .Lwm_ret\n\t.subsection " #n "\n\ts_branch 7b\n\t.subsection 0")
#define MK_TABLE() asm volatile(".subsection 1\n.Lwm_ret:\n\ts_setpc_b64 s[96:97]\n.Lwm_w:\n\t.subsection 3\n.Lwm_q:\n\t.subsection 5\n.Lwm_p:\n\t" \
                                ".subsection 7\n.Lwm_s:\n\t.subsection 9\n.Lwm_e:\n\t.fill 128, 4, 0xBF810000\n\t.subsection 0")
#define WARM(A, B, first, step) do { u64 keep_; unsigned off_ = (first) * 4u; \
  asm volatile("s_mov_b64 %0, exec\n\ts_mov_b64 exec, 0\n\ts_getpc_b64 s[96:97]\n8:\n\t" \
               "s_cmp_ge_u32 %1, .Lwm_" #B "-.Lwm_" #A "\n\ts_cbranch_scc1 9f\n\t" \
               "s_add_u32 s94, %1, .Lwm_" #A "-8b\n\ts_add_u32 s94, s94, s96\n\ts_addc_u32 s95, s97, 0\n\t" \
               "s_add_u32 %1, %1, %2\n\ts_setpc_b64 s[94:95]\n9:\n\ts_mov_b64 exec, %0" \
               : "=&s"(keep_), "+s"(off_) : "i"((step) * 4) : "s94", "s95", "s96", "s97", "scc"); } while (0)

#define MW() MK(2)
#define MQ() MK(4)
#define MP() MK(6)
#define MS() MK(8)

DI void wg_barrier() {
  LGKM0();
  __builtin_amdgcn_s_barrier();
  CFENCE();
}
DI void cnt_addv(unsigned c, unsigned v) {
  asm volatile("s_mov_b64 exec, 1\n\tds_add_u32 %0, %1 offset:%2\n\ts_mov_b64 exec, -1" ::"v"(0u), "v"(v), "i"(L_CNT + c * 4u) : "memory");
}
DI void cnt_add(unsigned c) { cnt_addv(c, 1u); }
DI void cnt_add2(unsigned c, unsigned v0, unsigned v1) {
  asm volatile("s_mov_b64 exec, 1\n\tds_add_u64 %0, %1 offset:%2\n\ts_mov_b64 exec, -1" ::"v"(0u), "v"(v2u{v0, v1}), "i"(L_CNT + c * 4u) : "memory");
}
#define AI __attribute__((always_inline))
#define RARE(c) __builtin_expect(!!(c), 0)
DI unsigned vm_pending() {
  unsigned v;
  asm volatile("s_getreg_b32 %0, hwreg(HW_REG_IB_STS)" : "=s"(v));
  return (v & 15u) | ((v >> 18) & 48u);
}
#define STR_(x) #x
#define STR(x) STR_(x)
#define VW1(n) "s_waitcnt vmcnt(" #n ")\n\ts_branch 2f\n\t"
#define MKR "7:\n\ts_cbranch_execz .Lwm_ret\n\t.subsection 8\n\ts_branch 7b\n\t.subsection 10\n\t"
#define MKB "7:\n\ts_cbranch_execz .Lwm_ret\n\t.subsection 6\n\ts_branch 7b\n\t.subsection 11\n\t"
#define L_CNT_ 32512
#define KV_RUN_BODY() asm volatile(".subsection 10\n" \
  MKR ".Lkv_chk:\n\t" \
  "s_mov_b32 s67, 1\n\ts_mov_b32 s91, m0\n\ts_mov_b32 s90, 0xffffff\n\ts_branch 12f\n" \
  ".Lkv_runp:\n\t" \
  "s_mov_b32 s67, 1\n\ts_branch 11f\n" \
  ".Lkv_run:\n\t" \
  "s_mov_b32 s67, 2\n" \
  "11:\n\t" \
  "s_mov_b32 s91, m0\n\ts_mov_b32 s90, 0xffffff\n" \
  "3:\n\t" \
  "v_mov_b32 v252, 0\n\t" \
  "ds_read_b128 v[252:255], v252 offset:%0\n\t" \
  "s_cmp_lg_u32 s67, 1\n\ts_cbranch_scc1 12f\n\t" \
  "v_add_u32 v249, s74, v250\n\t" \
  "s_bitcmp1_b32 s74, 12\n\ts_cbranch_scc1 14f\n\t" \
  "ds_read_b128 v[232:235], v249\n\tds_read_b128 v[236:239], v249 offset:1024\n\tds_read_b128 v[240:243], v249 offset:2048\n\tds_read_b128 v[244:247], v249 offset:3072\n\t" \
  "s_branch 12f\n" \
  "14:\n\t" \
  "ds_read_b128 v[216:219], v249\n\tds_read_b128 v[220:223], v249 offset:1024\n\tds_read_b128 v[224:227], v249 offset:2048\n\tds_read_b128 v[228:231], v249 offset:3072\n" \
  "12:\n\t" \
  "s_cmp_eq_u32 s77, 4\n\ts_cbranch_scc1 6f\n\t" \
  "s_getreg_b32 s87, hwreg(HW_REG_IB_STS)\n\t" \
  "s_lshl_b32 s89, s77, 4\n\ts_add_u32 s89, s89, 16\n\t" \
  "s_lshr_b32 s88, s87, 18\n\ts_and_b32 s88, s88, 48\n\ts_and_b32 s87, s87, 15\n\ts_or_b32 s87, s87, s88\n\t" \
  "s_add_u32 s89, s89, s87\n\ts_cmp_lt_u32 s76, s89\n\ts_cbranch_scc1 4f\n\t" \
  "s_min_u32 s88, s87, 23\n\ts_lshl_b32 s88, s88, 3\n\t" \
  "s_getpc_b64 s[94:95]\n0:\n\ts_add_u32 s88, s88, 1f-0b\n\ts_add_u32 s94, s94, s88\n\ts_addc_u32 s95, s95, 0\n\ts_setpc_b64 s[94:95]\n" \
  MKR "1:\n\t" \
  VW1(0) VW1(1) VW1(2) VW1(3) VW1(4) VW1(5) VW1(6) VW1(7) VW1(8) VW1(9) VW1(10) VW1(11) VW1(12) VW1(13) VW1(14) VW1(15) \
  VW1(16) VW1(17) VW1(18) VW1(19) VW1(20) VW1(21) VW1(22) VW1(23) \
  MKR "2:\n\t" \
  "s_lshl_b32 s88, s77, 2\n\ts_add_u32 s88, s88, s83\n\ts_lshl_b32 s88, 1, s88\n\t" \
  "v_mov_b32 v248, 0\n\tv_mov_b32 v249, s88\n\ts_mov_b64 exec, 1\n\tds_add_u32 v248, v249 offset:%1\n\ts_mov_b64 exec, -1\n\t" \
  "s_add_u32 s77, s77, 1\n" \
  "4:\n\t" \
    \
  "s_lshr_b32 s89, s86, s83\n\ts_and_b32 s89, s89, 0x11000000\n\ts_and_b32 s88, s86, 0x440000\n\ts_or_b32 s89, s89, s88\n\t" \
  "s_cmp_eq_u32 s89, 0x11440000\n\ts_cselect_b32 s88, 64, 48\n\t" \
  "s_and_b32 s89, s89, 0x1040000\n\ts_cmp_eq_u32 s89, 0x1040000\n\ts_cselect_b32 s88, s88, 32\n\t" \
  "s_and_b32 s89, s75, %2 | 0x100\n\ts_cmp_eq_u32 s89, 0x100\n\ts_cselect_b32 s89, " STR(PMIN) ", 64\n\ts_min_u32 s88, s88, s89\n\t" \
  "s_and_b32 s89, s75, %2\n\ts_cselect_b32 s89, " STR(NF2) ", " STR(NF) "\n\t" \
  "s_add_u32 s89, s89, s76\n\ts_sub_u32 s89, s89, s87\n\ts_min_u32 s88, s88, s89\n\t" \
  "s_cmp_ge_i32 s76, s88\n\ts_cbranch_scc1 6f\n" \
  "5:\n\t" \
  "v_readlane_b32 s89, v251, s76\n\ts_add_u32 s76, s76, 1\n\ts_and_b32 s87, s89, 0x1fc00\n\tv_xor_b32 v249, s89, v250\n\t" \
  "s_add_u32 m0, s87, s82\n\ts_cmp_lt_u32 s76, s88\n\tglobal_load_lds_dwordx4 v249, s[80:81] nt\n\ts_cbranch_scc1 5b\n\t" \
  "6:\n\t" MKR \
  "s_waitcnt lgkmcnt(0)\n\t" \
  "v_readfirstlane_b32 s85, v252\n\tv_readfirstlane_b32 s86, v254\n\tv_readfirstlane_b32 s84, v253\n\tv_readfirstlane_b32 s75, v255\n\t" \
  "s_bitset1_b32 s85, 7\n\ts_and_b32 s89, s86, s78\n\ts_cmp_eq_u32 s89, s78\n\ts_cbranch_scc0 9f\n\ts_and_b32 s89, s85, s79\n\ts_cbranch_scc0 9f\n\t" \
  "s_cmp_lg_u32 s67, 0\n\ts_cbranch_scc1 10f\n" \
  "9:\n\ts_cmp_eq_u32 s67, 2\n\ts_cbranch_scc1 13f\n\ts_and_b32 s89, s85, s68\n\ts_cselect_b32 s67, 1, 0\n" \
  "13:\n\ts_sub_u32 s90, s90, 1\n\ts_cbranch_scc0 3b\n" \
  "10:\n\ts_mov_b32 m0, s91\n" \
  "15:\n\ts_cmp_eq_u32 s77, 2\n\ts_cselect_b32 s66, 0, 15\n\ts_or_b32 s66, s66, 0xc00000\n\t" \
  "s_lshr_b32 s65, s77, 2\n\ts_cmp_eq_u32 s76, 64\n\ts_cselect_b32 s65, s65, 1\n\ts_lshl_b32 s65, s65, 8\n\ts_cmp_eq_u32 s76, s76\n\ts_setpc_b64 s[92:93]\n" \
  MKR ".Lkv_sig:\n\t" \
  "s_waitcnt vmcnt(16)\n\ts_cmp_eq_u32 s77, 2\n\ts_cbranch_scc1 16f\n\ts_waitcnt vmcnt(0)\n" \
  "16:\n\ts_lshl_b32 s88, s77, 2\n\ts_add_u32 s88, s88, s83\n\ts_lshl_b32 s88, 1, s88\n\t" \
  "v_mov_b32 v248, 0\n\tv_mov_b32 v249, s88\n\ts_mov_b64 exec, 1\n\tds_add_u32 v248, v249 offset:%1\n\ts_mov_b64 exec, -1\n\t" \
  "s_add_u32 s77, s77, 1\n\ts_branch 15b\n\t.subsection 12\n\t.fill 64, 4, 0xBF810000\n\t.subsection 0" \
  :: "i"(L_CNT), "i"(L_CNT + C_ST * 4), "i"(RES ? 4 : 0x40))
DI unsigned cnt_add_rtn(unsigned c) {
  u64 keep;
  unsigned v;
  asm volatile("s_mov_b64 %0, exec\n\ts_mov_b64 exec, 1\n\tds_add_rtn_u32 %1, %2, %3 offset:%4\n\ts_waitcnt lgkmcnt(0)\n\ts_mov_b64 exec, %0"
               : "=&s"(keep), "=&v"(v) : "v"(c * 4u), "v"(1u), "i"(L_CNT) : "memory");
  return __builtin_amdgcn_readfirstlane(v);
}
DI unsigned cnt_get(unsigned c) {
  unsigned v;
  asm volatile("ds_read_b32 %0, %1 offset:%2\n\ts_waitcnt lgkmcnt(0)" : "=v"(v) : "v"(c * 4u), "i"(L_CNT) : "memory");
  return __builtin_amdgcn_readfirstlane(v);
}
template <int SLP = 0> DI void cnt_wait(unsigned c, unsigned target) {
#pragma unroll 1
  for (unsigned s = 0; s < SPIN_CAP * 16u; ++s) {
    if (cnt_get(c) >= target) break;
    if (SLP) __builtin_amdgcn_s_sleep(SLP);
  }
}
DI unsigned now32() { return (unsigned)__builtin_amdgcn_s_memrealtime(); }
DI void wait_until(unsigned t0, unsigned ticks) {
  if (ticks == 0) return;
#pragma unroll 1
  while (now32() - t0 < ticks) __builtin_amdgcn_s_sleep(1);
}
DI v4u ld_sc1(__amdgpu_buffer_rsrc_t rs, unsigned off) { return __builtin_amdgcn_raw_buffer_load_b128(rs, off, 0, 16); }
DI v2u ld_sc1_64(__amdgpu_buffer_rsrc_t rs, unsigned off) { return __builtin_amdgcn_raw_buffer_load_b64(rs, off, 0, 16); }
DI void st_sc1(__amdgpu_buffer_rsrc_t rs, unsigned off, v4u v) { __builtin_amdgcn_raw_buffer_store_b128(v, rs, off, 0, 16); }
DI void st_sc1_64(__amdgpu_buffer_rsrc_t rs, unsigned off, v2u v) { __builtin_amdgcn_raw_buffer_store_b64(v, rs, off, 0, 16); }
DI void st_sc1_32(__amdgpu_buffer_rsrc_t rs, unsigned off, unsigned v) { __builtin_amdgcn_raw_buffer_store_b32(v, rs, off, 0, 16); }
DI unsigned fswz(unsigned row) { return ((row & 3u) << 2) | ((0x78u >> (2u * ((row >> 2) & 3u))) & 3u); }
template <int N> DI float ror16(float v) {
  return __int_as_float(__builtin_amdgcn_update_dpp(0, __float_as_int(v), 0x120 + N, 0xF, 0xF, false));
}
DI float xor1(float v) { return __int_as_float(__builtin_amdgcn_update_dpp(0, __float_as_int(v), 0xB1, 0xF, 0xF, false)); }
DI float add16(float v) { auto r = __builtin_amdgcn_permlane16_swap(__float_as_uint(v), __float_as_uint(v), false, false); return __uint_as_float(r[0]) + __uint_as_float(r[1]); }
DI float add32(float v) { auto r = __builtin_amdgcn_permlane32_swap(__float_as_uint(v), __float_as_uint(v), false, false); return __uint_as_float(r[0]) + __uint_as_float(r[1]); }
DI float rs_add16(float a, float b) { auto r = __builtin_amdgcn_permlane16_swap(__float_as_uint(a), __float_as_uint(b), false, false); return __uint_as_float(r[0]) + __uint_as_float(r[1]); }
DI float rs_add32(float a, float b) { auto r = __builtin_amdgcn_permlane32_swap(__float_as_uint(a), __float_as_uint(b), false, false); return __uint_as_float(r[0]) + __uint_as_float(r[1]); }
DI float rs_max16(float a, float b) { auto r = __builtin_amdgcn_permlane16_swap(__float_as_uint(a), __float_as_uint(b), false, false); return fmaxf(__uint_as_float(r[0]), __uint_as_float(r[1])); }
DI float max32(float v) { auto r = __builtin_amdgcn_permlane32_swap(__float_as_uint(v), __float_as_uint(v), false, false); return fmaxf(__uint_as_float(r[0]), __uint_as_float(r[1])); }
DI float rowsum(float v) { v += ror16<8>(v); v += ror16<4>(v); v += ror16<2>(v); v += ror16<1>(v); return v; }
DI float rowmax(float v) { v = fmaxf(v, ror16<8>(v)); v = fmaxf(v, ror16<4>(v)); v = fmaxf(v, ror16<2>(v)); v = fmaxf(v, ror16<1>(v)); return v; }
DI float ex2(float v) { return __builtin_amdgcn_exp2f(v); }
constexpr float LOG2E = 1.4426950408889634f;

DI void transpose8(const v4u (&r)[8], v4u (&c)[8]) {
#pragma unroll
  for (int q = 0; q < 4; ++q) {
    if (q == 0 || q == 2) { SB0(); MW(); }
#pragma unroll
    for (int e2 = 0; e2 < 4; ++e2) {
      unsigned a = r[2 * e2][q], b = r[2 * e2 + 1][q];
      c[2 * q][e2] = __builtin_amdgcn_perm(b, a, 0x05040100u);
      c[2 * q + 1][e2] = __builtin_amdgcn_perm(b, a, 0x07060302u);
    }
    asm volatile("" : "+v"(c[2 * q]), "+v"(c[2 * q + 1]));
  }
}
#define PLD(f) asm volatile("global_atomic_add_x2 %0, %1, %2, %3 sc0 sc1" : "=&v"(f) : "v"(fo), "v"(zz), "s"(wsp) : "memory")
#define PCHK(f) (__extension__({ asm volatile("s_waitcnt vmcnt(2)" : "+v"(f)::"memory"); __all(((f[0] ^ tag) | (f[1] ^ tag)) == 0); }))
DI void poll_flags(const unsigned char* wsp, unsigned fo, unsigned tag, v2u& f0, v2u& f1, v2u& f2) {
  const v2u zz = {0u, 0u};
  PLD(f0); __builtin_amdgcn_s_sleep(POLL_GAP);
  PLD(f1); __builtin_amdgcn_s_sleep(POLL_GAP);
  PLD(f2);
#pragma unroll 1
  for (unsigned spin = 0; spin < SPIN_CAP; ++spin) {
    if (PCHK(f0)) break;
    PLD(f0);
    if (PCHK(f1)) break;
    PLD(f1);
    MW();
    if (PCHK(f2)) break;
    PLD(f2);
  }
}

template <int N> DI unsigned fpos(unsigned l) { return N ? (l % N) * 128 + (l / N) * 8 : l * 8; }
DI void send_flags(__amdgpu_buffer_rsrc_t rs, int wi, int lane, unsigned tag) {
#pragma unroll
  for (int t = 0; t < (T_O == 0 || T_F == 0 || T_UV == 1 ? 4 : 2); ++t) st_sc1_32(rs, WS_FLAG + (64 * t + lane) * 1024 + fpos<FLW>(wi >> 1) + (wi & 1) * 4, tag);
}

#define PIN(x) asm volatile("" : "+v"(x))
__global__ void __launch_bounds__(512, 2) fused_kernel(Params p) {
  extern __shared__ __attribute__((aligned(16))) unsigned char smem[];
  const u64 t0l = __builtin_amdgcn_s_memrealtime();
  const unsigned t0 = (unsigned)t0l;
  const int tid = threadIdx.x, lane = tid & 63;
  const int w = __builtin_amdgcn_readfirstlane(tid >> 6);
  const int cu = blockIdx.x;
  const int xcd = cu & 7, r8 = cu >> 3, idx = r8 >> 1;
  const __amdgpu_buffer_rsrc_t rs = __builtin_amdgcn_make_buffer_rsrc((void*)p.ws, 0, (int)WS_END, 0x00020000);
  const int g4 = lane >> 4, i16 = lane & 15;
  unsigned gen;
  asm volatile("s_load_dword %0, %1, 0x0" : "=s"(gen) : "s"(p.ws), "s"((unsigned)(size_t)(LAS unsigned char*)smem) : "memory");
  unsigned gen1;
#define GET_TAG() (__extension__({ asm volatile("s_waitcnt lgkmcnt(0)\n\ts_add_u32 %0, %1, 1" : "=s"(gen1) : "s"(gen) : "memory", "scc"); (gen1 & 0x3FFFu) | ((gen1 << 2) & 0x3FFF0000u); }))

  MK_TABLE();
  if ((r8 & 1) == 0) {
    const int b = BMAP ? idx & 7 : xcd, s = BMAP ? xcd + (idx & 8) : idx, kvi = b * 16 + s;
    const unsigned hh = lane >> 5, hd = lane & 31;
    WARM(w, e, idx * 8 + w, 128);
    auto wait_p = [&](unsigned pm, unsigned fm) AI {
      v4u cv;
#pragma unroll 1
      for (unsigned sp = 0; sp < SPIN_CAP * 16u; ++sp) {
        asm volatile("ds_read_b128 %0, %1 offset:%2\n\ts_waitcnt lgkmcnt(0)" : "=v"(cv) : "v"(0u), "i"(L_CNT) : "memory");
        if ((__builtin_amdgcn_readfirstlane(cv[0]) & pm) && (__builtin_amdgcn_readfirstlane(cv[3]) & fm) == fm) break;
      }
      return __builtin_amdgcn_readfirstlane(cv[1]);
    };
    auto wait_bits = [&](unsigned c, unsigned m) AI {
#pragma unroll 1
      for (unsigned sp = 0; sp < SPIN_CAP * 16u; ++sp)
        if ((cnt_get(c) & m) == m) break;
    };
    if (w >= 4) {
      const int k = w - 4;
      (void)GET_TAG();
      unsigned l16 = lane * 16, ap0;
      {
        const unsigned blk = (lane >> 4) & 1, q4 = i16 >> 2, pp = lane & 3;
        const unsigned r0 = 8 * hh + q4;
        const unsigned ch = 16 * k + 2 * (pp & 1) + blk;
        ap0 = L_SLOT + r0 * 1024 + ((ch ^ fswz(r0)) * 16) + 8 * (pp >> 1);
      }
      PIN(l16); PIN(ap0);
      v4u vf[2][16];
      unsigned ap1 = ap0 + 65536;
      PIN(ap1);
      auto ldv1 = [&](int r, int n) AI {
        const unsigned ap = (r & 1) ? ap1 : ap0;
        const v2u t0_ = trread((ap ^ ((n >> 2) * 64)) + (n & 3) * 16384), t1_ = trread((ap ^ ((n >> 2) * 64 + 32)) + (n & 3) * 16384 + 4096);
        vf[r & 1][n] = v4u{t0_[0], t0_[1], t1_[0], t1_[1]};
      };
      auto ldv = [&](int r) AI {
#pragma unroll
        for (int n = 0; n < 16; ++n) {
          ldv1(r, n);
          if ((n & 7) == 7) { if (r < 2) MS(); else MP(); }
        }
      };
      const char* base = (const char*)p.ckv + ((size_t)kvi * 256 + 16 * k) * 1024;
      unsigned xt = ((lane >> 4) << 16) | (i16 << 10) | (fswz(i16) << 4);
      unsigned ni = 0, sg = 0, ovf = 0, pc = 0, st = 0, fl = 0, cm = 0, cw = 0;
      KV_RUN_BODY();
      f32x16 oacc[4];
      v4u pf[2][4], cv4;
#define CALL0(L) "s_getpc_b64 s[92:93]\n\ts_cbranch_scc0 " L
#define RUN_TXT(L) "s_cmp_lg_u32 s76, s76\n\t" CALL0(L)
#define SIG_TXT "s_getreg_b32 s87, hwreg(HW_REG_IB_STS)\n\ts_and_b32 s87, s87, s66\n\ts_cmp_lg_u32 s87, s65\n\t" CALL0(".Lkv_sig")
#define RUN_OUT "+{s76}"(ni), "+{s77}"(sg), "={s84}"(ovf), "={s85}"(pc), "+{s86}"(st), "+{s75}"(fl), "+{s66}"(cm), "+{s65}"(cw)
#define RUN_IN "{s78}"(xm), "{s79}"(pm), "{s[80:81]}"(base), "{s82}"(L_SLOT + k * 16384), "{s83}"(k), "{v250}"(l16), "{v251}"(xt)
#define RUN_CL "s67", "s87", "s88", "s89", "s90", "s91", "s92", "s93", "s94", "s95", "v248", "v249", "scc", "memory"
      auto run = [&](unsigned xm, unsigned pm) AI { asm volatile(RUN_TXT(".Lkv_run") : RUN_OUT, "={v[252:255]}"(cv4) : RUN_IN : RUN_CL); };
#define PF0(c) c "{v[232:235]}"(pf[0][0]), c "{v[236:239]}"(pf[0][1]), c "{v[240:243]}"(pf[0][2]), c "{v[244:247]}"(pf[0][3]), c "{v[252:255]}"(cv4)
#define PF1(c) c "{v[216:219]}"(pf[1][0]), c "{v[220:223]}"(pf[1][1]), c "{v[224:227]}"(pf[1][2]), c "{v[228:231]}"(pf[1][3]), c "{v[252:255]}"(cv4)
#define RUNP_IN RUN_IN, "{s74}"(L_P + r * 4096), "{s68}"(r ? ANY : 0x7Fu)
      auto runp = [&](int r, unsigned pm, bool chk) AI {
        const unsigned xm = 0;
        if (chk) {
#define CHK_TXT(a, b, c, d) "s_cmp_eq_u32 s76, 64\n\ts_cselect_b32 s89, s79, 0\n\ts_mov_b32 s90, 0xffffff\n3:\n\ts_waitcnt lgkmcnt(0)\n\t" \
                "v_readfirstlane_b32 s85, v252\n\tv_readfirstlane_b32 s86, v254\n\tv_readfirstlane_b32 s84, v253\n\tv_readfirstlane_b32 s75, v255\n\t" \
                "s_and_b32 s87, s89, s85\n\ts_getpc_b64 s[92:93]\n\ts_cbranch_scc0 1f\n\t.subsection 11\n\t" MKB "1:\n\t" \
                "s_cmp_eq_u32 s89, 0\n\ts_cbranch_scc1 .Lkv_chk\n\tv_mov_b32 v252, 0\n\tv_add_u32 v249, s74, v250\n\tds_read_b128 v[252:255], v252 offset:" STR(L_CNT_) "\n\t" \
                "ds_read_b128 " a ", v249\n\tds_read_b128 " b ", v249 offset:1024\n\tds_read_b128 " c ", v249 offset:2048\n\tds_read_b128 " d ", v249 offset:3072\n\t" \
                SIG_TXT "\n\ts_sub_u32 s90, s90, 1\n\ts_cselect_b32 s89, 0, s89\n\ts_branch 3b\n\t.subsection 0"
          if (r & 1) asm volatile(CHK_TXT("v[216:219]", "v[220:223]", "v[224:227]", "v[228:231]") : RUN_OUT, PF1("+") : RUNP_IN : RUN_CL);
          else asm volatile(CHK_TXT("v[232:235]", "v[236:239]", "v[240:243]", "v[244:247]") : RUN_OUT, PF0("+") : RUNP_IN : RUN_CL);
        } else {
          if (r & 1) asm volatile(RUN_TXT(".Lkv_runp") : RUN_OUT, PF1("=") : RUNP_IN : RUN_CL);
          else asm volatile(RUN_TXT(".Lkv_runp") : RUN_OUT, PF0("=") : RUNP_IN : RUN_CL);
        }
      };
#define SPEC_TXT "v_mov_b32 v252, 0\n\tds_read_b128 %4, v252 offset:%6\n\tds_read_b128 %0, %5 offset:%7\n\tds_read_b128 %1, %5 offset:%7+1024\n\t" \
                 "ds_read_b128 %2, %5 offset:%7+2048\n\tds_read_b128 %3, %5 offset:%7+3072"
      auto spec = [&](int r) AI {
        if (r & 1) asm volatile(SPEC_TXT : PF1("=") : "v"(l16), "i"(L_CNT), "i"(L_P + r * 4096) : "memory");
        else asm volatile(SPEC_TXT : PF0("=") : "v"(l16), "i"(L_CNT), "i"(L_P + r * 4096) : "memory");
      };
      WARM(s, e, k, 4);
      wait_until(t0, KV_DLY);
      run(0u, ANY);
      st = 0; fl = 0;
      wg_barrier();
      MS();
      run(0xFu, ANY);
      ldv(0);
      cnt_addv(C_ST, 0x10000u);
      run(0xF0u, ANY);
      ldv(1);
      cnt_addv(C_ST, 0x100000u);
      auto sigchk = [&]() AI {
        asm volatile(SIG_TXT
                     : "+{s77}"(sg), "+{s66}"(cm), "+{s65}"(cw) : "{s76}"(ni), "{s83}"(k) : RUN_CL);
      };
      auto getp = [&](int r, bool chk) AI {
        runp(r, 0xCu << (8 * r), chk);
        if (r && RARE(ovf & (7u << (8 * r)))) {
          runp(r, 8u << (8 * r), false);
          const float alpha = lds<float>(L_AL + r * 128 + hd * 4);
#pragma unroll
          for (int ct = 0; ct < 4; ++ct)
#pragma unroll
            for (int e = 0; e < 16; ++e) oacc[ct][e] *= alpha;
        }
        MP();
      };
      auto pv = [&](int r) AI {
#pragma unroll
        for (int n = 0; n < 16; ++n) {
          SB0();
          if (r == 0 && (n & 3) == 0) {
            f32x16 z;
#pragma unroll
            for (int e = 0; e < 16; ++e) z[e] = 0.f;
            oacc[n >> 2] = mfma32(vf[r & 1][n], pf[r & 1][n & 3], z);
          } else {
            oacc[n >> 2] = mfma32(vf[r & 1][n], pf[r & 1][n & 3], oacc[n >> 2]);
          }
          PIN(oacc[n >> 2]);
          SB0();
          if (r && n < 8) { ldv1(r + 1, 2 * n); ldv1(r + 1, 2 * n + 1); }
          if (n == SPAT) spec(r + 1);
          if (MIDSIG && (n & 3) == 1) sigchk();
          if ((n & 3) == 3) MP();
        }
      };
      MP();
      getp(0, false);
      if (PPRIO) __builtin_amdgcn_s_setprio(PPRIO);
      pv(0);
      getp(1, true);
      pv(1);
      if (RARE((st & 0xF00u) != 0xF00u)) {
        MP();
        run(0xF00u, ANY);
        ldv(2);
        spec(2);
      }
      cnt_addv(C_FL, 0x1000u);
      MP();
      getp(2, true);
      pv(2);
      if ((st & 0xF000u) != 0xF000u) {
        if (RARE(ni != 64)) run(0xF000u, ANY);
#pragma unroll 1
        for (unsigned sp = 0; sp < SPIN_CAP * 16u; ++sp) {
          sigchk();
          if ((cnt_get(C_ST) & 0xF000u) == 0xF000u) break;
        }
        if (VD) __builtin_amdgcn_s_sleep(VD);
        ldv(3);
        spec(3);
      }
      {
        const unsigned sb = L_SLOT + k * 16384;
        unsigned sa = sb + hd * 272 + hh * 64;
        unsigned rx = sb + (lane >> 3) * 272 + (lane & 7) * 16;
        unsigned gx = WS_CIN + ((lane >> 3) * 4 + k) * CIN_WI + (kvi ^ (CSW ? (lane >> 3) * 16 : 0)) * CIN_ROW + (lane & 7) * 16;
        MP();
        PIN(sa); PIN(rx); PIN(gx);
        getp(3, true);
        if (RARE(!(fl & 0x4000u))) wait_bits(C_FL, 0x4000u);
        auto dwd = [&](int d) AI { return pk2(oacc[d >> 3][2 * (d & 7)], oacc[d >> 3][2 * (d & 7) + 1]); };
        auto cvt = [&](int ct) AI {
#pragma unroll
          for (int g = 2 * ct; g < 2 * ct + 2; ++g) lds<v4u>(sa + (g >> 2) * 128 + (g & 3) * 16) = v4u{san(dwd(4 * g)), dwd(4 * g + 1), dwd(4 * g + 2), dwd(4 * g + 3)};
        };
        v4u ov[4];
        auto ord = [&](int hf) AI {
#pragma unroll
          for (int t = 0; t < 4; ++t) ov[t] = lds128(rx + t * 2176 + hf * 128);
        };
        auto ost = [&](int hf) AI {
#pragma unroll
          for (int t = 0; t < 4; ++t) __builtin_amdgcn_raw_buffer_store_b128(ov[t], rs, gx + hf * 128, t * 32 * CIN_WI, 16);
        };
#pragma unroll
        for (int n = 0; n < 16; ++n) {
          SB0();
          oacc[n >> 2] = mfma32(vf[1][n], pf[1][n & 3], oacc[n >> 2]);
          SB0();
          if ((n & 3) == 1) MP();
          if (n == 5) cvt(0);
          if (n == 9) { cvt(1); ord(0); }
          if (n == OSAT) { ost(0); if (MLAT == 16) cnt_addv(C_XS, 1u << k); }
          if (n == 13) cvt(2);
        }
        SB0();
        MP();
        if (MLAT == 17) cnt_addv(C_XS, 1u << k);
        cvt(3);
        ord(1);
        SB0();
        ost(1);
        if (MLAT == 18) cnt_addv(C_XS, 1u << k);
      }
    } else {
      const int k = w;
      v2u f0 = {0u, 0u}, f1 = f0, f2 = f0;
      if (k == 0 && lane < C_N) lds<unsigned>(L_CNT + lane * 4) = 0u;
      wg_barrier();
      const unsigned tag = GET_TAG();
      WARM(q, s, k, 4);
      if (QPRIO) __builtin_amdgcn_s_setprio(QPRIO);
      float nr0 = 0.f, nr1 = 0.f;
      v2f lsum = {0.f, 0.f};
      const float OVT = 56.f;
      unsigned nx = 0;
      unsigned aq0 = L_SLOT + (16 * k + i16) * 1024 + ((fswz(i16) ^ g4) * 16);
      unsigned po = L_P + k * 1024 + (i16 + 32 * (g4 >> 1)) * 16 + (g4 & 1) * 8;
      PIN(po); PIN(aq0);
      v4u kf[2][16];
      auto ldk = [&](int r) AI {
#pragma unroll
        for (int u = 0; u < 16; ++u) kf[r & 1][u] = lds128(((aq0 + (r & 1) * 65536) ^ ((u & 3) * 64)) + (u >> 2) * 256);
      };
      v4u sel;
#pragma unroll
      for (int q = 0; q < 4; ++q) sel[q] = ((g4 & 1) == (i16 >> 3) && ((i16 & 7) >> 1) == q) ? ((i16 & 1) ? 0x3F800000u : 0x3F80u) : 0u;
      unsigned qs = WS_QABS + (g4 >> 1) * 262144 + b * 32768 + k * 8192 + (lane & 31) * 16;
      unsigned qd = WS_QABS + b * 32768 + (16 + i16) * 1024 + (60 + g4) * 16;
      unsigned qw[8], qw15;
#pragma unroll
      for (int u = 0; u < 8; ++u) { qw[u] = L_Q + k * 8192 + ((i16 ^ fswz(8 * k + u)) * 16) + (g4 >> 1) * 256 + (g4 & 1) * 8; PIN(qw[u]); }
      qw15 = qw[7] - (k == 3 && g4 >= 2 && i16 >= 12 ? 4096u : 0u);
      unsigned qr = L_Q + i16 * 1024 + ((fswz(i16) ^ g4) * 16);
      PIN(sel); PIN(qs); PIN(qd); PIN(qw15); PIN(qr);
      unsigned mxw = L_Q + (16 + (hd >> 2)) * 1024 + 768 + ((3 ^ ((hd >> 2) & 3)) * 64) + (hd & 3) * 16 + k * 4;
      unsigned mxr = L_Q + (16 + (i16 >> 2)) * 1024 + 768 + ((3 ^ (i16 >> 2)) * 64) + (i16 & 3) * 16;
      PIN(mxw); PIN(mxr);
      wait_bits(C_ST, 1u << k);
      ldk(0);
      cnt_addv(C_ST, 1u << (24 + k));
      MQ();
      if (K1AT == 0) {
        wait_bits(C_ST, 16u << k);
        ldk(1);
        cnt_addv(C_ST, 1u << (28 + k));
        MQ();
      }
      v4u qf[32], qv[16], da, db;
      const unsigned char* wsp = p.ws;
      const unsigned char* wsp2 = p.ws + 4096;
#define QLD(t) asm volatile("global_load_dwordx4 %0, %1, %2 offset:%3 sc1" : "=v"(qv[t]) : "v"(qs), "s"((t) < 8 ? wsp : wsp2), "i"(((t) & 7) * 512) : "memory")
      if (k == 0) {
        wait_until(t0, POLL_START);
        const unsigned fo = WS_FLAG + kvi * 1024 + fpos<FLW>(lane);
        const v2u zz = {0u, 0u};
        u64 seen = 0;
        bool sent = false;
        auto chk = [&](v2u& f) AI {
          asm volatile("s_waitcnt vmcnt(2)" : "+v"(f)::"memory");
          const u64 ok = __ballot(((f[0] ^ tag) | (f[1] ^ tag)) == 0) | seen;
          if (ok != seen) {
            seen = ok;
            lds<v2u>(L_CNT + C_MASK * 4) = v2u{(unsigned)ok, (unsigned)(ok >> 32)};
            if (PAUSE && !sent && __popcll(~ok) <= PK) { cnt_addv(C_FL, 0x100u); sent = true; }
          }
          return ok == ~0ull;
        };
        MQ();
        PLD(f0); __builtin_amdgcn_s_sleep(POLL_GAP);
        PLD(f1); __builtin_amdgcn_s_sleep(POLL_GAP);
        PLD(f2);
#pragma unroll 1
        for (unsigned spin = 0; spin < SPIN_CAP; ++spin) {
          if (chk(f0)) break;
          PLD(f0);
          MQ();
          if (chk(f1)) break;
          PLD(f1);
          MQ();
          if (chk(f2)) break;
          PLD(f2);
        }
        MQ();
#pragma unroll
        for (int t = 0; t < 16; ++t) QLD(t);
      } else {
        unsigned pend = 0xFFFFu;
#pragma unroll 1
        for (unsigned spin = 0; spin < SPIN_CAP * 16u; ++spin) {
          v2u mk;
          asm volatile("ds_read_b64 %0, %1 offset:%2\n\ts_waitcnt lgkmcnt(0)" : "=v"(mk) : "v"(C_MASK * 4u), "i"(L_CNT) : "memory");
          const unsigned m0 = __builtin_amdgcn_readfirstlane(mk[0]), m1 = __builtin_amdgcn_readfirstlane(mk[1]);
          const unsigned nw = ((k < 2 ? m0 : m1) >> (16 * (k & 1))) & pend;
          if (PROG ? nw != 0u : (m0 & m1) == ~0u) {
            pend &= ~nw;
#pragma unroll
            for (int t = 0; t < 16; ++t)
              if (nw >> t & 1u) { QLD(t); if ((t & 7) == 0) MQ(); }
          }
          if ((m0 & m1) == ~0u) break;
          __builtin_amdgcn_s_sleep(1);
        }
      }
#define QLD2() asm volatile("global_load_dwordx4 %0, %2, %3 sc1\n\tglobal_load_dwordx4 %1, %2, %4 sc1" : "=&v"(da), "=&v"(db) : "v"(qd), "s"(wsp), "s"(wsp + 262144) : "memory")
      QLD2();
      cnt_add(C_FL);
      MQ();
      if (K1AT == 1) {
        wait_bits(C_ST, 16u << k);
        ldk(1);
        cnt_addv(C_ST, 1u << (28 + k));
      }
#pragma unroll 1
      for (unsigned spin = 0; spin < SPIN_CAP; ++spin) {
        if (spin) {
#pragma unroll
          for (int t = 0; t < 16; ++t) QLD(t);
          QLD2();
        }
        MQ();
        asm volatile("s_waitcnt vmcnt(2)" : W8(qv, 0), W8(qv, 8), "+v"(f0), "+v"(f1), "+v"(f2)::"memory");
        float chk = 0.f;
        f32x4 d[16];
#pragma unroll
        for (int t = 0; t < 18; ++t) {
          if (t < 16) d[t] = mfma16(sel, qv[t], f32x4{0.f, 0.f, 0.f, 0.f});
          if (t >= 2) {
            const int u = t - 2;
            lds<v2u>((u == 15 ? qw15 : qw[u >> 1]) + (u >> 1) * 1024 + (u & 1) * 512) = v2u{pk2(d[u][0], d[u][1]), pk2(d[u][2], d[u][3])};
            chk += d[u][0];
          }
          SB0();
          if ((t & 3) == 1) MQ();
        }
        asm volatile("s_waitcnt vmcnt(0)" : "+v"(da), "+v"(db)::"memory");
        if (!__any(chk != chk || max(da[0], db[0]) == POISON)) break;
        unsigned mx = max(da[0], db[0]);
#pragma unroll
        for (int t = 0; t < 16; ++t) mx = max(mx, qv[t][0]);
        if (!__any(mx == POISON)) break;
        CFENCE();
      }
      MQ();
      cnt_addv(C_FL, 16u);
#pragma unroll
      for (int e = 0; e < 4; ++e) qf[31][e] = pk2(bf2f(da[e]) + bf2f(db[e]), bfhi(da[e]) + bfhi(db[e]));
      PIN(qf[31]);
      MQ();
      wait_bits(C_FL, 0x40u);
      MQ();
      f32x4 s0 = {0.f, 0.f, 0.f, 0.f}, s1 = s0, a0 = s0, a1 = s0, t0, t1;
      auto ldq = [&](int f) AI { qf[(f >> 1) + 16 * (f & 1)] = lds128((qr ^ (((f >> 1) & 3) * 64)) + (f >> 3) * 256 + (f & 1) * 16384); };
      v4u kn[16], cs;
      auto ldn = [&](int r, int u) AI { kn[u] = lds128(((aq0 + (r & 1) * 65536) ^ ((u & 3) * 64)) + (u >> 2) * 256); };
#define CRD() asm volatile("ds_read_b128 %0, %1 offset:%2" : "=v"(cs) : "v"(0u), "i"(L_CNT) : "memory")
#define CWT() asm volatile("s_waitcnt lgkmcnt(0)" : "+v"(cs)::"memory")
#pragma unroll
      for (int f = 0; f < 12; ++f) ldq(f);
#pragma unroll
      for (int u = 0; u < 16; ++u) {
        SB0();
        s0 = mfma16(kf[0][u], qf[u], s0);
        s1 = mfma16(kf[0][u], qf[16 + u], s1);
        if (MIX && (u & 1)) a1 = mfma16(kf[1][u >> 1], qf[16 + (u >> 1)], a1);
        else if (MIX) a0 = mfma16(kf[1][u >> 1], qf[u >> 1], a0);
        SB0();
        if ((u & 3) == 0) MQ();
        if (12 + 2 * u < 31) ldq(12 + 2 * u);
        if (13 + 2 * u < 31) ldq(13 + 2 * u);
      }
      MQ();
      unsigned cx;
      f32x4 ma, mb;
      auto max4 = [&](const f32x4& v) AI { return fmaxf(fmaxf(v[0], v[1]), fmaxf(v[2], v[3])); };
      auto post_max = [&](bool first) AI {
        const float mw = max32(rs_max16(max4(s0), max4(s1)));
        lds<float>(mxw) = mw;
        if (first) cnt_addv(C_P, 0x10u);
        else { cnt_add(C_MX); nx += 4; }
      };
#define XRD(c, w) asm volatile("ds_read_b32 %0, %3 offset:%5\n\tds_read_b128 %1, %4\n\tds_read_b128 %2, %4 offset:4096" w : "=&v"(cx), "=&v"(ma), "=&v"(mb) : "v"(0u), "v"(mxr), "i"(L_CNT + (c) * 4) : "memory")
      auto exchange = [&](int r) AI {
        asm volatile("s_waitcnt lgkmcnt(0)" : "+v"(cx), "+v"(ma), "+v"(mb)::"memory");
        if (RARE(r ? __builtin_amdgcn_readfirstlane(cx) < nx : !(__builtin_amdgcn_readfirstlane(cx) & 0x40u))) {
#pragma unroll 1
          for (unsigned sp = 0; sp < SPIN_CAP * 16u; ++sp) {
            if (r == 0) MQ();
            XRD(r ? C_MX : C_P, "\n\ts_waitcnt lgkmcnt(0)");
            if (r ? __builtin_amdgcn_readfirstlane(cx) >= nx : (__builtin_amdgcn_readfirstlane(cx) & 0x40u) != 0) break;
          }
        }
        float d0 = max4(ma), d1 = max4(mb);
        if (r) {
          d0 = fmaxf(d0, 0.f); d1 = fmaxf(d1, 0.f);
          const float al0 = ex2(-d0), al1 = ex2(-d1);
          lsum[0] *= al0; lsum[1] *= al1;
          lds<float>(L_AL + r * 128 + hd * 4) = (lane & 16) ? al1 : al0;
        }
#pragma unroll
        for (int e = 0; e < 4; ++e) { s0[e] -= d0; s1[e] -= d1; }
        nr0 -= d0; nr1 -= d1;
      };
      v2f ln;
      auto do_p = [&](int r, const f32x4& x0, const f32x4& x1, unsigned ov) AI {
        float e0[4], e1[4];
#pragma unroll
        for (int e = 0; e < 4; ++e) { e0[e] = ex2(x0[e]); e1[e] = ex2(x1[e]); }
        lds<v2u>(po + r * 4096) = v2u{pk2(e0[0], e0[1]), pk2(e0[2], e0[3])};
        lds<v2u>(po + r * 4096 + 256) = v2u{pk2(e1[0], e1[1]), pk2(e1[2], e1[3])};
        cnt_add2(C_P, 1u << (8 * r), ov);
        ln = lsum + ((v2f{e0[0], e1[0]} + v2f{e0[1], e1[1]}) + (v2f{e0[2], e1[2]} + v2f{e0[3], e1[3]}));
      };
      auto redo = [&](int r) AI {
        post_max(false);
        XRD(C_MX, "");
        exchange(r);
        do_p(r, s0, s1, 0u);
      };
      auto fin = [&](int r, const f32x4& x0, const f32x4& x1) AI {
        MQ();
#pragma unroll
        for (int e = 0; e < 4; ++e) { s0[e] = x0[e] + nr0; s1[e] = x1[e] + nr1; }
        const unsigned ov = __any(fmaxf(max4(s0), max4(s1)) > OVT) ? 1u << (8 * r) : 0u;
        do_p(r, s0, s1, ov);
      };
#pragma unroll
      for (int u = 8 * MIX; u < 16; ++u) {
        SB0();
        a0 = mfma16(kf[1][u], qf[u], a0);
        a1 = mfma16(kf[1][u], qf[16 + u], a1);
        PIN(a0); PIN(a1);
        SB0();
        if (u <= 8 * MIX + XAT + 4 || u >= 13) MQ();
        if (u == 8 * MIX) post_max(true);
        if (u == 8 * MIX + XAT) XRD(C_P, "");
        if (u == 8 * MIX + 2 + XAT) exchange(0);
        if (u == 8 * MIX + 3 + XAT) {
          do_p(0, s0, s1, 0u);
          lsum = ln;
        }
        if (u == 13) {
          CRD();
#pragma unroll
          for (int v = 0; v < 16; ++v) ldn(2, v);
        }
      }
      fin(1, a0, a1);
      asm volatile("s_waitcnt lgkmcnt(14)" : "+v"(cs)::"memory");
#pragma unroll
      for (int r = 2; r < 4; ++r) {
        MQ();
        if (r == 3 || RARE(!(__builtin_amdgcn_readfirstlane(cs[2]) >> (8 + k) & 1u))) {
          if (r == 2) MQ();
          wait_bits(C_ST, 1u << (4 * r + k));
#pragma unroll
          for (int u = 0; u < 16; ++u) ldn(r, u);
        }
        MQ();
        CRD();
        t0 = f32x4{0.f, 0.f, 0.f, 0.f}; t1 = t0;
#pragma unroll
        for (int u = 0; u < 16; ++u) {
          SB0();
          t0 = mfma16(kn[u], qf[u], t0);
          t1 = mfma16(kn[u], qf[16 + u], t1);
          SB0();
          if ((u & 7) == 0) MQ();
          if (u == CKAT) {
            PIN(t0); PIN(t1);
            CWT();
            unsigned ov = __builtin_amdgcn_readfirstlane(cs[1]);
            if (RARE(!(__builtin_amdgcn_readfirstlane(cs[0]) >> (8 * r - 8) & 12u))) { MQ(); ov = wait_p(12u << (8 * r - 8), 0u); }
            if (RARE(ov & (7u << (8 * r - 8)))) redo(r - 1);
            lsum = ln;
            MQ();
          }
        }
        fin(r, t0, t1);
      }
      MQ();
      if (RARE(wait_p(0xC000000u, 0u) & 0x7000000u)) redo(3);
      lsum = ln;
      {
        MQ();
        const float lt = add32(rs_add16(lsum[0], lsum[1]));
        lds<float>(L_LS + k * 128 + hd * 4) = lt;
        cnt_add(C_MX);
        nx += 4;
        cnt_wait(C_MX, nx);
        const LAS float* ls = (const LAS float*)(size_t)(L_LS + hd * 4);
        v4u mlv = {__float_as_uint((lane & 16) ? -nr1 : -nr0), tag, __float_as_uint((ls[0] + ls[32]) + (ls[64] + ls[96])), tag};
        unsigned gm = WS_ML + ((hd * 4 + k) * 128 + kvi) * 16;
        PIN(mlv); PIN(gm);
        MQ();
        wait_bits(C_XS, 1u << k);
        if (lane < 32) st_sc1(rs, gm, mlv);
      }
    }
  } else {
    const int h = 4 * xcd + (idx & 3), j = idx >> 2, wi = 4 * h + j;
    const bool ffn = wi < 86;
    const unsigned hh = lane >> 5;
    v4u xa, xb;
    asm volatile("s_cmp_gt_u32 %3, 3\n\ts_cbranch_scc1 1f\n\tglobal_load_dwordx4 %0, %2, %4\n\tglobal_load_dwordx4 %1, %2, %4 offset:512\n1:"
                 : "=&v"(xa), "=&v"(xb) : "v"((2 * w + hh) * 1024 + (lane & 31) * 16), "s"(w), "s"(p.x) : "memory", "scc");
    constexpr int NT = FR4 ? 2 : 4;
    const int hf = FMAP ? (idx & 7) + 8 * (xcd & 3) : h, jf = FMAP ? (idx >> 3) + 2 * (xcd >> 2) : j;
    const int fr = FR4 ? 1 : __builtin_amdgcn_readfirstlane((jf >> 1) ^ 1);
    const int dj = jf & 1, c0 = (FR4 ? (jf >> 1) * 256 : 0) + w * 16 * NT;
    unsigned qb = WS_QABS + dj * 262144 + (i16 * 32 + hf) * 1024 + (c0 + 8 * g4) * 2;
    auto ffn_pf = [&](unsigned& d) {
      const int u = idx * 8 + w, q5 = lane / 5, r5 = lane - 5 * q5;
      const char* pb = (const char*)((q5 & 1) ? p.wu : p.wg) + (4 * u + (min(q5, 7) >> 1)) * (FF * 2) + min(xcd * 512 + min(r5 * 128, 508), FF * 2 - 4);
      asm volatile("global_load_dword %0, %1, off" : "=&v"(d) : "v"(pb) : "memory");
    };
    unsigned dpf = 0;
    SB0();
    if (WPF) {
      unsigned d0, d1;
      if (WPF == 1) {
        const unsigned po = (32 * idx + 4 * w + g4) * 8192 + xcd * 1024 + i16 * 64;
        asm volatile("global_load_dword %0, %4, %5\n\tglobal_load_dword %1, %4, %6\n\ts_waitcnt vmcnt(0)" : "=&v"(d0), "=&v"(d1), "+v"(xa), "+v"(xb) : "v"(po), "s"(p.wq), "s"(p.wuk) : "memory");
      } else {
        const char* pa = (const char*)(hh ? p.wuk : p.wq) + (32 * idx + 4 * w + ((lane >> 3) & 3)) * 8192 + xcd * 1024 + (lane & 7) * 128;
        asm volatile("global_load_dword %0, %1, off" : "=&v"(d0) : "v"(pa) : "memory");
        if (PZT) asm volatile("global_load_dword %0, %1, %2" : "=&v"(dpf) : "v"(WS_CIN + wi * CIN_WI + w * 4096), "s"(p.ws) : "memory");
        if (PZE == 1) {
#pragma unroll
          for (int t = 0; t < 4; ++t) st_sc1(rs, WS_CIN + wi * CIN_WI + w * 4096 + t * 1024 + lane * 16, v4u{POISON, POISON, POISON, POISON});
          CFENCE();
        }
        if (w >= 4) {
          wait_until(t0, WM_DLY);
          WARM(w, q, w - 4, 4);
          if (PZE == 3) VMWAIT(0);
        } else if (PZE == 3) {
          wait_until(t0, PZ_AT);
#pragma unroll
          for (int t = 0; t < 8; ++t) st_sc1(rs, WS_CIN + wi * CIN_WI + w * 8192 + t * 1024 + lane * 16, v4u{POISON, POISON, POISON, POISON});
          CFENCE();
        }
        asm volatile("s_waitcnt vmcnt(%4)" : "+v"(d0), "+v"(xa), "+v"(xb), "+v"(dpf) : "i"(PZE == 1 ? 4 : PZE == 3 ? 8 : 0) : "memory");
      }
      if (WPF_DLY) __builtin_amdgcn_s_sleep(WPF_DLY);
    }
    __builtin_amdgcn_s_barrier();
    MW();
    v4u wukf[8];
    if (fr) {
      const char* src = (const char*)p.wq + hf * 256 + dj * 128 + (size_t)(64 * w) * 8192;
#pragma unroll
      for (int t = 0; t < 8; ++t) {
        glds16s((lane >> 3) * 8192 + (((lane & 7) ^ (((g4 & 1) | ((t & 1) << 1)) << 1)) * 16), src + t * 65536, L_WQ + (64 * w + 8 * t) * 128);
        if (t == 3) MW();
      }
      MW();
    }
    {
      const unsigned vk = (8 * (i16 >> 2) + (lane & 3)) * 8192 + g4 * 16;
      const char* kb = (const char*)p.wuk + hf * 256 + dj * 128 + (size_t)c0 * 8192;
#pragma unroll
      for (int u = 0; u < NT / 2; ++u)
        asm volatile("s_cmp_eq_u32 %5, 0\n\ts_cbranch_scc1 1f\n\t"
                     "global_load_dwordx4 %0, %4, %6\n\tglobal_load_dwordx4 %1, %4, %6 offset:64\n\t"
                     "global_load_dwordx4 %2, %4, %7\n\tglobal_load_dwordx4 %3, %4, %7 offset:64\n1:"
                     : "=&v"(wukf[4 * u]), "=&v"(wukf[4 * u + 1]), "=&v"(wukf[4 * u + 2]), "=&v"(wukf[4 * u + 3])
                     : "v"(vk), "s"(fr), "s"(kb + 32 * u * 8192), "s"(kb + (32 * u + 4) * 8192) : "memory", "scc");
    }
    {
      MW();
      const v4u pz = {POISON, POISON, POISON, POISON};
      if (fr && i16 < 8) {
#pragma unroll
        for (int u = 0; u < NT / 2; ++u) st_sc1(rs, qb + u * 64, pz);
      }
      if (PZE == 2) {
        if (w >= 4) {
          if (PZ_DLY) __builtin_amdgcn_s_sleep(PZ_DLY);
          MW();
#pragma unroll
          for (int t = 0; t < 2; ++t) st_sc1(rs, WS_EIN + wi * 8192 + (w - 4) * 2048 + t * 1024 + lane * 16, pz);
#pragma unroll
          for (int t = 0; t < 8; ++t) st_sc1(rs, WS_CIN + wi * CIN_WI + (w - 4) * 8192 + t * 1024 + lane * 16, pz);
          VMWAIT(2 * NT + NT / 2 + 10);
        }
      } else {
      st_sc1(rs, WS_EIN + wi * 8192 + w * 1024 + lane * 16, pz);
      if (!PZE) {
#pragma unroll
        for (int t = 0; t < 4; ++t) st_sc1(rs, WS_CIN + wi * CIN_WI + w * 4096 + t * 1024 + lane * 16, pz);
      }
      }
    }
    if (w < 4) {
      asm volatile("s_waitcnt vmcnt(%3)\n\ts_cmp_lg_u32 %2, 0\n\ts_cbranch_scc1 1f\n\ts_waitcnt vmcnt(1)\n1:" : "+v"(xa), "+v"(xb) : "s"(fr), "i"(8 + 2 * NT + NT / 2 + 1 + 8) : "memory", "scc");
      MW();
      v2f v[8];
#pragma unroll
      for (int e = 0; e < 4; ++e) { v[e] = v2f{bf2f(xa[e]), bfhi(xa[e])}; v[4 + e] = v2f{bf2f(xb[e]), bfhi(xb[e])}; }
      const unsigned p0 = __builtin_amdgcn_readlane(__float_as_uint(v[0][0]), 0), p1 = __builtin_amdgcn_readlane(__float_as_uint(v[0][0]), 32);
      const float piv = __uint_as_float(hh ? p1 : p0);
      v2f sm = {0.f, 0.f}, ss = {0.f, 0.f};
#pragma unroll
      for (int e = 0; e < 8; ++e) {
        if (e == 3) { SB0(); MW(); }
        const v2f d = v[e] - v2f{piv, piv}; sm += d; ss += d * d;
      }
      SB0();
      MW();
      float t = rowsum(rs_add16(sm[0] + sm[1], ss[0] + ss[1]));
      auto sw2 = __builtin_amdgcn_permlane16_swap(__float_as_uint(t), __float_as_uint(t), false, false);
      const float md = __uint_as_float(sw2[0]) * (1.f / DM);
      const float var = fmaxf(fmaf(-md, md, __uint_as_float(sw2[1]) * (1.f / DM)), 0.f);
      const float mb = rbf(piv + md), vb = rbf(var);
      const float rden = __builtin_amdgcn_rcpf(rbf(__builtin_amdgcn_sqrtf(rbf(vb + 1e-5f))));
      SB0();
      MW();
      v4u o4[2];
#pragma unroll
      for (int e = 0; e < 8; ++e) {
        if (e == 3 || e == 6) { SB0(); MW(); }
        const v2f d = v[e] - v2f{mb, mb};
        const unsigned u = pk2(d[0], d[1]);
        const v2f o = v2f{bf2f(u), bfhi(u)} * v2f{rden, rden};
        o4[e >> 2][e & 3] = pk2(o[0], o[1]);
      }
      const unsigned xo = L_XN + (2 * w + hh) * 1040 + (lane & 31) * 16;
      lds<v4u>(xo) = o4[0];
      lds<v4u>(xo + 512) = o4[1];
      if (tid < C_N) lds<unsigned>(L_CNT + tid * 4) = 0u;
      if (PZE == 2) VMWAIT(2 * NT + NT / 2);
    }
    MW();
    const unsigned tag = GET_TAG();
    unsigned qa = L_QH + (lane & 7) * 144 + g4 * 16;
    {
      unsigned ta = L_WQ + (8 * g4 + (i16 >> 2)) * 128 + (((2 * w + ((lane & 3) >> 1)) ^ (((i16 >> 3) | ((g4 & 1) << 1)) << 1)) * 16) + 8 * (lane & 1);
      unsigned xq = L_XN + (lane & 7) * 1040 + g4 * 16;
      unsigned qw = L_QH + (lane & 7) * 144 + (16 * w + 4 * g4) * 2;
      PIN(ta); PIN(xq); PIN(qw); PIN(qa); PIN(qb);
      if (PZE != 2) VMWAIT(2 * NT + NT / 2 + 1 + (PZE ? 0 : 4));
      wg_barrier();
      MW();
      if (fr && w < 4) {
        f32x4 c0a = {0.f, 0.f, 0.f, 0.f}, c1a = c0a;
#pragma unroll
        for (int ks = 0; ks < 16; ks += 2) {
          if (ks == 4 || ks == 10) MW();
          const v4u x0 = lds128(xq + ks * 64), x1 = lds128(xq + ks * 64 + 64);
          const v2u a0 = trread(ta + ks * 4096), a1 = trread(ta + ks * 4096 + 512), b0 = trread(ta + ks * 4096 + 4096), b1 = trread(ta + ks * 4096 + 4608);
          c0a = mfma16(v4u{a0[0], a0[1], a1[0], a1[1]}, x0, c0a);
          c1a = mfma16(v4u{b0[0], b0[1], b1[0], b1[1]}, x1, c1a);
        }
        MW();
        c0a += c1a;
        lds<v2u>(qw) = v2u{pk2(c0a[0], c0a[1]), pk2(c0a[2], c0a[3])};
      }
    }
    MW();
    wg_barrier();
    {
      const v4u q0 = lds128(qa), q1 = lds128(qa + 64);
      if (NT == 4) asm volatile("s_waitcnt vmcnt(0)" : W8(wukf, 0)::"memory");
      else asm volatile("s_waitcnt vmcnt(0)" : "+v"(wukf[0]), "+v"(wukf[1]), "+v"(wukf[2]), "+v"(wukf[3])::"memory");
      const bool last = cnt_add_rtn(C_QST) == 7u;
      if (FLAG_FIRST && last) send_flags(rs, wi, lane, tag);
      if (fr) {
        const float sc = 0.08838834764831845f * LOG2E;
#pragma unroll
        for (int u = 0; u < NT / 2; ++u) {
          f32x4 d0 = mfma16(wukf[4 * u], q0, f32x4{0.f, 0.f, 0.f, 0.f}), d1 = mfma16(wukf[4 * u + 2], q0, f32x4{0.f, 0.f, 0.f, 0.f});
          d0 = mfma16(wukf[4 * u + 1], q1, d0);
          d1 = mfma16(wukf[4 * u + 3], q1, d1);
          d0 *= sc; d1 *= sc;
          if (i16 < 8) st_sc1(rs, qb + u * 64, v4u{san(pk2(d0[0], d0[1])), pk2(d0[2], d0[3]), pk2(d1[0], d1[1]), pk2(d1[2], d1[3])});
        }
      }
      MW();
      if (!FLAG_FIRST && last) send_flags(rs, wi, lane, tag);
      if (last) cnt_add(C_SENT);
      if (KPF) {
        if (KPF_AT) wait_until(t0, KPF_AT);
        if (KPF_S) cnt_wait<1>(C_SENT, 1);
        if (KPF == 1) {
          const unsigned ko = (unsigned)(xcd * 16 + idx) * 262144u + 196608u + w * 8192 + lane * 128;
          asm volatile("global_load_dword %0, %1, %2" : "=&v"(dpf) : "v"(ko), "s"(p.ckv) : "memory");
        } else {
          const unsigned ko = min((unsigned)lane * 131072u + (w & 1) * 65536u + idx * 4096u, WS_END - 4u);
          asm volatile("s_cmp_gt_u32 %3, %4\n\ts_cbranch_scc1 1f\n\tglobal_load_dword %0, %1, %2 sc1\n1:" : "=&v"(dpf) : "v"(ko), "s"(p.ws), "s"(w), "i"(KPF - 3) : "memory", "scc");
        }
      }
      if (FPF == 3 && xcd < 6) {
        cnt_wait<1>(C_SENT, 1);
        ffn_pf(dpf);
        asm volatile("s_waitcnt vmcnt(0)" : "+v"(dpf)::"memory");
      }
    }
    if (w == 4 && (T_O == 0 || T_F == 0 || T_UV == 1)) {
      v2u f0, f1, f2;
      MW();
      wait_until(t0, POLL_START);
      poll_flags(p.ws, WS_FLAG + (128 + wi) * 1024 + fpos<FLW>(lane), tag, f0, f1, f2);
      MW();
      if (GO_DLY) wait_until(now32(), GO_DLY);
      cnt_add(C_GO);
      asm volatile("s_waitcnt vmcnt(0)" : "+v"(f0), "+v"(f1), "+v"(f2)::"memory");
    }
    MW();
    v4u wof[4][8];
    f32x4 oc[8];
    if (w >= 4) {
      const int hk = w - 4;
      if (T_O) { wait_until(t0, T_O); cnt_wait<1>(C_SENT, 1); } else cnt_wait<2>(C_GO, 1);
      v4u wor[4][8], wdr[8], wdf[8];
      {
        const unsigned vo = 8 * g4 * 1024 + i16 * 16, vd = 8 * (g4 & 1) * 1024 + i16 * 16;
        const char* so = (const char*)p.wo + (size_t)(h * 128) * 1024 + hk * 256;
        const char* sd = (const char*)p.wd + (size_t)(ffn ? 16 * wi : 0) * 1024 + hk * 256;
#pragma unroll
        for (int n = 0; n < 40; ++n) {
          if (n < 8) asm volatile("global_load_dwordx4 %0, %1, %2 nt" : "=v"(wdr[n]) : "v"(vd), "s"(sd + n * 1024) : "memory");
          else asm volatile("global_load_dwordx4 %0, %1, %2" : "=v"(wor[(n - 8) >> 3][n & 7]) : "v"(vo), "s"(so + (32 * ((((n - 8) >> 3) + j) & 3) + (n & 7)) * 1024) : "memory");
          if (n >= NFW - 1 && n < 39) VMWAIT(NFW - 1);
          if ((n & 7) == 0) MW();
        }
      }
      asm volatile("s_waitcnt vmcnt(32)" : W8(wdr, 0), "+v"(dpf)::"memory");
      transpose8(wdr, wdf);
      asm volatile("s_waitcnt vmcnt(24)" : W8(wor[0], 0)::"memory");
      transpose8(wor[0], wof[0]);
      asm volatile("s_waitcnt vmcnt(16)" : W8(wor[1], 0)::"memory");
      transpose8(wor[1], wof[1]);
      asm volatile("s_waitcnt vmcnt(8)" : W8(wor[2], 0)::"memory");
      transpose8(wor[2], wof[2]);
      asm volatile("s_waitcnt vmcnt(0)" : W8(wor[3], 0)::"memory");
      transpose8(wor[3], wof[3]);
#pragma unroll
      for (int jj = 0; jj < 8; ++jj) oc[jj] = f32x4{0.f, 0.f, 0.f, 0.f};
      if (ffn) {
        MW();
        cnt_wait<1>(C_FFN, 1);
        const v4u a = lds128(L_HF + ((lane & 7) ^ ((lane & 8) >> 2)) * 80 + g4 * 16);
#pragma unroll
        for (int jj = 0; jj < 8; ++jj) oc[jj] = mfma16(a, wdf[jj], oc[jj]);
      }
#pragma unroll
      for (int u = 0; u < 4; ++u) asm volatile("" : W8(wof[u], 0));
      asm volatile("" : W8(oc, 0));
    } else {
      MW();
      if (T_F) { wait_until(t0, T_F); cnt_wait<1>(C_SENT, 1); } else cnt_wait<2>(C_GO, 1);
      if (h < 22) {
        const char* src = (const char*)(((i16 >> 3) ^ (g4 >> 1)) ? p.wu : p.wg) + (size_t)(128 * j + 32 * w + g4) * (FF * 2) + h * 128;
#pragma unroll
        for (int t = 0; t < 8; ++t) {
          glds16<0>(src + (size_t)t * 4 * (FF * 2) + min((i16 ^ fswz(4 * t + g4)) & 7u, h == 21 ? 3u : 7u) * 16, L_GU + (32 * w + 4 * t) * 256);
          if (t & 1) MW();
        }
      }
      auto ld_uv = [&]() {
#pragma unroll
        for (int t = 0; t < 8; ++t) {
          const unsigned row = 32 * w + 4 * t + g4;
          if ((t & 1) == 0) MW();
          glds16<1>((const char*)p.wuv + (size_t)(128 * j + row) * 8192 + h * 256 + ((i16 ^ fswz(row)) * 16), L_WUV + (32 * w + 4 * t) * 256);
        }
      };
      if (!T_UV) ld_uv();
      if (h < 22) {
        if (T_UV) VMWAIT(0); else VMWAIT(8);
        MW();
        cnt_add(C_GU);
        cnt_wait(C_GU, 4);
        f32x4 ag = {0.f, 0.f, 0.f, 0.f}, au = {0.f, 0.f, 0.f, 0.f};
        const unsigned ra = 8 * g4 + (i16 >> 2), ch = 2 * w + ((lane & 3) >> 1);
        const unsigned r0 = L_GU + ra * 256 + ((ch ^ fswz(ra)) * 16) + 8 * (lane & 1), r1 = L_GU + (ra + 4) * 256 + ((ch ^ fswz(ra + 4)) * 16) + 8 * (lane & 1);
        MW();
#pragma unroll
        for (int ks = 0; ks < 4; ++ks) {
          const v4u a = lds128(L_XN + (lane & 7) * 1040 + (128 * j + 32 * ks + 8 * g4) * 2);
          const v2u b0 = trread(r0 + ks * 8192), b1 = trread(r1 + ks * 8192);
          const v2u c0 = trread((r0 ^ 128) + ks * 8192), c1 = trread((r1 ^ 128) + ks * 8192);
          if (ks == 2) MW();
          ag = mfma16(a, v4u{b0[0], b0[1], b1[0], b1[1]}, ag);
          au = mfma16(a, v4u{c0[0], c0[1], c1[0], c1[1]}, au);
        }
        MW();
        const v4u pv = __builtin_bit_cast(v4u, hh ? au : ag);
        const unsigned fo = WS_FIN + (4 * h + w) * 8192 + j * 2048 + lane * 16;
        st_sc1(rs, fo, v4u{pv[0], pv[1], pv[2], tag});
        st_sc1(rs, fo + 1024, v4u{pv[3], pv[3], pv[3], tag});
      }
      if (T_UV) {
        if (T_UV > 1) wait_until(t0, T_UV); else cnt_wait<2>(C_GO, 1);
        ld_uv();
      }
      asm volatile("s_waitcnt vmcnt(0)" : "+v"(dpf)::"memory");
      cnt_add(C_UV);
      if (ffn && w == 1) {
        v4u fv[8];
#pragma unroll 1
        for (unsigned spin = 0; spin < SPIN_CAP; ++spin) {
          MW();
          unsigned bad = 0;
#pragma unroll
          for (int t = 0; t < 8; ++t) fv[t] = ld_sc1(rs, WS_FIN + wi * 8192 + t * 1024 + lane * 16);
#pragma unroll
          for (int t = 0; t < 8; ++t) bad |= fv[t][3] ^ tag;
          if (!__any(bad != 0u)) break;
          __builtin_amdgcn_s_sleep(FX_SLP);
          CFENCE();
        }
        MW();
        float sv[4];
#pragma unroll
        for (int e = 0; e < 4; ++e) {
          const int o = e == 3, q = e % 3;
          sv[e] = (__uint_as_float(fv[o][q]) + __uint_as_float(fv[2 + o][q])) + (__uint_as_float(fv[4 + o][q]) + __uint_as_float(fv[6 + o][q]));
        }
#pragma unroll
        for (int e = 0; e < 2; ++e) {
          SB0();
          MW();
          const auto gu = __builtin_amdgcn_permlane32_swap(__float_as_uint(sv[e]), __float_as_uint(sv[e + 2]), false, false);
          const float gg = rbf(__uint_as_float(gu[0])), uu = rbf(__uint_as_float(gu[1]));
          const float inner = 0.7978845608028654f * (gg + 0.044715f * gg * gg * gg);
          const float th = 1.f - 2.f / (__expf(2.f * inner) + 1.f);
          SB0();
          MW();
          const float ge = rbf(0.5f * gg * (1.f + th));
          lds<u16>(L_HF + (4 * (g4 & 1) + 2 * hh + e) * 80 + i16 * 2) = (u16)(pk2(ge * uu, 0.f) & 0xFFFFu);
        }
        if (lane < 16) lds<v4u>(L_HF + (lane >> 1) * 80 + 32 + (lane & 1) * 16) = v4u{0u, 0u, 0u, 0u};
        MW();
        cnt_add(C_FFN);
      }
    }
    v4u uvf[4];
    MW();
    cnt_wait<2>(C_UV, 4);
#pragma unroll
    for (int ks = 0; ks < 4; ++ks) {
      const unsigned ra = 32 * (2 * (ks & 1) + (g4 >> 1)) + 16 * (ks >> 1) + 8 * (g4 & 1) + (i16 >> 2), rb = ra + 4, ch = 2 * w + ((lane & 3) >> 1);
      const v2u b0 = trread(L_WUV + ra * 256 + ((ch ^ fswz(ra)) * 16) + 8 * (lane & 1));
      const v2u b1 = trread(L_WUV + rb * 256 + ((ch ^ fswz(rb)) * 16) + 8 * (lane & 1));
      uvf[ks] = v4u{b0[0], b0[1], b1[0], b1[1]};
      PIN(uvf[ks]);
      MW();
    }
    unsigned cx = WS_CIN + wi * CIN_WI + (w ^ (CSW ? h & 7 : 0)) * 4096 + lane * 16;
    unsigned clx = L_CL + w * 272 + (((i16 >> 2) & 1) * 32 + (i16 >> 3) * 16 + (i16 & 3) * 4 + g4) * 4;
    PIN(cx); PIN(clx);
    MW();
    unsigned cla = L_CL + (lane & 7) * 272 + g4 * 16;
    unsigned cxw = L_CTX + (lane & 7) * 272 + (16 * w + 4 * g4) * 2;
    PIN(cla); PIN(cxw);
    MW();
    unsigned cxa[4];
#pragma unroll
    for (int u = 0; u < 4; ++u) { cxa[u] = L_CTX + ((lane & 7) ^ ((lane & 8) >> 2)) * 272 + (32 * ((u + j) & 3) + 8 * g4) * 2; PIN(cxa[u]); }
    unsigned eb = w >= 4 ? WS_EIN + ((4 * (g4 & 1) + 2 * hh) * 16 + 4 * (w - 4) + (i16 >> 2)) * 8192 + (EMAP ? (wi & 15) * 8 + (wi >> 4) : wi) * 64 + (i16 & 3) * 16 : WS_EIN + wi * 8192 + lane * 16;
    unsigned f2a = WS_FLAG2 + (64 * (w & 1) + lane) * 1024 + fpos<FLW2>(wi >> 1) + (wi & 1) * 4;
    PIN(f2a);
    MW();
    unsigned msk[4];
#pragma unroll
    for (int q = 0; q < 4; ++q) { msk[q] = (i16 >> 1) == 2 * q ? ((i16 & 1) ? 0xFFFF0000u : 0xFFFFu) : 0u; PIN(msk[q]); }
    MW();
    PIN(cx); PIN(clx); PIN(cla); PIN(cxw); PIN(eb);
    {
      v2u ml;
      v4u dv[4];
      const unsigned ma = WS_ML + (wi * 128 + w * 16) * 16 + (lane & 31) * 8;
      const v2u zz = {0u, 0u};
      unsigned pend = 15u;
#pragma unroll 1
      for (unsigned spin = 0; spin < SPIN_CAP; ++spin) {
        asm volatile("global_atomic_add_x2 %0, %1, %2, %3 sc0 sc1\n\ts_waitcnt vmcnt(0)" : "=&v"(ml) : "v"(ma), "v"(zz), "s"(p.ws) : "memory");
        const unsigned okm = (unsigned)__ballot(ml[1] == tag);
#pragma unroll
        for (int t = 0; t < 4; ++t) {
          if ((pend >> t & 1u) && ((okm >> (8 * t)) & 255u) == 255u) {
            dv[t] = ld_sc1(rs, cx + t * 1024);
            pend &= ~(1u << t);
            if (t & 1) MW();
          }
        }
        if (pend == 0) break;
      }
      MW();
      v4u aw[4];
      {
        const float o = xor1(__uint_as_float(ml[0]));
        const float ms = (lane & 1) ? o : __uint_as_float(ml[0]), lsv = (lane & 1) ? __uint_as_float(ml[0]) : o;
        float M = rowmax(ms);
        M = rs_max16(M, M);
        const float ee = ex2(ms - M);
        const float wsv = 2.f * ee / add16(rowsum(ee * lsv));
        unsigned wb = pk2(wsv, wsv);
        PIN(wb);
        MW();
#pragma unroll
        for (int t = 0; t < 4; ++t) {
          const unsigned u = __shfl(wb, 8 * t + 2 * g4);
          aw[t] = v4u{u & msk[0], u & msk[1], u & msk[2], u & msk[3]};
          PIN(aw[t]);
          if (t & 1) MW();
        }
      }
#pragma unroll 1
      for (unsigned spin = 0; spin < SPIN_CAP; ++spin) {
        bool ok = true;
        bool bad[4];
#pragma unroll
        for (int t = 0; t < 4; ++t) { bad[t] = __any(dv[t][0] == POISON); ok = ok && !bad[t]; }
        if (ok) break;
        CFENCE();
#pragma unroll
        for (int t = 0; t < 4; ++t)
          if (bad[t]) dv[t] = ld_sc1(rs, cx + t * 1024);
      }
      MW();
      f32x4 dx = {0.f, 0.f, 0.f, 0.f}, dy = dx;
#pragma unroll
      for (int t = 0; t < 2; ++t) { dx = mfma16(aw[t], dv[t], dx); dy = mfma16(aw[t + 2], dv[t + 2], dy); }
      lds<unsigned>(clx) = pk2(dx[0] + dy[0], dx[1] + dy[1]);
    }
    wg_barrier();
    MW();
    if (F2AT == 0 && (w == 2 || w == 3)) st_sc1_32(rs, f2a, tag);
    {
      v4u a[4];
#pragma unroll
      for (int ks = 0; ks < 4; ++ks) a[ks] = lds128(cla + ks * 64);
      SB0();
      f32x4 c0 = mfma16(uvf[0], a[0], f32x4{0.f, 0.f, 0.f, 0.f}), c1 = mfma16(uvf[1], a[1], f32x4{0.f, 0.f, 0.f, 0.f});
      c0 = mfma16(uvf[2], a[2], c0);
      c1 = mfma16(uvf[3], a[3], c1);
      c0 += c1;
      lds<v2u>(cxw) = v2u{pk2(c0[0], c0[1]), pk2(c0[2], c0[3])};
    }
    wg_barrier();
    MW();
    if (F2AT == 1 && (w == 2 || w == 3)) st_sc1_32(rs, f2a, tag);
    if (w >= 4) {
      v4u a[4];
#pragma unroll
      for (int u = 0; u < 4; ++u) a[u] = lds128(cxa[u]);
      SB0();
#pragma unroll
      for (int u = 0; u < 4; ++u) {
#pragma unroll
        for (int jj = 0; jj < 8; ++jj) oc[jj] = mfma16(a[u], wof[u][jj], oc[jj]);
        if (u & 1) { SB0(); MW(); }
      }
#pragma unroll
      for (int e = 0; e < 2; ++e)
        __builtin_amdgcn_raw_buffer_store_b128(v4u{san(pk2(oc[0][e], oc[1][e])), san(pk2(oc[2][e], oc[3][e])), san(pk2(oc[4][e], oc[5][e])), san(pk2(oc[6][e], oc[7][e]))},
                                               rs, eb, e * 16 * 8192, 16);
    } else if (w == 0) {
      const unsigned oo = wi * 64 + (i16 & 3) * 16 + g4 * 4;
      const unsigned xf = *(const GAS unsigned*)((const char*)p.x + oo);
      const unsigned one = (i16 & 2) ? 0u : ((i16 & 1) ? 0x3F800000u : 0x3F80u);
      v4u sel;
#pragma unroll
      for (int q = 0; q < 4; ++q) sel[q] = (i16 >> 2) == q ? one : 0u;
      v2u f0, f1, f2;
      MW();
      poll_flags(p.ws, WS_FLAG2 + wi * 1024 + fpos<FLW2>(lane), tag, f0, f1, f2);
      MW();
      if (E_DLY) __builtin_amdgcn_s_sleep(E_DLY);
      float r0, r1;
#pragma unroll 1
      for (unsigned spin = 0; spin < SPIN_CAP; ++spin) {
        v4u ev[8];
#pragma unroll
        for (int t = 0; t < 8; ++t) ev[t] = ld_sc1(rs, eb + t * 1024);
        f32x4 d0 = {0.f, 0.f, 0.f, 0.f}, d1 = d0;
#pragma unroll
        for (int t = 0; t < 4; ++t) { d0 = mfma16(sel, ev[2 * t], d0); d1 = mfma16(sel, ev[2 * t + 1], d1); }
        MW();
        r0 = d0[0] + d1[0]; r1 = d0[1] + d1[1];
        r0 += ror16<8>(r0); r1 += ror16<8>(r1);
        r0 += ror16<4>(r0); r1 += ror16<4>(r1);
        if (!__any(r0 != r0)) break;
        unsigned mxv = 0;
#pragma unroll
        for (int t = 0; t < 8; ++t) mxv = max(mxv, max(max(ev[t][0], ev[t][1]), max(ev[t][2], ev[t][3])));
        if (__all(mxv != POISON)) break;
        CFENCE();
      }
      MW();
      if (i16 < 4) *(GAS unsigned*)((char*)p.out + oo) = pk2(bf2f(xf) + r0, bfhi(xf) + r1);
      asm volatile("s_waitcnt vmcnt(0)" : "+v"(f0), "+v"(f1), "+v"(f2)::"memory");
    } else if (w == 2) {
      if (wi == 0 && lane == 0) st_sc1_32(rs, WS_GEN, gen1);
    }
  }
}

extern "C" void kernel_launch(void* const* d_in, const int* in_sizes, int n_in, void* d_out, int out_size, void* d_ws,
                              size_t ws_size, hipStream_t stream) {
  Params p;
  p.x = (const u16*)d_in[0];
  p.ckv = (const u16*)d_in[1];
  p.wq = (const u16*)d_in[2];
  p.wuk = (const u16*)d_in[3];
  p.wuv = (const u16*)d_in[4];
  p.wo = (const u16*)d_in[5];
  p.wg = (const u16*)d_in[7];
  p.wu = (const u16*)d_in[8];
  p.wd = (const u16*)d_in[9];
  p.out = (u16*)d_out;
  p.ws = (unsigned char*)d_ws;
  (void)hipFuncSetAttribute((const void*)fused_kernel, hipFuncAttributeMaxDynamicSharedMemorySize, LDS_BYTES);
  fused_kernel<<<256, 512, LDS_BYTES, stream>>>(p);
}
```

```cpp
#include <hip/hip_runtime.h>
#include <stdint.h>

typedef unsigned short u16;
typedef unsigned long long u64;
typedef unsigned v4u __attribute__((ext_vector_type(4)));
typedef unsigned v2u __attribute__((ext_vector_type(2)));
typedef float f32x4 __attribute__((ext_vector_type(4)));
typedef float f32x16 __attribute__((ext_vector_type(16)));
typedef float v2f __attribute__((ext_vector_type(2)));
typedef short bf16x8 __attribute__((ext_vector_type(8)));
typedef short v4s __attribute__((ext_vector_type(4)));
typedef __bf16 v2bf __attribute__((ext_vector_type(2)));
#define GAS __attribute__((address_space(1)))
#define LAS __attribute__((address_space(3)))
#define DI __device__ __forceinline__

#define NF 12
#define R2AT (47 + NF)
#define POLL_START 250
#define POLL_GAP 4
#define Q_DLY 0
#define KV_DLY 50
#define E_DLY 10
#define T_F 800
#define T_UV 0
#define T_O 780
#define MLAT 17
#define FR4 1
#define PAUSE 1
#define QPF 0
#define WPF 2
#define BPF 0
#define BPF_DLY 0
#define WPF_DLY 0
#define FMAP 0
#define OD 6
#define V3 4
#define QPRIO 1
#define PK 8
#define WM_DLY 110
#define FPF 0
#define FLAG_AT 1
#define FL_DLY 0
#define PZ_DLY 0
#define B0W 2
#define NF2 16
#define PMIN 32
#define VD 0
#define MIDSIG 1
#define V2AT 0
#define CKAT 10
#define PPRIO 0
#define SPAT 13
#define MIX 0
#define PROG 1
#define RES 1
#define XAT 2
#define PUTAT 3
#define FLAG_FIRST 0
#define F2AT 0
#define GO_DLY 100
#define C_DLY 0
#define KPF 1
#define KPF_AT 600
#define KPF_S 1
#define FX_SLP 8
#define OSAT 11
#define PZE 0
#define PZT 0
#define PZ_AT 150
#define K1AT 1
#define CSW 1
#define FLW 0
#define FLW2 0
#define BMAP 0
#define EMAP 0
#define G0T 1
#define KPF_ST 20
#define K3S 1
#define QST 0
#define F2D 3
#define F2G 4
#define F2X 1
#define QPM 1
#define MLDP 1
#define MLP 1
#define MLG 10
#define NFW 40

constexpr int DM = 512, FF = 1376;
constexpr unsigned WS_GEN = 0;
constexpr unsigned WS_FLAG = 4096;
constexpr unsigned WS_FLAG2 = WS_FLAG + 262144;
constexpr unsigned WS_QABS = WS_FLAG2 + 131072;
constexpr unsigned WS_ML = WS_QABS + 524288;
constexpr unsigned WS_EIN = WS_ML + 262144;
constexpr unsigned WS_CIN = WS_EIN + 1048576;
constexpr unsigned CIN_ROW = 256, CIN_WI = 128 * CIN_ROW;
constexpr unsigned WS_FIN = WS_CIN + 128 * CIN_WI;
constexpr unsigned WS_END = WS_FIN + 88 * 8192;
constexpr unsigned L_Q = 0;
constexpr unsigned L_P = L_Q;
constexpr unsigned L_LS = L_Q + 24576;
constexpr unsigned L_AL = L_Q + 25600;
constexpr unsigned L_SLOT = 32768;
constexpr unsigned L_XN = 0;
constexpr unsigned L_QH = 8448;
constexpr unsigned L_CL = L_QH + 2176;
constexpr unsigned L_CTX = L_CL + 2176;
constexpr unsigned L_HF = L_CTX + 2176;
constexpr unsigned L_FP = L_HF + 640;
constexpr unsigned L_WQ = 32768;
constexpr unsigned L_GU = 65536;
constexpr unsigned L_WUV = 98304;
constexpr unsigned L_CNT = 32512;
constexpr unsigned LDS_BYTES = 163840;
enum { C_P = 0, C_OVF = 1, C_ST = 2, C_FL = 3, C_MX = 5, C_PVD, C_XS, C_MASK = 8, C_N = 16 };
enum { C_QST = 0, C_GU, C_FFN, C_UV, C_GO, C_SENT };

constexpr unsigned ANY = 0x80u;
constexpr unsigned POISON = 0xFFFFFFFFu;
constexpr unsigned SPIN_CAP = 1u << 20;

struct Params {
  const u16 *x, *ckv, *wq, *wuk, *wuv, *wo, *wg, *wu, *wd;
  u16* out;
  unsigned char* ws;
};

DI float bf2f(unsigned v) { return __uint_as_float(v << 16); }
DI float bfhi(unsigned v) { return __uint_as_float(v & 0xFFFF0000u); }
DI unsigned pk2(float a, float b) {
  v2f t = {a, b};
  return __builtin_bit_cast(unsigned, __builtin_convertvector(t, v2bf));
}
DI float rbf(float a) { return bf2f(pk2(a, 0.f) & 0xFFFFu); }
DI unsigned san(unsigned v) { return min(v, 0xFFFFFFFEu); }
DI f32x4 mfma16(v4u a, v4u b, f32x4 c) {
  return __builtin_amdgcn_mfma_f32_16x16x32_bf16(__builtin_bit_cast(bf16x8, a), __builtin_bit_cast(bf16x8, b), c, 0, 0, 0);
}
DI f32x16 mfma32(v4u a, v4u b, f32x16 c) {
  return __builtin_amdgcn_mfma_f32_32x32x16_bf16(__builtin_bit_cast(bf16x8, a), __builtin_bit_cast(bf16x8, b), c, 0, 0, 0);
}
DI v4u lds128(unsigned a) { return *(LAS v4u*)(size_t)a; }
DI v2u trread(unsigned a) { return __builtin_bit_cast(v2u, __builtin_amdgcn_ds_read_tr16_b64_v4i16((LAS v4s*)(size_t)a)); }
template <class T> DI LAS T& lds(unsigned a) { return *(LAS T*)(size_t)a; }
template <int POL> DI void glds16(const void* gsrc, unsigned lds_dst) {
  unsigned keep;
  if (POL == 1) asm volatile("s_mov_b32 %0, m0\n\ts_mov_b32 m0, %2\n\ts_nop 0\n\tglobal_load_lds_dwordx4 %1, off nt\n\ts_mov_b32 m0, %0" : "=&s"(keep) : "v"(gsrc), "s"(lds_dst) : "memory");
  else if (POL == 2) asm volatile("s_mov_b32 %0, m0\n\ts_mov_b32 m0, %2\n\ts_nop 0\n\tglobal_load_lds_dwordx4 %1, off sc1\n\ts_mov_b32 m0, %0" : "=&s"(keep) : "v"(gsrc), "s"(lds_dst) : "memory");
  else asm volatile("s_mov_b32 %0, m0\n\ts_mov_b32 m0, %2\n\ts_nop 0\n\tglobal_load_lds_dwordx4 %1, off\n\ts_mov_b32 m0, %0" : "=&s"(keep) : "v"(gsrc), "s"(lds_dst) : "memory");
}
DI void glds16s(unsigned voff, const void* sbase, unsigned lds_dst) {
  unsigned keep;
  asm volatile("s_mov_b32 %0, m0\n\ts_mov_b32 m0, %3\n\ts_nop 0\n\tglobal_load_lds_dwordx4 %1, %2 nt\n\ts_mov_b32 m0, %0" : "=&s"(keep) : "v"(voff), "s"(sbase), "s"(lds_dst) : "memory");
}
DI void gld16s(v4u& d, unsigned voff, const void* sbase) {
  asm volatile("global_load_dwordx4 %0, %1, %2 nt" : "=v"(d) : "v"(voff), "s"(sbase) : "memory");
}
#define VMWAIT(n) asm volatile("s_waitcnt vmcnt(%0)" ::"i"(n) : "memory")
#define LGKM0() asm volatile("s_waitcnt lgkmcnt(0)" ::: "memory")
#define CFENCE() asm volatile("" ::: "memory")
#define SB0() __builtin_amdgcn_sched_barrier(0)
#define W8(a, o) "+v"(a[o]), "+v"(a[o + 1]), "+v"(a[o + 2]), "+v"(a[o + 3]), "+v"(a[o + 4]), "+v"(a[o + 5]), "+v"(a[o + 6]), "+v"(a[o + 7])
#define WARMUP 1
#define MK(n) asm volatile("7:\n\ts_cbranch_execz .Lwm_ret\n\t.subsection " #n "\n\ts_branch 7b\n\t.subsection 0")
#define MK_TABLE() asm volatile(".subsection 1\n.Lwm_ret:\n\ts_setpc_b64 s[96:97]\n.Lwm_w:\n\t.subsection 3\n.Lwm_q:\n\t.subsection 5\n.Lwm_p:\n\t" \
                                ".subsection 7\n.Lwm_s:\n\t.subsection 9\n.Lwm_e:\n\t.fill 128, 4, 0xBF810000\n\t.subsection 0")
#define WARM(A, B, first, step) do { u64 keep_; unsigned off_ = (first) * 4u; \
  asm volatile("s_mov_b64 %0, exec\n\ts_mov_b64 exec, 0\n\ts_getpc_b64 s[96:97]\n8:\n\t" \
               "s_cmp_ge_u32 %1, .Lwm_" #B "-.Lwm_" #A "\n\ts_cbranch_scc1 9f\n\t" \
               "s_add_u32 s94, %1, .Lwm_" #A "-8b\n\ts_add_u32 s94, s94, s96\n\ts_addc_u32 s95, s97, 0\n\t" \
               "s_add_u32 %1, %1, %2\n\ts_setpc_b64 s[94:95]\n9:\n\ts_mov_b64 exec, %0" \
               : "=&s"(keep_), "+s"(off_) : "i"((step) * 4) : "s94", "s95", "s96", "s97", "scc"); } while (0)

#define MW() MK(2)
#define MQ() MK(4)
#define MP() MK(6)
#define MS() MK(8)

DI void wg_barrier() {
  LGKM0();
  __builtin_amdgcn_s_barrier();
  CFENCE();
}
DI void cnt_addv(unsigned c, unsigned v) {
  asm volatile("s_mov_b64 exec, 1\n\tds_add_u32 %0, %1 offset:%2\n\ts_mov_b64 exec, -1" ::"v"(0u), "v"(v), "i"(L_CNT + c * 4u) : "memory");
}
DI void cnt_add(unsigned c) { cnt_addv(c, 1u); }
DI void cnt_add2(unsigned c, unsigned v0, unsigned v1) {
  asm volatile("s_mov_b64 exec, 1\n\tds_add_u64 %0, %1 offset:%2\n\ts_mov_b64 exec, -1" ::"v"(0u), "v"(v2u{v0, v1}), "i"(L_CNT + c * 4u) : "memory");
}
#define AI __attribute__((always_inline))
#define RARE(c) __builtin_expect(!!(c), 0)
DI unsigned vm_pending() {
  unsigned v;
  asm volatile("s_getreg_b32 %0, hwreg(HW_REG_IB_STS)" : "=s"(v));
  return (v & 15u) | ((v >> 18) & 48u);
}
#define STR_(x) #x
#define STR(x) STR_(x)
#define VW1(n) "s_waitcnt vmcnt(" #n ")\n\ts_branch 2f\n\t"
#define MKR "7:\n\ts_cbranch_execz .Lwm_ret\n\t.subsection 8\n\ts_branch 7b\n\t.subsection 10\n\t"
#define MKB "7:\n\ts_cbranch_execz .Lwm_ret\n\t.subsection 6\n\ts_branch 7b\n\t.subsection 11\n\t"
#define L_CNT_ 32512
#define KV_RUN_BODY() asm volatile(".subsection 10\n" \
  MKR ".Lkv_chk:\n\t" \
  "s_mov_b32 s67, 1\n\ts_mov_b32 s91, m0\n\ts_mov_b32 s90, 0xffffff\n\ts_branch 12f\n" \
  ".Lkv_runp:\n\t" \
  "s_mov_b32 s67, 1\n\ts_branch 11f\n" \
  ".Lkv_run:\n\t" \
  "s_mov_b32 s67, 2\n" \
  "11:\n\t" \
  "s_mov_b32 s91, m0\n\ts_mov_b32 s90, 0xffffff\n\ts_mov_b32 s64, -1\n" \
  "3:\n\t" \
  "v_mov_b32 v252, 0\n\t" \
  "ds_read_b128 v[252:255], v252 offset:%0\n\t" \
  "s_cmp_lg_u32 s67, 1\n\ts_cbranch_scc1 12f\n\t" \
  "v_add_u32 v249, s74, v250\n\t" \
  "s_bitcmp1_b32 s74, 12\n\ts_cbranch_scc1 14f\n\t" \
  "ds_read_b128 v[232:235], v249\n\tds_read_b128 v[236:239], v249 offset:1024\n\tds_read_b128 v[240:243], v249 offset:2048\n\tds_read_b128 v[244:247], v249 offset:3072\n\t" \
  "s_branch 12f\n" \
  "14:\n\t" \
  "ds_read_b128 v[216:219], v249\n\tds_read_b128 v[220:223], v249 offset:1024\n\tds_read_b128 v[224:227], v249 offset:2048\n\tds_read_b128 v[228:231], v249 offset:3072\n" \
  "12:\n\t" \
  "s_cmp_eq_u32 s77, 4\n\ts_cbranch_scc1 6f\n\t" \
  "s_getreg_b32 s87, hwreg(HW_REG_IB_STS)\n\t" \
  "s_lshl_b32 s89, s77, 4\n\ts_add_u32 s89, s89, 16\n\t" \
  "s_lshr_b32 s88, s87, 18\n\ts_and_b32 s88, s88, 48\n\ts_and_b32 s87, s87, 15\n\ts_or_b32 s87, s87, s88\n\t" \
  "s_add_u32 s89, s89, s87\n\ts_cmp_lt_u32 s76, s89\n\ts_cbranch_scc1 4f\n\t" \
  "s_min_u32 s88, s87, 23\n\ts_lshl_b32 s88, s88, 3\n\t" \
  "s_getpc_b64 s[94:95]\n0:\n\ts_add_u32 s88, s88, 1f-0b\n\ts_add_u32 s94, s94, s88\n\ts_addc_u32 s95, s95, 0\n\ts_setpc_b64 s[94:95]\n" \
  MKR "1:\n\t" \
  VW1(0) VW1(1) VW1(2) VW1(3) VW1(4) VW1(5) VW1(6) VW1(7) VW1(8) VW1(9) VW1(10) VW1(11) VW1(12) VW1(13) VW1(14) VW1(15) \
  VW1(16) VW1(17) VW1(18) VW1(19) VW1(20) VW1(21) VW1(22) VW1(23) \
  MKR "2:\n\t" \
  "s_lshl_b32 s88, s77, 2\n\ts_add_u32 s88, s88, s83\n\ts_lshl_b32 s88, 1, s88\n\t" \
  "v_mov_b32 v248, 0\n\tv_mov_b32 v249, s88\n\ts_mov_b64 exec, 1\n\tds_add_u32 v248, v249 offset:%1\n\ts_mov_b64 exec, -1\n\t" \
  "s_add_u32 s77, s77, 1\n" \
  "4:\n\t" \
    \
  "s_lshr_b32 s89, s86, s83\n\ts_and_b32 s89, s89, 0x11000000\n\ts_and_b32 s88, s86, 0x440000\n\ts_or_b32 s89, s89, s88\n\t" \
  "s_cmp_eq_u32 s89, 0x11440000\n\ts_cselect_b32 s88, 64, 48\n\t" \
  "s_and_b32 s89, s89, 0x1040000\n\ts_cmp_eq_u32 s89, 0x1040000\n\ts_cselect_b32 s88, s88, 32\n\t" \
  "s_and_b32 s89, s75, %2 | 0x100\n\ts_cmp_eq_u32 s89, 0x100\n\ts_cselect_b32 s89, " STR(PMIN) ", 64\n\ts_min_u32 s88, s88, s89\n\t" \
  "s_and_b32 s89, s75, %2\n\ts_cselect_b32 s89, " STR(NF2) ", " STR(NF) "\n\t" \
  "s_add_u32 s89, s89, s76\n\ts_sub_u32 s89, s89, s87\n\ts_min_u32 s88, s88, s89\n\t" \
  "s_cmp_ge_i32 s76, s88\n\ts_cbranch_scc1 6f\n" \
  "5:\n\t" \
  "v_readlane_b32 s89, v251, s76\n\ts_add_u32 s76, s76, 1\n\ts_and_b32 s87, s89, 0x1fc00\n\tv_xor_b32 v249, s89, v250\n\t" \
  "s_add_u32 m0, s87, s82\n\ts_cmp_lt_u32 s76, s88\n\tglobal_load_lds_dwordx4 v249, s[80:81] nt\n\ts_cbranch_scc1 5b\n\t" \
  "6:\n\t" MKR \
  "s_waitcnt lgkmcnt(0)\n\t" \
  "v_readfirstlane_b32 s85, v252\n\tv_readfirstlane_b32 s86, v254\n\tv_readfirstlane_b32 s84, v253\n\tv_readfirstlane_b32 s75, v255\n\t" \
  "s_bitset1_b32 s85, 7\n\ts_and_b32 s89, s86, s78\n\ts_cmp_eq_u32 s89, s78\n\ts_cbranch_scc0 9f\n\ts_and_b32 s89, s85, s79\n\ts_cbranch_scc0 9f\n\t" \
  "s_cmp_lg_u32 s67, 0\n\ts_cbranch_scc1 10f\n" \
  "9:\n\ts_cmp_eq_u32 s67, 2\n\ts_cbranch_scc1 13f\n\ts_and_b32 s89, s85, s68\n\ts_cselect_b32 s67, 1, 0\n" \
  "13:\n\ts_mov_b32 s89, s79\n\ts_cmp_eq_u32 s76, s64\n\ts_cbranch_scc1 17f\n\ts_sub_u32 s90, s90, 1\n\ts_cbranch_scc0 3b\n" \
  "10:\n\ts_mov_b32 s64, 0\n" \
  "17:\n\ts_mov_b32 m0, s91\n\ts_mov_b32 s87, s64\n" \
  "15:\n\ts_cmp_eq_u32 s77, 2\n\ts_cselect_b32 s66, 0, 15\n\ts_or_b32 s66, s66, 0xc00000\n\t" \
  "s_lshr_b32 s65, s77, 2\n\ts_cmp_eq_u32 s76, 64\n\ts_cselect_b32 s65, s65, 1\n\ts_lshl_b32 s65, s65, 8\n\ts_cmp_eq_u32 s87, 0\n\ts_setpc_b64 s[92:93]\n" \
  MKR ".Lkv_sig:\n\t" \
  "s_waitcnt vmcnt(16)\n\ts_cmp_eq_u32 s77, 2\n\ts_cbranch_scc1 16f\n\ts_waitcnt vmcnt(0)\n" \
  "16:\n\ts_lshl_b32 s88, s77, 2\n\ts_add_u32 s88, s88, s83\n\ts_lshl_b32 s88, 1, s88\n\t" \
  "v_mov_b32 v248, 0\n\tv_mov_b32 v249, s88\n\ts_mov_b64 exec, 1\n\tds_add_u32 v248, v249 offset:%1\n\ts_mov_b64 exec, -1\n\t" \
  "s_add_u32 s77, s77, 1\n\ts_mov_b32 s87, 0\n\ts_branch 15b\n\t.subsection 12\n\t.fill 64, 4, 0xBF810000\n\t.subsection 0" \
  :: "i"(L_CNT), "i"(L_CNT + C_ST * 4), "i"(RES ? 4 : 0x40))
DI unsigned cnt_add_rtn(unsigned c) {
  u64 keep;
  unsigned v;
  asm volatile("s_mov_b64 %0, exec\n\ts_mov_b64 exec, 1\n\tds_add_rtn_u32 %1, %2, %3 offset:%4\n\ts_waitcnt lgkmcnt(0)\n\ts_mov_b64 exec, %0"
               : "=&s"(keep), "=&v"(v) : "v"(c * 4u), "v"(1u), "i"(L_CNT) : "memory");
  return __builtin_amdgcn_readfirstlane(v);
}
DI unsigned cnt_get(unsigned c) {
  unsigned v;
  asm volatile("ds_read_b32 %0, %1 offset:%2\n\ts_waitcnt lgkmcnt(0)" : "=v"(v) : "v"(c * 4u), "i"(L_CNT) : "memory");
  return __builtin_amdgcn_readfirstlane(v);
}
template <int SLP = 0> DI void cnt_wait(unsigned c, unsigned target) {
#pragma unroll 1
  for (unsigned s = 0; s < SPIN_CAP * 16u; ++s) {
    if (cnt_get(c) >= target) break;
    if (SLP) __builtin_amdgcn_s_sleep(SLP);
  }
}
DI unsigned now32() { return (unsigned)__builtin_amdgcn_s_memrealtime(); }
DI void wait_until(unsigned t0, unsigned ticks) {
  if (ticks == 0) return;
#pragma unroll 1
  while (now32() - t0 < ticks) __builtin_amdgcn_s_sleep(1);
}
DI v4u ld_sc1(__amdgpu_buffer_rsrc_t rs, unsigned off) { return __builtin_amdgcn_raw_buffer_load_b128(rs, off, 0, 16); }
DI v2u ld_sc1_64(__amdgpu_buffer_rsrc_t rs, unsigned off) { return __builtin_amdgcn_raw_buffer_load_b64(rs, off, 0, 16); }
DI void st_sc1(__amdgpu_buffer_rsrc_t rs, unsigned off, v4u v) { __builtin_amdgcn_raw_buffer_store_b128(v, rs, off, 0, 16); }
DI void st_sc1_64(__amdgpu_buffer_rsrc_t rs, unsigned off, v2u v) { __builtin_amdgcn_raw_buffer_store_b64(v, rs, off, 0, 16); }
DI void st_sc1_32(__amdgpu_buffer_rsrc_t rs, unsigned off, unsigned v) { __builtin_amdgcn_raw_buffer_store_b32(v, rs, off, 0, 16); }
DI unsigned fswz(unsigned row) { return ((row & 3u) << 2) | ((0x78u >> (2u * ((row >> 2) & 3u))) & 3u); }
template <int N> DI float ror16(float v) {
  return __int_as_float(__builtin_amdgcn_update_dpp(0, __float_as_int(v), 0x120 + N, 0xF, 0xF, false));
}
DI float xor1(float v) { return __int_as_float(__builtin_amdgcn_update_dpp(0, __float_as_int(v), 0xB1, 0xF, 0xF, false)); }
DI float add16(float v) { auto r = __builtin_amdgcn_permlane16_swap(__float_as_uint(v), __float_as_uint(v), false, false); return __uint_as_float(r[0]) + __uint_as_float(r[1]); }
DI float add32(float v) { auto r = __builtin_amdgcn_permlane32_swap(__float_as_uint(v), __float_as_uint(v), false, false); return __uint_as_float(r[0]) + __uint_as_float(r[1]); }
DI float rs_add16(float a, float b) { auto r = __builtin_amdgcn_permlane16_swap(__float_as_uint(a), __float_as_uint(b), false, false); return __uint_as_float(r[0]) + __uint_as_float(r[1]); }
DI float rs_add32(float a, float b) { auto r = __builtin_amdgcn_permlane32_swap(__float_as_uint(a), __float_as_uint(b), false, false); return __uint_as_float(r[0]) + __uint_as_float(r[1]); }
DI float rs_max16(float a, float b) { auto r = __builtin_amdgcn_permlane16_swap(__float_as_uint(a), __float_as_uint(b), false, false); return fmaxf(__uint_as_float(r[0]), __uint_as_float(r[1])); }
DI float max32(float v) { auto r = __builtin_amdgcn_permlane32_swap(__float_as_uint(v), __float_as_uint(v), false, false); return fmaxf(__uint_as_float(r[0]), __uint_as_float(r[1])); }
DI float rowsum(float v) { v += ror16<8>(v); v += ror16<4>(v); v += ror16<2>(v); v += ror16<1>(v); return v; }
DI float rowmax(float v) { v = fmaxf(v, ror16<8>(v)); v = fmaxf(v, ror16<4>(v)); v = fmaxf(v, ror16<2>(v)); v = fmaxf(v, ror16<1>(v)); return v; }
DI float ex2(float v) { return __builtin_amdgcn_exp2f(v); }
constexpr float LOG2E = 1.4426950408889634f;

DI void transpose8(const v4u (&r)[8], v4u (&c)[8]) {
#pragma unroll
  for (int q = 0; q < 4; ++q) {
    if (q == 0 || q == 2) { SB0(); MW(); }
#pragma unroll
    for (int e2 = 0; e2 < 4; ++e2) {
      unsigned a = r[2 * e2][q], b = r[2 * e2 + 1][q];
      c[2 * q][e2] = __builtin_amdgcn_perm(b, a, 0x05040100u);
      c[2 * q + 1][e2] = __builtin_amdgcn_perm(b, a, 0x07060302u);
    }
    asm volatile("" : "+v"(c[2 * q]), "+v"(c[2 * q + 1]));
  }
}
#define PLD(f) asm volatile("global_atomic_add_x2 %0, %1, %2, %3 sc0 sc1" : "=&v"(f) : "v"(fo), "v"(zz), "s"(wsp) : "memory")
#define PLDM(f) asm volatile("s_not_b64 exec, %4\n\tglobal_atomic_add_x2 %0, %1, %2, %3 sc0 sc1\n\ts_mov_b64 exec, -1" : "+v"(f) : "v"(fo), "v"(zz), "s"(wsp), "s"(seen) : "memory", "scc")
#define PL1 "global_atomic_add_x2 %0, %2, %3, %4 sc0 sc1\n\t"
#define PL1S PL1 "s_sleep " STR(F2G) "\n\t"
#define PLPRE PL1S PL1S
#define F2XS "s_mov_b64 exec, vcc\n\t"
#define MLX "s_mov_b64 exec, vcc\n\t"
DI void poll_flags(const unsigned char* wsp, unsigned fo, unsigned tag, v2u& f) {
  unsigned cap = SPIN_CAP;
  asm volatile(PLPRE "1:\n\t" PL1 "s_sub_u32 %1, %1, 1\n\ts_waitcnt vmcnt(" STR(F2D) " - 1)\n\tv_cmp_ne_u64 vcc, %5, %0\n\ts_cselect_b64 vcc, 0, vcc\n\t" F2XS
               "s_cbranch_vccnz 1b\n\ts_mov_b64 exec, -1"
               : "=&v"(f), "+s"(cap) : "v"(fo), "v"(v2u{0u, 0u}), "s"(wsp), "s"((u64)tag * 0x100000001ull) : "memory", "vcc", "scc");
}

template <int N> DI unsigned fpos(unsigned l) { return N ? (l % N) * 128 + (l / N) * 8 : l * 8; }
DI void send_flags(__amdgpu_buffer_rsrc_t rs, int wi, int lane, unsigned tag) {
#pragma unroll
  for (int t = 0; t < (T_O == 0 || T_F == 0 || T_UV == 1 ? 4 : 2); ++t) st_sc1_32(rs, WS_FLAG + (64 * t + lane) * 1024 + fpos<FLW>(wi >> 1) + (wi & 1) * 4, tag);
}

#define PIN(x) asm volatile("" : "+v"(x))
__global__ void __launch_bounds__(512, 2) fused_kernel(Params p) {
  extern __shared__ __attribute__((aligned(16))) unsigned char smem[];
  const u64 t0l = __builtin_amdgcn_s_memrealtime();
  const unsigned t0 = (unsigned)t0l;
  const int tid = threadIdx.x, lane = tid & 63;
  const int w = __builtin_amdgcn_readfirstlane(tid >> 6);
  const int cu = blockIdx.x;
  const int xcd = cu & 7, r8 = cu >> 3, idx = r8 >> 1;
  const __amdgpu_buffer_rsrc_t rs = __builtin_amdgcn_make_buffer_rsrc((void*)p.ws, 0, (int)WS_END, 0x00020000);
  const int g4 = lane >> 4, i16 = lane & 15;
  unsigned gen;
  asm volatile("s_load_dword %0, %1, 0x0" : "=s"(gen) : "s"(p.ws), "s"((unsigned)(size_t)(LAS unsigned char*)smem) : "memory");
  unsigned gen1;
#define GET_TAG() (__extension__({ asm volatile("s_waitcnt lgkmcnt(0)\n\ts_add_u32 %0, %1, 1" : "=s"(gen1) : "s"(gen) : "memory", "scc"); (gen1 & 0x3FFFu) | ((gen1 << 2) & 0x3FFF0000u); }))

  MK_TABLE();
  if ((r8 & 1) == 0) {
    const int b = BMAP ? idx & 7 : xcd, s = BMAP ? xcd + (idx & 8) : idx, kvi = b * 16 + s;
    const unsigned hh = lane >> 5, hd = lane & 31;
    WARM(w, e, idx * 8 + w, 128);
    auto wait_p = [&](unsigned pm, unsigned fm) AI {
      v4u cv;
#pragma unroll 1
      for (unsigned sp = 0; sp < SPIN_CAP * 16u; ++sp) {
        asm volatile("ds_read_b128 %0, %1 offset:%2\n\ts_waitcnt lgkmcnt(0)" : "=v"(cv) : "v"(0u), "i"(L_CNT) : "memory");
        if ((__builtin_amdgcn_readfirstlane(cv[0]) & pm) && (__builtin_amdgcn_readfirstlane(cv[3]) & fm) == fm) break;
      }
      return __builtin_amdgcn_readfirstlane(cv[1]);
    };
    auto wait_bits = [&](unsigned c, unsigned m) AI {
#pragma unroll 1
      for (unsigned sp = 0; sp < SPIN_CAP * 16u; ++sp)
        if ((cnt_get(c) & m) == m) break;
    };
    if (w >= 4) {
      const int k = w - 4;
      (void)GET_TAG();
      unsigned l16 = lane * 16, ap0;
      {
        const unsigned blk = (lane >> 4) & 1, q4 = i16 >> 2, pp = lane & 3;
        const unsigned r0 = 8 * hh + q4;
        const unsigned ch = 16 * k + 2 * (pp & 1) + blk;
        ap0 = L_SLOT + r0 * 1024 + ((ch ^ fswz(r0)) * 16) + 8 * (pp >> 1);
      }
      PIN(l16); PIN(ap0);
      v4u vf[2][16];
      unsigned ap1 = ap0 + 65536;
      PIN(ap1);
      auto ldv1 = [&](int r, int n) AI {
        const unsigned ap = (r & 1) ? ap1 : ap0;
        const v2u t0_ = trread((ap ^ ((n >> 2) * 64)) + (n & 3) * 16384), t1_ = trread((ap ^ ((n >> 2) * 64 + 32)) + (n & 3) * 16384 + 4096);
        vf[r & 1][n] = v4u{t0_[0], t0_[1], t1_[0], t1_[1]};
      };
      auto ldv = [&](int r) AI {
#pragma unroll
        for (int n = 0; n < 16; ++n) {
          ldv1(r, n);
          if ((n & 7) == 7) { if (r < 2) MS(); else MP(); }
        }
      };
      const char* base = (const char*)p.ckv + ((size_t)kvi * 256 + 16 * k) * 1024;
      unsigned xt = ((lane >> 4) << 16) | (i16 << 10) | (fswz(i16) << 4);
      unsigned ni = 0, sg = 0, ovf = 0, pc = 0, st = 0, fl = 0, cm = 0, cw = 0;
      KV_RUN_BODY();
      f32x16 oacc[4];
      v4u pf[2][4], cv4;
#define CALL0(L) "s_getpc_b64 s[92:93]\n\ts_cbranch_scc0 " L
#define RUN_TXT(L) "s_cmp_lg_u32 s76, s76\n\t" CALL0(L)
#define SIG_TXT "s_getreg_b32 s87, hwreg(HW_REG_IB_STS)\n\ts_and_b32 s87, s87, s66\n\ts_cmp_lg_u32 s87, s65\n\t" CALL0(".Lkv_sig")
#define RUN_OUT "+{s76}"(ni), "+{s77}"(sg), "={s84}"(ovf), "={s85}"(pc), "+{s86}"(st), "+{s75}"(fl), "+{s66}"(cm), "+{s65}"(cw)
#define RUN_IN "{s78}"(xm), "{s79}"(pm), "{s[80:81]}"(base), "{s82}"(L_SLOT + k * 16384), "{s83}"(k), "{v250}"(l16), "{v251}"(xt)
#define RUN_CL "s64", "s67", "s87", "s88", "s89", "s90", "s91", "s92", "s93", "s94", "s95", "v248", "v249", "scc", "memory"
      auto run = [&](unsigned xm, unsigned pm) AI { asm volatile(RUN_TXT(".Lkv_run") : RUN_OUT, "={v[252:255]}"(cv4) : RUN_IN : RUN_CL); };
#define PF0(c) c "{v[232:235]}"(pf[0][0]), c "{v[236:239]}"(pf[0][1]), c "{v[240:243]}"(pf[0][2]), c "{v[244:247]}"(pf[0][3]), c "{v[252:255]}"(cv4)
#define PF1(c) c "{v[216:219]}"(pf[1][0]), c "{v[220:223]}"(pf[1][1]), c "{v[224:227]}"(pf[1][2]), c "{v[228:231]}"(pf[1][3]), c "{v[252:255]}"(cv4)
#define RUNP_IN RUN_IN, "{s74}"(L_P + r * 4096), "{s68}"(r ? ANY : 0x7Fu)
      auto runp = [&](int r, unsigned pm, bool chk) AI {
        const unsigned xm = 0;
        if (chk) {
#define CHK_TXT(a, b, c, d) "s_cmp_eq_u32 s76, 64\n\ts_cselect_b32 s89, s79, 0\n\ts_mov_b32 s90, 0xffffff\n\ts_mov_b32 s64, 64\n3:\n\ts_waitcnt lgkmcnt(0)\n\t" \
                "v_readfirstlane_b32 s85, v252\n\tv_readfirstlane_b32 s86, v254\n\tv_readfirstlane_b32 s84, v253\n\tv_readfirstlane_b32 s75, v255\n\t" \
                "s_and_b32 s87, s89, s85\n\ts_getpc_b64 s[92:93]\n\ts_cbranch_scc0 1f\n\t.subsection 11\n\t" MKB "1:\n\t" \
                "s_cmp_eq_u32 s89, 0\n\ts_cbranch_scc1 .Lkv_chk\n\tv_mov_b32 v252, 0\n\tv_add_u32 v249, s74, v250\n\tds_read_b128 v[252:255], v252 offset:" STR(L_CNT_) "\n\t" \
                "ds_read_b128 " a ", v249\n\tds_read_b128 " b ", v249 offset:1024\n\tds_read_b128 " c ", v249 offset:2048\n\tds_read_b128 " d ", v249 offset:3072\n\t" \
                SIG_TXT "\n\ts_sub_u32 s90, s90, 1\n\ts_cselect_b32 s89, 0, s89\n\ts_cselect_b32 s64, -1, s64\n\ts_branch 3b\n\t.subsection 0"
          if (r & 1) asm volatile(CHK_TXT("v[216:219]", "v[220:223]", "v[224:227]", "v[228:231]") : RUN_OUT, PF1("+") : RUNP_IN : RUN_CL);
          else asm volatile(CHK_TXT("v[232:235]", "v[236:239]", "v[240:243]", "v[244:247]") : RUN_OUT, PF0("+") : RUNP_IN : RUN_CL);
        } else {
          if (r & 1) asm volatile(RUN_TXT(".Lkv_runp") : RUN_OUT, PF1("=") : RUNP_IN : RUN_CL);
          else asm volatile(RUN_TXT(".Lkv_runp") : RUN_OUT, PF0("=") : RUNP_IN : RUN_CL);
        }
      };
#define SPEC_TXT "v_mov_b32 v252, 0\n\tds_read_b128 %4, v252 offset:%6\n\tds_read_b128 %0, %5 offset:%7\n\tds_read_b128 %1, %5 offset:%7+1024\n\t" \
                 "ds_read_b128 %2, %5 offset:%7+2048\n\tds_read_b128 %3, %5 offset:%7+3072"
      auto spec = [&](int r) AI {
        if (r & 1) asm volatile(SPEC_TXT : PF1("=") : "v"(l16), "i"(L_CNT), "i"(L_P + r * 4096) : "memory");
        else asm volatile(SPEC_TXT : PF0("=") : "v"(l16), "i"(L_CNT), "i"(L_P + r * 4096) : "memory");
      };
      WARM(s, e, k, 4);
      wait_until(t0, KV_DLY);
      run(0u, ANY);
      st = 0; fl = 0;
      wg_barrier();
      MS();
      run(0xFu, ANY);
      ldv(0);
      cnt_addv(C_ST, 0x10000u);
      run(0xF0u, ANY);
      ldv(1);
      cnt_addv(C_ST, 0x100000u);
      auto sigchk = [&]() AI {
        asm volatile(SIG_TXT
                     : "+{s77}"(sg), "+{s66}"(cm), "+{s65}"(cw) : "{s76}"(ni), "{s83}"(k) : RUN_CL);
      };
      auto getp = [&](int r, bool chk) AI {
        runp(r, 0xCu << (8 * r), chk);
        if (r && RARE(ovf & (7u << (8 * r)))) {
          runp(r, 8u << (8 * r), false);
          const float alpha = lds<float>(L_AL + r * 128 + hd * 4);
#pragma unroll
          for (int ct = 0; ct < 4; ++ct)
#pragma unroll
            for (int e = 0; e < 16; ++e) oacc[ct][e] *= alpha;
        }
        MP();
      };
      auto pv = [&](int r) AI {
#pragma unroll
        for (int n = 0; n < 16; ++n) {
          SB0();
          if (r == 0 && (n & 3) == 0) {
            f32x16 z;
#pragma unroll
            for (int e = 0; e < 16; ++e) z[e] = 0.f;
            oacc[n >> 2] = mfma32(vf[r & 1][n], pf[r & 1][n & 3], z);
          } else {
            oacc[n >> 2] = mfma32(vf[r & 1][n], pf[r & 1][n & 3], oacc[n >> 2]);
          }
          PIN(oacc[n >> 2]);
          SB0();
          if (r && n < 8) { ldv1(r + 1, 2 * n); ldv1(r + 1, 2 * n + 1); }
          if (n == SPAT) spec(r + 1);
          if (MIDSIG && (n & 3) == 1) sigchk();
          if ((n & 3) == 3) MP();
        }
      };
      MP();
      if (G0T) { run(0u, 0x7Fu); spec(0); }
      getp(0, G0T);
      if (PPRIO) __builtin_amdgcn_s_setprio(PPRIO);
      pv(0);
      getp(1, true);
      pv(1);
      if (RARE((st & 0xF00u) != 0xF00u)) {
        MP();
        run(0xF00u, ANY);
        ldv(2);
        spec(2);
      }
      cnt_addv(C_FL, 0x1000u);
      MP();
      getp(2, true);
      pv(2);
      if ((st & 0xF000u) != 0xF000u) {
        if (RARE(ni != 64)) run(0xF000u, ANY);
#pragma unroll 1
        for (unsigned sp = 0; sp < SPIN_CAP * 16u; ++sp) {
          sigchk();
          if ((cnt_get(C_ST) & 0xF000u) == 0xF000u) break;
        }
        if (VD) __builtin_amdgcn_s_sleep(VD);
        ldv(3);
        spec(3);
      }
      {
        const unsigned sb = L_SLOT + k * 16384;
        unsigned sa = sb + hd * 272 + hh * 64;
        unsigned rx = sb + (lane >> 3) * 272 + (lane & 7) * 16;
        unsigned gx = WS_CIN + ((lane >> 3) * 4 + k) * CIN_WI + (kvi ^ (CSW ? (lane >> 3) * 16 : 0)) * CIN_ROW + (lane & 7) * 16;
        MP();
        PIN(sa); PIN(rx); PIN(gx);
        getp(3, true);
        if (RARE(!(fl & 0x4000u))) wait_bits(C_FL, 0x4000u);
        auto dwd = [&](int d) AI { return pk2(oacc[d >> 3][2 * (d & 7)], oacc[d >> 3][2 * (d & 7) + 1]); };
        auto cvt1 = [&](int g) AI { lds<v4u>(sa + (g >> 2) * 128 + (g & 3) * 16) = v4u{san(dwd(4 * g)), dwd(4 * g + 1), dwd(4 * g + 2), dwd(4 * g + 3)}; };
        auto cvt = [&](int ct) AI { cvt1(2 * ct); cvt1(2 * ct + 1); };
        v4u ov[4];
        auto ord = [&](int hf) AI {
#pragma unroll
          for (int t = 0; t < 4; ++t) ov[t] = lds128(rx + t * 2176 + hf * 128);
        };
        auto ost = [&](int hf) AI {
#pragma unroll
          for (int t = 0; t < 4; ++t) __builtin_amdgcn_raw_buffer_store_b128(ov[t], rs, gx + hf * 128, t * 32 * CIN_WI, 16);
        };
#pragma unroll
        for (int n = 0; n < 16; ++n) {
          SB0();
          oacc[n >> 2] = mfma32(vf[1][n], pf[1][n & 3], oacc[n >> 2]);
          SB0();
          if ((n & 3) == 1) MP();
          if (QST) {
            if (n >= 5 && n < 15 && ((n - 1) & 2) == 0) cvt1(2 * ((n - 5) >> 2) + ((n - 1) & 1));
            if (n == 10) cnt_addv(C_XS, 16u << k);
          } else {
          if (n == 5) cvt(0);
          if (n == 9) { cvt(1); ord(0); }
          if (n == OSAT) { ost(0); if (MLAT == 16) cnt_addv(C_XS, 1u << k); }
          if (n == 13) cvt(2);
          }
        }
        SB0();
        MP();
        if (MLAT == 17) cnt_addv(C_XS, 1u << k);
        cvt(3);
        ord(1);
        SB0();
        ost(1);
        if (MLAT == 18) cnt_addv(C_XS, 1u << k);
      }
    } else {
      const int k = w;
      v2u f0 = {0u, 0u}, f1 = f0, f2 = f0;
      if (k == 0 && lane < C_N) lds<unsigned>(L_CNT + lane * 4) = 0u;
      wg_barrier();
      const unsigned tag = GET_TAG();
      WARM(q, s, k, 4);
      if (QPRIO) __builtin_amdgcn_s_setprio(QPRIO);
      float nr0 = 0.f, nr1 = 0.f;
      v2f lsum = {0.f, 0.f};
      const float OVT = 56.f;
      unsigned nx = 0;
      unsigned aq0 = L_SLOT + (16 * k + i16) * 1024 + ((fswz(i16) ^ g4) * 16);
      unsigned po = L_P + k * 1024 + (i16 + 32 * (g4 >> 1)) * 16 + (g4 & 1) * 8;
      PIN(po); PIN(aq0);
      v4u kf[2][16];
      auto ldk = [&](int r) AI {
#pragma unroll
        for (int u = 0; u < 16; ++u) kf[r & 1][u] = lds128(((aq0 + (r & 1) * 65536) ^ ((u & 3) * 64)) + (u >> 2) * 256);
      };
      v4u sel;
#pragma unroll
      for (int q = 0; q < 4; ++q) sel[q] = ((g4 & 1) == (i16 >> 3) && ((i16 & 7) >> 1) == q) ? ((i16 & 1) ? 0x3F800000u : 0x3F80u) : 0u;
      unsigned qs = WS_QABS + (g4 >> 1) * 262144 + b * 32768 + k * 8192 + (lane & 31) * 16;
      unsigned qd = WS_QABS + b * 32768 + (16 + i16) * 1024 + (60 + g4) * 16;
      unsigned qw[8], qw15;
#pragma unroll
      for (int u = 0; u < 8; ++u) { qw[u] = L_Q + k * 8192 + ((i16 ^ fswz(8 * k + u)) * 16) + (g4 >> 1) * 256 + (g4 & 1) * 8; PIN(qw[u]); }
      qw15 = qw[7] - (k == 3 && g4 >= 2 && i16 >= 12 ? 4096u : 0u);
      unsigned qr = L_Q + i16 * 1024 + ((fswz(i16) ^ g4) * 16);
      PIN(sel); PIN(qs); PIN(qd); PIN(qw15); PIN(qr);
      unsigned mxw = L_Q + (16 + (hd >> 2)) * 1024 + 768 + ((3 ^ ((hd >> 2) & 3)) * 64) + (hd & 3) * 16 + k * 4;
      unsigned mxr = L_Q + (16 + (i16 >> 2)) * 1024 + 768 + ((3 ^ (i16 >> 2)) * 64) + (i16 & 3) * 16;
      PIN(mxw); PIN(mxr);
      wait_bits(C_ST, 1u << k);
      ldk(0);
      cnt_addv(C_ST, 1u << (24 + k));
      MQ();
      if (K1AT == 0) {
        wait_bits(C_ST, 16u << k);
        ldk(1);
        cnt_addv(C_ST, 1u << (28 + k));
        MQ();
      }
      v4u qf[32], qv[16], da, db;
      const unsigned char* wsp = p.ws;
      const unsigned char* wsp2 = p.ws + 4096;
#define QLD(t) asm volatile("global_load_dwordx4 %0, %1, %2 offset:%3 sc1" : "=v"(qv[t]) : "v"(qs), "s"((t) < 8 ? wsp : wsp2), "i"(((t) & 7) * 512) : "memory")
      if (k == 0) {
        wait_until(t0, POLL_START);
        const unsigned fo = WS_FLAG + kvi * 1024 + fpos<FLW>(lane);
        const v2u zz = {0u, 0u};
        u64 seen = 0;
        bool sent = false;
        auto chk = [&](v2u& f) AI {
          asm volatile("s_waitcnt vmcnt(2)" : "+v"(f)::"memory");
          const u64 ok = __ballot(((f[0] ^ tag) | (f[1] ^ tag)) == 0) | seen;
          if (ok != seen) {
            seen = ok;
            lds<v2u>(L_CNT + C_MASK * 4) = v2u{(unsigned)ok, (unsigned)(ok >> 32)};
            if (PAUSE && !sent && __popcll(~ok) <= PK) { cnt_addv(C_FL, 0x100u); sent = true; }
          }
          return ok == ~0ull;
        };
        MQ();
        PLD(f0); __builtin_amdgcn_s_sleep(POLL_GAP);
        PLD(f1); __builtin_amdgcn_s_sleep(POLL_GAP);
        PLD(f2);
#pragma unroll 1
        for (unsigned spin = 0; spin < SPIN_CAP; ++spin) {
          if (chk(f0)) break;
          if (QPM) PLDM(f0); else PLD(f0);
          MQ();
          if (chk(f1)) break;
          if (QPM) PLDM(f1); else PLD(f1);
          MQ();
          if (chk(f2)) break;
          if (QPM) PLDM(f2); else PLD(f2);
        }
        MQ();
#pragma unroll
        for (int t = 0; t < 16; ++t) QLD(t);
      } else {
        unsigned pend = 0xFFFFu;
#pragma unroll 1
        for (unsigned spin = 0; spin < SPIN_CAP * 16u; ++spin) {
          v2u mk;
          asm volatile("ds_read_b64 %0, %1 offset:%2\n\ts_waitcnt lgkmcnt(0)" : "=v"(mk) : "v"(C_MASK * 4u), "i"(L_CNT) : "memory");
          const unsigned m0 = __builtin_amdgcn_readfirstlane(mk[0]), m1 = __builtin_amdgcn_readfirstlane(mk[1]);
          const unsigned nw = ((k < 2 ? m0 : m1) >> (16 * (k & 1))) & pend;
          if (PROG ? nw != 0u : (m0 & m1) == ~0u) {
            pend &= ~nw;
#pragma unroll
            for (int t = 0; t < 16; ++t)
              if (nw >> t & 1u) { QLD(t); if ((t & 7) == 0) MQ(); }
          }
          if ((m0 & m1) == ~0u) break;
          __builtin_amdgcn_s_sleep(1);
        }
      }
#define QLD2() asm volatile("global_load_dwordx4 %0, %2, %3 sc1\n\tglobal_load_dwordx4 %1, %2, %4 sc1" : "=&v"(da), "=&v"(db) : "v"(qd), "s"(wsp), "s"(wsp + 262144) : "memory")
      QLD2();
      cnt_add(C_FL);
      MQ();
      if (K1AT == 1) {
        wait_bits(C_ST, 16u << k);
        ldk(1);
        cnt_addv(C_ST, 1u << (28 + k));
      }
#pragma unroll 1
      for (unsigned spin = 0; spin < SPIN_CAP; ++spin) {
        if (spin) {
#pragma unroll
          for (int t = 0; t < 16; ++t) QLD(t);
          QLD2();
        }
        MQ();
        asm volatile("s_waitcnt vmcnt(2)" : W8(qv, 0), W8(qv, 8), "+v"(f0), "+v"(f1), "+v"(f2)::"memory");
        float chk = 0.f;
        f32x4 d[16];
#pragma unroll
        for (int t = 0; t < 18; ++t) {
          if (t < 16) d[t] = mfma16(sel, qv[t], f32x4{0.f, 0.f, 0.f, 0.f});
          if (t >= 2) {
            const int u = t - 2;
            lds<v2u>((u == 15 ? qw15 : qw[u >> 1]) + (u >> 1) * 1024 + (u & 1) * 512) = v2u{pk2(d[u][0], d[u][1]), pk2(d[u][2], d[u][3])};
            chk += d[u][0];
          }
          SB0();
          if ((t & 3) == 1) MQ();
        }
        asm volatile("s_waitcnt vmcnt(0)" : "+v"(da), "+v"(db)::"memory");
        if (!__any(chk != chk || max(da[0], db[0]) == POISON)) break;
        unsigned mx = max(da[0], db[0]);
#pragma unroll
        for (int t = 0; t < 16; ++t) mx = max(mx, qv[t][0]);
        if (!__any(mx == POISON)) break;
        CFENCE();
      }
      MQ();
      cnt_addv(C_FL, 16u);
#pragma unroll
      for (int e = 0; e < 4; ++e) qf[31][e] = pk2(bf2f(da[e]) + bf2f(db[e]), bfhi(da[e]) + bfhi(db[e]));
      PIN(qf[31]);
      MQ();
      wait_bits(C_FL, 0x40u);
      MQ();
      f32x4 s0 = {0.f, 0.f, 0.f, 0.f}, s1 = s0, a0 = s0, a1 = s0, t0, t1;
      auto ldq = [&](int f) AI { qf[(f >> 1) + 16 * (f & 1)] = lds128((qr ^ (((f >> 1) & 3) * 64)) + (f >> 3) * 256 + (f & 1) * 16384); };
      v4u kn[2][16], cs;
      auto ldn = [&](int r, int u) AI { kn[r & 1][u] = lds128(((aq0 + (r & 1) * 65536) ^ ((u & 3) * 64)) + (u >> 2) * 256); };
#define CRD() asm volatile("ds_read_b128 %0, %1 offset:%2" : "=v"(cs) : "v"(0u), "i"(L_CNT) : "memory")
#define CWT() asm volatile("s_waitcnt lgkmcnt(0)" : "+v"(cs)::"memory")
#pragma unroll
      for (int f = 0; f < 12; ++f) ldq(f);
#pragma unroll
      for (int u = 0; u < 16; ++u) {
        SB0();
        s0 = mfma16(kf[0][u], qf[u], s0);
        s1 = mfma16(kf[0][u], qf[16 + u], s1);
        if (MIX && (u & 1)) a1 = mfma16(kf[1][u >> 1], qf[16 + (u >> 1)], a1);
        else if (MIX) a0 = mfma16(kf[1][u >> 1], qf[u >> 1], a0);
        SB0();
        if ((u & 3) == 0) MQ();
        if (12 + 2 * u < 31) ldq(12 + 2 * u);
        if (13 + 2 * u < 31) ldq(13 + 2 * u);
      }
      MQ();
      unsigned cx;
      f32x4 ma, mb;
      auto max4 = [&](const f32x4& v) AI { return fmaxf(fmaxf(v[0], v[1]), fmaxf(v[2], v[3])); };
      auto post_max = [&](bool first) AI {
        const float mw = max32(rs_max16(max4(s0), max4(s1)));
        lds<float>(mxw) = mw;
        if (first) cnt_addv(C_P, 0x10u);
        else { cnt_add(C_MX); nx += 4; }
      };
#define XRD(c, w) asm volatile("ds_read_b32 %0, %3 offset:%5\n\tds_read_b128 %1, %4\n\tds_read_b128 %2, %4 offset:4096" w : "=&v"(cx), "=&v"(ma), "=&v"(mb) : "v"(0u), "v"(mxr), "i"(L_CNT + (c) * 4) : "memory")
      auto exchange = [&](int r) AI {
        asm volatile("s_waitcnt lgkmcnt(0)" : "+v"(cx), "+v"(ma), "+v"(mb)::"memory");
        if (RARE(r ? __builtin_amdgcn_readfirstlane(cx) < nx : !(__builtin_amdgcn_readfirstlane(cx) & 0x40u))) {
#pragma unroll 1
          for (unsigned sp = 0; sp < SPIN_CAP * 16u; ++sp) {
            if (r == 0) MQ();
            XRD(r ? C_MX : C_P, "\n\ts_waitcnt lgkmcnt(0)");
            if (r ? __builtin_amdgcn_readfirstlane(cx) >= nx : (__builtin_amdgcn_readfirstlane(cx) & 0x40u) != 0) break;
          }
        }
        float d0 = max4(ma), d1 = max4(mb);
        if (r) {
          d0 = fmaxf(d0, 0.f); d1 = fmaxf(d1, 0.f);
          const float al0 = ex2(-d0), al1 = ex2(-d1);
          lsum[0] *= al0; lsum[1] *= al1;
          lds<float>(L_AL + r * 128 + hd * 4) = (lane & 16) ? al1 : al0;
        }
#pragma unroll
        for (int e = 0; e < 4; ++e) { s0[e] -= d0; s1[e] -= d1; }
        nr0 -= d0; nr1 -= d1;
      };
      v2f ln;
      auto do_p = [&](int r, const f32x4& x0, const f32x4& x1, unsigned ov) AI {
        float e0[4], e1[4];
#pragma unroll
        for (int e = 0; e < 4; ++e) { e0[e] = ex2(x0[e]); e1[e] = ex2(x1[e]); }
        lds<v2u>(po + r * 4096) = v2u{pk2(e0[0], e0[1]), pk2(e0[2], e0[3])};
        lds<v2u>(po + r * 4096 + 256) = v2u{pk2(e1[0], e1[1]), pk2(e1[2], e1[3])};
        cnt_add2(C_P, 1u << (8 * r), ov);
        ln = lsum + ((v2f{e0[0], e1[0]} + v2f{e0[1], e1[1]}) + (v2f{e0[2], e1[2]} + v2f{e0[3], e1[3]}));
      };
      auto redo = [&](int r) AI {
        post_max(false);
        XRD(C_MX, "");
        exchange(r);
        do_p(r, s0, s1, 0u);
      };
      auto fin = [&](int r, const f32x4& x0, const f32x4& x1) AI {
        MQ();
#pragma unroll
        for (int e = 0; e < 4; ++e) { s0[e] = x0[e] + nr0; s1[e] = x1[e] + nr1; }
        const unsigned ov = __any(fmaxf(max4(s0), max4(s1)) > OVT) ? 1u << (8 * r) : 0u;
        do_p(r, s0, s1, ov);
      };
#pragma unroll
      for (int u = 8 * MIX; u < 16; ++u) {
        SB0();
        a0 = mfma16(kf[1][u], qf[u], a0);
        a1 = mfma16(kf[1][u], qf[16 + u], a1);
        PIN(a0); PIN(a1);
        SB0();
        if (u <= 8 * MIX + XAT + 4 || u >= 13) MQ();
        if (u == 8 * MIX) post_max(true);
        if (u == 8 * MIX + XAT) XRD(C_P, "");
        if (u == 8 * MIX + 2 + XAT) exchange(0);
        if (u == 8 * MIX + 3 + XAT) {
          do_p(0, s0, s1, 0u);
          lsum = ln;
        }
        if (u == 13) {
          CRD();
#pragma unroll
          for (int v = 0; v < 16; ++v) ldn(2, v);
        }
      }
      fin(1, a0, a1);
      asm volatile("s_waitcnt lgkmcnt(14)" : "+v"(cs)::"memory");
#pragma unroll
      for (int r = 2; r < 4; ++r) {
        MQ();
        if ((r == 3 && !K3S) || RARE(!(__builtin_amdgcn_readfirstlane(cs[2]) >> (4 * r + k) & 1u))) {
          if (r == 2 || K3S) MQ();
          wait_bits(C_ST, 1u << (4 * r + k));
#pragma unroll
          for (int u = 0; u < 16; ++u) ldn(r, u);
        }
        MQ();
        CRD();
        t0 = f32x4{0.f, 0.f, 0.f, 0.f}; t1 = t0;
#pragma unroll
        for (int u = 0; u < 16; ++u) {
          SB0();
          t0 = mfma16(kn[r & 1][u], qf[u], t0);
          t1 = mfma16(kn[r & 1][u], qf[16 + u], t1);
          SB0();
          if ((u & 7) == 0) MQ();
          if (K3S && r == 2 && u == 13) {
            PIN(t0); PIN(t1);
            CRD();
#pragma unroll
            for (int v = 0; v < 16; ++v) ldn(3, v);
            MQ();
          }
          if (u == CKAT) {
            PIN(t0); PIN(t1);
            CWT();
            unsigned ov = __builtin_amdgcn_readfirstlane(cs[1]);
            if (RARE(!(__builtin_amdgcn_readfirstlane(cs[0]) >> (8 * r - 8) & 12u))) { MQ(); ov = wait_p(12u << (8 * r - 8), 0u); }
            if (RARE(ov & (7u << (8 * r - 8)))) redo(r - 1);
            lsum = ln;
            MQ();
          }
        }
        fin(r, t0, t1);
        if (K3S && r == 2) asm volatile("s_waitcnt lgkmcnt(14)" : "+v"(cs)::"memory");
      }
      MQ();
      if (RARE(wait_p(0xC000000u, 0u) & 0x7000000u)) redo(3);
      lsum = ln;
      {
        MQ();
        const float lt = add32(rs_add16(lsum[0], lsum[1]));
        lds<float>(L_LS + k * 128 + hd * 4) = lt;
        cnt_add(C_MX);
        nx += 4;
        cnt_wait(C_MX, nx);
        const LAS float* ls = (const LAS float*)(size_t)(L_LS + hd * 4);
        v4u mlv = {__float_as_uint((lane & 16) ? -nr1 : -nr0), tag, __float_as_uint((ls[0] + ls[32]) + (ls[64] + ls[96])), tag};
        unsigned gm = WS_ML + ((hd * 4 + k) * 128 + kvi) * 16;
        PIN(mlv); PIN(gm);
        MQ();
        if (QST) {
          unsigned rx = L_SLOT + k * 16384 + (lane >> 3) * 272 + (lane & 7) * 16;
          unsigned gx = WS_CIN + ((lane >> 3) * 4 + k) * CIN_WI + (kvi ^ (CSW ? (lane >> 3) * 16 : 0)) * CIN_ROW + (lane & 7) * 16;
          PIN(rx); PIN(gx);
          MQ();
          wait_bits(C_XS, 16u << k);
          v4u ov[4];
#pragma unroll
          for (int t = 0; t < 4; ++t) ov[t] = lds128(rx + t * 2176);
#pragma unroll
          for (int t = 0; t < 4; ++t) __builtin_amdgcn_raw_buffer_store_b128(ov[t], rs, gx, t * 32 * CIN_WI, 16);
          MQ();
        }
        wait_bits(C_XS, 1u << k);
        if (lane < 32) st_sc1(rs, gm, mlv);
      }
    }
  } else {
    const int h = 4 * xcd + (idx & 3), j = idx >> 2, wi = 4 * h + j;
    const bool ffn = wi < 86;
    const unsigned hh = lane >> 5;
    v4u xa, xb;
    asm volatile("s_cmp_gt_u32 %3, 3\n\ts_cbranch_scc1 1f\n\tglobal_load_dwordx4 %0, %2, %4\n\tglobal_load_dwordx4 %1, %2, %4 offset:512\n1:"
                 : "=&v"(xa), "=&v"(xb) : "v"((2 * w + hh) * 1024 + (lane & 31) * 16), "s"(w), "s"(p.x) : "memory", "scc");
    constexpr int NT = FR4 ? 2 : 4;
    const int hf = FMAP ? (idx & 7) + 8 * (xcd & 3) : h, jf = FMAP ? (idx >> 3) + 2 * (xcd >> 2) : j;
    const int fr = FR4 ? 1 : __builtin_amdgcn_readfirstlane((jf >> 1) ^ 1);
    const int dj = jf & 1, c0 = (FR4 ? (jf >> 1) * 256 : 0) + w * 16 * NT;
    unsigned qb = WS_QABS + dj * 262144 + (i16 * 32 + hf) * 1024 + (c0 + 8 * g4) * 2;
    auto ffn_pf = [&](unsigned& d) {
      const int u = idx * 8 + w, q5 = lane / 5, r5 = lane - 5 * q5;
      const char* pb = (const char*)((q5 & 1) ? p.wu : p.wg) + (4 * u + (min(q5, 7) >> 1)) * (FF * 2) + min(xcd * 512 + min(r5 * 128, 508), FF * 2 - 4);
      asm volatile("global_load_dword %0, %1, off" : "=&v"(d) : "v"(pb) : "memory");
    };
    unsigned dpf = 0;
    SB0();
    if (WPF) {
      unsigned d0, d1;
      if (WPF == 1) {
        const unsigned po = (32 * idx + 4 * w + g4) * 8192 + xcd * 1024 + i16 * 64;
        asm volatile("global_load_dword %0, %4, %5\n\tglobal_load_dword %1, %4, %6\n\ts_waitcnt vmcnt(0)" : "=&v"(d0), "=&v"(d1), "+v"(xa), "+v"(xb) : "v"(po), "s"(p.wq), "s"(p.wuk) : "memory");
      } else {
        const char* pa = (const char*)(hh ? p.wuk : p.wq) + (32 * idx + 4 * w + ((lane >> 3) & 3)) * 8192 + xcd * 1024 + (lane & 7) * 128;
        asm volatile("global_load_dword %0, %1, off" : "=&v"(d0) : "v"(pa) : "memory");
        if (PZT) asm volatile("global_load_dword %0, %1, %2" : "=&v"(dpf) : "v"(WS_CIN + wi * CIN_WI + w * 4096), "s"(p.ws) : "memory");
        if (PZE == 1) {
#pragma unroll
          for (int t = 0; t < 4; ++t) st_sc1(rs, WS_CIN + wi * CIN_WI + w * 4096 + t * 1024 + lane * 16, v4u{POISON, POISON, POISON, POISON});
          CFENCE();
        }
        if (w >= 4) {
          wait_until(t0, WM_DLY);
          WARM(w, q, w - 4, 4);
          if (PZE == 3) VMWAIT(0);
        } else if (PZE == 3) {
          wait_until(t0, PZ_AT);
#pragma unroll
          for (int t = 0; t < 8; ++t) st_sc1(rs, WS_CIN + wi * CIN_WI + w * 8192 + t * 1024 + lane * 16, v4u{POISON, POISON, POISON, POISON});
          CFENCE();
        }
        asm volatile("s_waitcnt vmcnt(%4)" : "+v"(d0), "+v"(xa), "+v"(xb), "+v"(dpf) : "i"(PZE == 1 ? 4 : PZE == 3 ? 8 : 0) : "memory");
      }
      if (WPF_DLY) __builtin_amdgcn_s_sleep(WPF_DLY);
    }
    __builtin_amdgcn_s_barrier();
    MW();
    v4u wukf[8];
    if (fr) {
      const char* src = (const char*)p.wq + hf * 256 + dj * 128 + (size_t)(64 * w) * 8192;
#pragma unroll
      for (int t = 0; t < 8; ++t) {
        glds16s((lane >> 3) * 8192 + (((lane & 7) ^ (((g4 & 1) | ((t & 1) << 1)) << 1)) * 16), src + t * 65536, L_WQ + (64 * w + 8 * t) * 128);
        if (t == 3) MW();
      }
      MW();
    }
    {
      const unsigned vk = (8 * (i16 >> 2) + (lane & 3)) * 8192 + g4 * 16;
      const char* kb = (const char*)p.wuk + hf * 256 + dj * 128 + (size_t)c0 * 8192;
#pragma unroll
      for (int u = 0; u < NT / 2; ++u)
        asm volatile("s_cmp_eq_u32 %5, 0\n\ts_cbranch_scc1 1f\n\t"
                     "global_load_dwordx4 %0, %4, %6\n\tglobal_load_dwordx4 %1, %4, %6 offset:64\n\t"
                     "global_load_dwordx4 %2, %4, %7\n\tglobal_load_dwordx4 %3, %4, %7 offset:64\n1:"
                     : "=&v"(wukf[4 * u]), "=&v"(wukf[4 * u + 1]), "=&v"(wukf[4 * u + 2]), "=&v"(wukf[4 * u + 3])
                     : "v"(vk), "s"(fr), "s"(kb + 32 * u * 8192), "s"(kb + (32 * u + 4) * 8192) : "memory", "scc");
    }
    {
      MW();
      const v4u pz = {POISON, POISON, POISON, POISON};
      if (fr && i16 < 8) {
#pragma unroll
        for (int u = 0; u < NT / 2; ++u) st_sc1(rs, qb + u * 64, pz);
      }
      if (PZE == 2) {
        if (w >= 4) {
          if (PZ_DLY) __builtin_amdgcn_s_sleep(PZ_DLY);
          MW();
#pragma unroll
          for (int t = 0; t < 2; ++t) st_sc1(rs, WS_EIN + wi * 8192 + (w - 4) * 2048 + t * 1024 + lane * 16, pz);
#pragma unroll
          for (int t = 0; t < 8; ++t) st_sc1(rs, WS_CIN + wi * CIN_WI + (w - 4) * 8192 + t * 1024 + lane * 16, pz);
          VMWAIT(2 * NT + NT / 2 + 10);
        }
      } else {
      st_sc1(rs, WS_EIN + wi * 8192 + w * 1024 + lane * 16, pz);
      if (!PZE) {
#pragma unroll
        for (int t = 0; t < 4; ++t) st_sc1(rs, WS_CIN + wi * CIN_WI + w * 4096 + t * 1024 + lane * 16, pz);
      }
      }
    }
    if (w < 4) {
      asm volatile("s_waitcnt vmcnt(%3)\n\ts_cmp_lg_u32 %2, 0\n\ts_cbranch_scc1 1f\n\ts_waitcnt vmcnt(1)\n1:" : "+v"(xa), "+v"(xb) : "s"(fr), "i"(8 + 2 * NT + NT / 2 + 1 + 8) : "memory", "scc");
      MW();
      v2f v[8];
#pragma unroll
      for (int e = 0; e < 4; ++e) { v[e] = v2f{bf2f(xa[e]), bfhi(xa[e])}; v[4 + e] = v2f{bf2f(xb[e]), bfhi(xb[e])}; }
      const unsigned p0 = __builtin_amdgcn_readlane(__float_as_uint(v[0][0]), 0), p1 = __builtin_amdgcn_readlane(__float_as_uint(v[0][0]), 32);
      const float piv = __uint_as_float(hh ? p1 : p0);
      v2f sm = {0.f, 0.f}, ss = {0.f, 0.f};
#pragma unroll
      for (int e = 0; e < 8; ++e) {
        if (e == 3) { SB0(); MW(); }
        const v2f d = v[e] - v2f{piv, piv}; sm += d; ss += d * d;
      }
      SB0();
      MW();
      float t = rowsum(rs_add16(sm[0] + sm[1], ss[0] + ss[1]));
      auto sw2 = __builtin_amdgcn_permlane16_swap(__float_as_uint(t), __float_as_uint(t), false, false);
      const float md = __uint_as_float(sw2[0]) * (1.f / DM);
      const float var = fmaxf(fmaf(-md, md, __uint_as_float(sw2[1]) * (1.f / DM)), 0.f);
      const float mb = rbf(piv + md), vb = rbf(var);
      const float rden = __builtin_amdgcn_rcpf(rbf(__builtin_amdgcn_sqrtf(rbf(vb + 1e-5f))));
      SB0();
      MW();
      v4u o4[2];
#pragma unroll
      for (int e = 0; e < 8; ++e) {
        if (e == 3 || e == 6) { SB0(); MW(); }
        const v2f d = v[e] - v2f{mb, mb};
        const unsigned u = pk2(d[0], d[1]);
        const v2f o = v2f{bf2f(u), bfhi(u)} * v2f{rden, rden};
        o4[e >> 2][e & 3] = pk2(o[0], o[1]);
      }
      const unsigned xo = L_XN + (2 * w + hh) * 1040 + (lane & 31) * 16;
      lds<v4u>(xo) = o4[0];
      lds<v4u>(xo + 512) = o4[1];
      if (tid < C_N) lds<unsigned>(L_CNT + tid * 4) = 0u;
      if (PZE == 2) VMWAIT(2 * NT + NT / 2);
    }
    MW();
    const unsigned tag = GET_TAG();
    unsigned qa = L_QH + (lane & 7) * 144 + g4 * 16;
    {
      unsigned ta = L_WQ + (8 * g4 + (i16 >> 2)) * 128 + (((2 * w + ((lane & 3) >> 1)) ^ (((i16 >> 3) | ((g4 & 1) << 1)) << 1)) * 16) + 8 * (lane & 1);
      unsigned xq = L_XN + (lane & 7) * 1040 + g4 * 16;
      unsigned qw = L_QH + (lane & 7) * 144 + (16 * w + 4 * g4) * 2;
      PIN(ta); PIN(xq); PIN(qw); PIN(qa); PIN(qb);
      if (PZE != 2) VMWAIT(2 * NT + NT / 2 + 1 + (PZE ? 0 : 4));
      wg_barrier();
      MW();
      if (fr && w < 4) {
        f32x4 c0a = {0.f, 0.f, 0.f, 0.f}, c1a = c0a;
#pragma unroll
        for (int ks = 0; ks < 16; ks += 2) {
          if (ks == 4 || ks == 10) MW();
          const v4u x0 = lds128(xq + ks * 64), x1 = lds128(xq + ks * 64 + 64);
          const v2u a0 = trread(ta + ks * 4096), a1 = trread(ta + ks * 4096 + 512), b0 = trread(ta + ks * 4096 + 4096), b1 = trread(ta + ks * 4096 + 4608);
          c0a = mfma16(v4u{a0[0], a0[1], a1[0], a1[1]}, x0, c0a);
          c1a = mfma16(v4u{b0[0], b0[1], b1[0], b1[1]}, x1, c1a);
        }
        MW();
        c0a += c1a;
        lds<v2u>(qw) = v2u{pk2(c0a[0], c0a[1]), pk2(c0a[2], c0a[3])};
      }
    }
    MW();
    wg_barrier();
    {
      const v4u q0 = lds128(qa), q1 = lds128(qa + 64);
      if (NT == 4) asm volatile("s_waitcnt vmcnt(0)" : W8(wukf, 0)::"memory");
      else asm volatile("s_waitcnt vmcnt(0)" : "+v"(wukf[0]), "+v"(wukf[1]), "+v"(wukf[2]), "+v"(wukf[3])::"memory");
      const bool last = cnt_add_rtn(C_QST) == 7u;
      if (FLAG_FIRST && last) send_flags(rs, wi, lane, tag);
      if (fr) {
        const float sc = 0.08838834764831845f * LOG2E;
#pragma unroll
        for (int u = 0; u < NT / 2; ++u) {
          f32x4 d0 = mfma16(wukf[4 * u], q0, f32x4{0.f, 0.f, 0.f, 0.f}), d1 = mfma16(wukf[4 * u + 2], q0, f32x4{0.f, 0.f, 0.f, 0.f});
          d0 = mfma16(wukf[4 * u + 1], q1, d0);
          d1 = mfma16(wukf[4 * u + 3], q1, d1);
          d0 *= sc; d1 *= sc;
          if (i16 < 8) st_sc1(rs, qb + u * 64, v4u{san(pk2(d0[0], d0[1])), pk2(d0[2], d0[3]), pk2(d1[0], d1[1]), pk2(d1[2], d1[3])});
        }
      }
      MW();
      if (!FLAG_FIRST && last) send_flags(rs, wi, lane, tag);
      if (last) cnt_add(C_SENT);
      if (KPF) {
        if (KPF_AT) wait_until(t0, KPF_AT + (w ^ 4) * KPF_ST);
        if (KPF_S) cnt_wait<1>(C_SENT, 1);
        if (KPF == 1) {
          const unsigned ko = (unsigned)(xcd * 16 + idx) * 262144u + 196608u + w * 8192 + lane * 128;
          asm volatile("global_load_dword %0, %1, %2" : "=&v"(dpf) : "v"(ko), "s"(p.ckv) : "memory");
        } else {
          const unsigned ko = min((unsigned)lane * 131072u + (w & 1) * 65536u + idx * 4096u, WS_END - 4u);
          asm volatile("s_cmp_gt_u32 %3, %4\n\ts_cbranch_scc1 1f\n\tglobal_load_dword %0, %1, %2 sc1\n1:" : "=&v"(dpf) : "v"(ko), "s"(p.ws), "s"(w), "i"(KPF - 3) : "memory", "scc");
        }
      }
      if (FPF == 3 && xcd < 6) {
        cnt_wait<1>(C_SENT, 1);
        ffn_pf(dpf);
        asm volatile("s_waitcnt vmcnt(0)" : "+v"(dpf)::"memory");
      }
    }
    if (w == 4 && (T_O == 0 || T_F == 0 || T_UV == 1)) {
      v2u f0;
      MW();
      wait_until(t0, POLL_START);
      poll_flags(p.ws, WS_FLAG + (128 + wi) * 1024 + fpos<FLW>(lane), tag, f0);
      MW();
      if (GO_DLY) wait_until(now32(), GO_DLY);
      cnt_add(C_GO);
      asm volatile("s_waitcnt vmcnt(0)" : "+v"(f0)::"memory");
    }
    MW();
    v4u wof[4][8];
    f32x4 oc[8];
    if (w >= 4) {
      const int hk = w - 4;
      if (T_O) { wait_until(t0, T_O); cnt_wait<1>(C_SENT, 1); } else cnt_wait<2>(C_GO, 1);
      v4u wor[4][8], wdr[8], wdf[8];
      {
        const unsigned vo = 8 * g4 * 1024 + i16 * 16, vd = 8 * (g4 & 1) * 1024 + i16 * 16;
        const char* so = (const char*)p.wo + (size_t)(h * 128) * 1024 + hk * 256;
        const char* sd = (const char*)p.wd + (size_t)(ffn ? 16 * wi : 0) * 1024 + hk * 256;
#pragma unroll
        for (int n = 0; n < 40; ++n) {
          if (n < 8) asm volatile("global_load_dwordx4 %0, %1, %2 nt" : "=v"(wdr[n]) : "v"(vd), "s"(sd + n * 1024) : "memory");
          else asm volatile("global_load_dwordx4 %0, %1, %2" : "=v"(wor[(n - 8) >> 3][n & 7]) : "v"(vo), "s"(so + (32 * ((((n - 8) >> 3) + j) & 3) + (n & 7)) * 1024) : "memory");
          if (n >= NFW - 1 && n < 39) VMWAIT(NFW - 1);
          if ((n & 7) == 0) MW();
        }
      }
      asm volatile("s_waitcnt vmcnt(32)" : W8(wdr, 0), "+v"(dpf)::"memory");
      transpose8(wdr, wdf);
      asm volatile("s_waitcnt vmcnt(24)" : W8(wor[0], 0)::"memory");
      transpose8(wor[0], wof[0]);
      asm volatile("s_waitcnt vmcnt(16)" : W8(wor[1], 0)::"memory");
      transpose8(wor[1], wof[1]);
      asm volatile("s_waitcnt vmcnt(8)" : W8(wor[2], 0)::"memory");
      transpose8(wor[2], wof[2]);
      asm volatile("s_waitcnt vmcnt(0)" : W8(wor[3], 0)::"memory");
      transpose8(wor[3], wof[3]);
#pragma unroll
      for (int jj = 0; jj < 8; ++jj) oc[jj] = f32x4{0.f, 0.f, 0.f, 0.f};
      if (ffn) {
        MW();
        cnt_wait<1>(C_FFN, 1);
        const v4u a = lds128(L_HF + ((lane & 7) ^ ((lane & 8) >> 2)) * 80 + g4 * 16);
#pragma unroll
        for (int jj = 0; jj < 8; ++jj) oc[jj] = mfma16(a, wdf[jj], oc[jj]);
      }
#pragma unroll
      for (int u = 0; u < 4; ++u) asm volatile("" : W8(wof[u], 0));
      asm volatile("" : W8(oc, 0));
    } else {
      MW();
      if (T_F) { wait_until(t0, T_F); cnt_wait<1>(C_SENT, 1); } else cnt_wait<2>(C_GO, 1);
      if (h < 22) {
        const char* src = (const char*)(((i16 >> 3) ^ (g4 >> 1)) ? p.wu : p.wg) + (size_t)(128 * j + 32 * w + g4) * (FF * 2) + h * 128;
#pragma unroll
        for (int t = 0; t < 8; ++t) {
          glds16<0>(src + (size_t)t * 4 * (FF * 2) + min((i16 ^ fswz(4 * t + g4)) & 7u, h == 21 ? 3u : 7u) * 16, L_GU + (32 * w + 4 * t) * 256);
          if (t & 1) MW();
        }
      }
      auto ld_uv = [&]() {
#pragma unroll
        for (int t = 0; t < 8; ++t) {
          const unsigned row = 32 * w + 4 * t + g4;
          if ((t & 1) == 0) MW();
          glds16<1>((const char*)p.wuv + (size_t)(128 * j + row) * 8192 + h * 256 + ((i16 ^ fswz(row)) * 16), L_WUV + (32 * w + 4 * t) * 256);
        }
      };
      if (!T_UV) ld_uv();
      if (h < 22) {
        if (T_UV) VMWAIT(0); else VMWAIT(8);
        MW();
        cnt_add(C_GU);
        cnt_wait(C_GU, 4);
        f32x4 ag = {0.f, 0.f, 0.f, 0.f}, au = {0.f, 0.f, 0.f, 0.f};
        const unsigned ra = 8 * g4 + (i16 >> 2), ch = 2 * w + ((lane & 3) >> 1);
        const unsigned r0 = L_GU + ra * 256 + ((ch ^ fswz(ra)) * 16) + 8 * (lane & 1), r1 = L_GU + (ra + 4) * 256 + ((ch ^ fswz(ra + 4)) * 16) + 8 * (lane & 1);
        MW();
#pragma unroll
        for (int ks = 0; ks < 4; ++ks) {
          const v4u a = lds128(L_XN + (lane & 7) * 1040 + (128 * j + 32 * ks + 8 * g4) * 2);
          const v2u b0 = trread(r0 + ks * 8192), b1 = trread(r1 + ks * 8192);
          const v2u c0 = trread((r0 ^ 128) + ks * 8192), c1 = trread((r1 ^ 128) + ks * 8192);
          if (ks == 2) MW();
          ag = mfma16(a, v4u{b0[0], b0[1], b1[0], b1[1]}, ag);
          au = mfma16(a, v4u{c0[0], c0[1], c1[0], c1[1]}, au);
        }
        MW();
        const v4u pv = __builtin_bit_cast(v4u, hh ? au : ag);
        const unsigned fo = WS_FIN + (4 * h + w) * 8192 + j * 2048 + lane * 16;
        st_sc1(rs, fo, v4u{pv[0], pv[1], pv[2], tag});
        st_sc1(rs, fo + 1024, v4u{pv[3], pv[3], pv[3], tag});
      }
      if (T_UV) {
        if (T_UV > 1) wait_until(t0, T_UV); else cnt_wait<2>(C_GO, 1);
        ld_uv();
      }
      asm volatile("s_waitcnt vmcnt(0)" : "+v"(dpf)::"memory");
      cnt_add(C_UV);
      if (ffn && w == 1) {
        v4u fv[8];
#pragma unroll 1
        for (unsigned spin = 0; spin < SPIN_CAP; ++spin) {
          MW();
          unsigned bad = 0;
#pragma unroll
          for (int t = 0; t < 8; ++t) fv[t] = ld_sc1(rs, WS_FIN + wi * 8192 + t * 1024 + lane * 16);
#pragma unroll
          for (int t = 0; t < 8; ++t) bad |= fv[t][3] ^ tag;
          if (!__any(bad != 0u)) break;
          __builtin_amdgcn_s_sleep(FX_SLP);
          CFENCE();
        }
        MW();
        float sv[4];
#pragma unroll
        for (int e = 0; e < 4; ++e) {
          const int o = e == 3, q = e % 3;
          sv[e] = (__uint_as_float(fv[o][q]) + __uint_as_float(fv[2 + o][q])) + (__uint_as_float(fv[4 + o][q]) + __uint_as_float(fv[6 + o][q]));
        }
#pragma unroll
        for (int e = 0; e < 2; ++e) {
          SB0();
          MW();
          const auto gu = __builtin_amdgcn_permlane32_swap(__float_as_uint(sv[e]), __float_as_uint(sv[e + 2]), false, false);
          const float gg = rbf(__uint_as_float(gu[0])), uu = rbf(__uint_as_float(gu[1]));
          const float inner = 0.7978845608028654f * (gg + 0.044715f * gg * gg * gg);
          const float th = 1.f - 2.f / (__expf(2.f * inner) + 1.f);
          SB0();
          MW();
          const float ge = rbf(0.5f * gg * (1.f + th));
          lds<u16>(L_HF + (4 * (g4 & 1) + 2 * hh + e) * 80 + i16 * 2) = (u16)(pk2(ge * uu, 0.f) & 0xFFFFu);
        }
        if (lane < 16) lds<v4u>(L_HF + (lane >> 1) * 80 + 32 + (lane & 1) * 16) = v4u{0u, 0u, 0u, 0u};
        MW();
        cnt_add(C_FFN);
      }
    }
    v4u uvf[4];
    MW();
    cnt_wait<2>(C_UV, 4);
#pragma unroll
    for (int ks = 0; ks < 4; ++ks) {
      const unsigned ra = 32 * (2 * (ks & 1) + (g4 >> 1)) + 16 * (ks >> 1) + 8 * (g4 & 1) + (i16 >> 2), rb = ra + 4, ch = 2 * w + ((lane & 3) >> 1);
      const v2u b0 = trread(L_WUV + ra * 256 + ((ch ^ fswz(ra)) * 16) + 8 * (lane & 1));
      const v2u b1 = trread(L_WUV + rb * 256 + ((ch ^ fswz(rb)) * 16) + 8 * (lane & 1));
      uvf[ks] = v4u{b0[0], b0[1], b1[0], b1[1]};
      PIN(uvf[ks]);
      MW();
    }
    unsigned cx = WS_CIN + wi * CIN_WI + (w ^ (CSW ? h & 7 : 0)) * 4096 + lane * 16;
    unsigned clx = L_CL + w * 272 + (((i16 >> 2) & 1) * 32 + (i16 >> 3) * 16 + (i16 & 3) * 4 + g4) * 4;
    PIN(cx); PIN(clx);
    MW();
    unsigned cla = L_CL + (lane & 7) * 272 + g4 * 16;
    unsigned cxw = L_CTX + (lane & 7) * 272 + (16 * w + 4 * g4) * 2;
    PIN(cla); PIN(cxw);
    MW();
    unsigned cxa[4];
#pragma unroll
    for (int u = 0; u < 4; ++u) { cxa[u] = L_CTX + ((lane & 7) ^ ((lane & 8) >> 2)) * 272 + (32 * ((u + j) & 3) + 8 * g4) * 2; PIN(cxa[u]); }
    unsigned eb = w >= 4 ? WS_EIN + ((4 * (g4 & 1) + 2 * hh) * 16 + 4 * (w - 4) + (i16 >> 2)) * 8192 + (EMAP ? (wi & 15) * 8 + (wi >> 4) : wi) * 64 + (i16 & 3) * 16 : WS_EIN + wi * 8192 + lane * 16;
    unsigned f2a = WS_FLAG2 + (64 * (w & 1) + lane) * 1024 + fpos<FLW2>(wi >> 1) + (wi & 1) * 4;
    PIN(f2a);
    MW();
    unsigned msk[4];
#pragma unroll
    for (int q = 0; q < 4; ++q) { msk[q] = (i16 >> 1) == 2 * q ? ((i16 & 1) ? 0xFFFF0000u : 0xFFFFu) : 0u; PIN(msk[q]); }
    MW();
    PIN(cx); PIN(clx); PIN(cla); PIN(cxw); PIN(eb);
    {
      v2u m0, m1;
      unsigned ml, cap = SPIN_CAP;
      v4u dv[4];
      const unsigned ma = WS_ML + (wi * 128 + w * 16) * 16 + (lane & 31) * 8;
#define MLD(r) "global_atomic_add_x2 " r ", %8, %9, %10 sc0 sc1\n\t"
#define DLD "s_mov_b64 exec, -1\n\tglobal_load_dwordx4 %0, %11, %10 sc1\n\tglobal_load_dwordx4 %1, %11, %10 offset:1024 sc1\n\t" \
            "global_load_dwordx4 %2, %11, %10 offset:2048 sc1\n\tglobal_load_dwordx4 %3, %11, %10 offset:3072 sc1"
#define MLOPS : "=&v"(dv[0]), "=&v"(dv[1]), "=&v"(dv[2]), "=&v"(dv[3]), "=&{v[248:249]}"(m0), "=&{v[250:251]}"(m1), "=&v"(ml), "+s"(cap) \
              : "v"(ma), "v"(v2u{0u, 0u}), "s"(p.ws), "v"(cx), "s"(tag), "s"(0xFFFFFFFFull) : "memory", "vcc", "scc"
      if (MLDP == 2)
        asm volatile("s_mov_b64 exec, %13\n\t" MLD("v[248:249]") "s_sleep " STR(MLG) "\n1:\n\t" MLD("v[250:251]")
                     "s_waitcnt vmcnt(1)\n\tv_cmp_ne_u32 vcc, %12, v249\n\tv_mov_b32 %6, v248\n\ts_mov_b64 exec, vcc\n\ts_cbranch_vccz 2f\n\t" MLD("v[248:249]")
                     "s_sub_u32 %7, %7, 1\n\ts_waitcnt vmcnt(1)\n\tv_cmp_ne_u32 vcc, %12, v251\n\tv_mov_b32 %6, v250\n\ts_cselect_b64 vcc, 0, vcc\n\t"
                     "s_mov_b64 exec, vcc\n\ts_cbranch_vccnz 1b\n2:\n\t" DLD MLOPS);
      else
        asm volatile("s_mov_b64 exec, %13\n1:\n\t" MLD("v[248:249]") "s_sub_u32 %7, %7, 1\n\ts_waitcnt vmcnt(0)\n\t"
                     "v_cmp_ne_u32 vcc, %12, v249\n\tv_mov_b32 %6, v248\n\ts_cselect_b64 vcc, 0, vcc\n\t" MLX "s_cbranch_vccnz 1b\n\t" DLD MLOPS);
      MW();
      v4u aw[4];
      {
        const float o = xor1(__uint_as_float(ml));
        const float ms = (lane & 1) ? o : __uint_as_float(ml), lsv = (lane & 1) ? __uint_as_float(ml) : o;
        float M = rowmax(ms);
        M = rs_max16(M, M);
        const float ee = ex2(ms - M);
        const float wsv = 2.f * ee / add16(rowsum(ee * lsv));
        unsigned wb = pk2(wsv, wsv);
        PIN(wb);
        MW();
#pragma unroll
        for (int t = 0; t < 4; ++t) {
          const unsigned u = __shfl(wb, 8 * t + 2 * g4);
          aw[t] = v4u{u & msk[0], u & msk[1], u & msk[2], u & msk[3]};
          PIN(aw[t]);
          if (t & 1) MW();
        }
      }
      asm volatile("s_waitcnt vmcnt(0)" : "+v"(dv[0]), "+v"(dv[1]), "+v"(dv[2]), "+v"(dv[3]), "+{v[248:249]}"(m0), "+{v[250:251]}"(m1)::"memory");
      if (RARE(__any(max(max(dv[0][0], dv[1][0]), max(dv[2][0], dv[3][0])) == POISON))) {
#pragma unroll 1
        for (unsigned spin = 0; spin < SPIN_CAP; ++spin) {
          MW();
#pragma unroll
          for (int t = 0; t < 4; ++t) dv[t] = ld_sc1(rs, cx + t * 1024);
          if (!__any(max(max(dv[0][0], dv[1][0]), max(dv[2][0], dv[3][0])) == POISON)) break;
          CFENCE();
        }
      }
      MW();
      f32x4 dx = {0.f, 0.f, 0.f, 0.f}, dy = dx;
#pragma unroll
      for (int t = 0; t < 2; ++t) { dx = mfma16(aw[t], dv[t], dx); dy = mfma16(aw[t + 2], dv[t + 2], dy); }
      lds<unsigned>(clx) = pk2(dx[0] + dy[0], dx[1] + dy[1]);
    }
    wg_barrier();
    MW();
    if (F2AT == 0 && (w == 2 || w == 3)) st_sc1_32(rs, f2a, tag);
    {
      v4u a[4];
#pragma unroll
      for (int ks = 0; ks < 4; ++ks) a[ks] = lds128(cla + ks * 64);
      SB0();
      f32x4 c0 = mfma16(uvf[0], a[0], f32x4{0.f, 0.f, 0.f, 0.f}), c1 = mfma16(uvf[1], a[1], f32x4{0.f, 0.f, 0.f, 0.f});
      c0 = mfma16(uvf[2], a[2], c0);
      c1 = mfma16(uvf[3], a[3], c1);
      c0 += c1;
      lds<v2u>(cxw) = v2u{pk2(c0[0], c0[1]), pk2(c0[2], c0[3])};
    }
    wg_barrier();
    MW();
    if (__builtin_expect(w >= 4, 1)) {
      v4u a[4];
#pragma unroll
      for (int u = 0; u < 4; ++u) a[u] = lds128(cxa[u]);
      SB0();
#pragma unroll
      for (int u = 0; u < 4; ++u) {
#pragma unroll
        for (int jj = 0; jj < 8; ++jj) oc[jj] = mfma16(a[u], wof[u][jj], oc[jj]);
        if (u & 1) { SB0(); MW(); }
      }
#pragma unroll
      for (int e = 0; e < 2; ++e)
        __builtin_amdgcn_raw_buffer_store_b128(v4u{san(pk2(oc[0][e], oc[1][e])), san(pk2(oc[2][e], oc[3][e])), san(pk2(oc[4][e], oc[5][e])), san(pk2(oc[6][e], oc[7][e]))},
                                               rs, eb, e * 16 * 8192, 16);
    } else if (w == 0) {
      const unsigned oo = wi * 64 + (i16 & 3) * 16 + g4 * 4;
      const unsigned xf = *(const GAS unsigned*)((const char*)p.x + oo);
      const unsigned one = (i16 & 2) ? 0u : ((i16 & 1) ? 0x3F800000u : 0x3F80u);
      v4u sel;
#pragma unroll
      for (int q = 0; q < 4; ++q) sel[q] = (i16 >> 2) == q ? one : 0u;
      v2u f0;
      unsigned eb2 = eb + 4096;
      float x0 = bf2f(xf), x1 = bfhi(xf);
      PIN(sel); PIN(eb2); PIN(x0); PIN(x1);
      MW();
      poll_flags(p.ws, WS_FLAG2 + wi * 1024 + fpos<FLW2>(lane), tag, f0);
      MW();
      if (E_DLY) __builtin_amdgcn_s_sleep(E_DLY);
      float r0, r1;
      v4u ev[8];
      auto esum = [&]() AI {
#pragma unroll
        for (int t = 0; t < 8; ++t) ev[t] = ld_sc1(rs, (t < 4 ? eb : eb2) + (t & 3) * 1024);
        SB0();
        f32x4 d[4];
#pragma unroll
        for (int t = 0; t < 8; ++t) d[t & 3] = mfma16(sel, ev[t], t < 4 ? f32x4{0.f, 0.f, 0.f, 0.f} : d[t & 3]);
        MW();
        r0 = (d[0][0] + d[1][0]) + (d[2][0] + d[3][0]); r1 = (d[0][1] + d[1][1]) + (d[2][1] + d[3][1]);
        r0 += ror16<8>(r0); r1 += ror16<8>(r1);
        r0 += ror16<4>(r0); r1 += ror16<4>(r1);
      };
      esum();
      if (RARE(__any(r0 != r0))) {
#pragma unroll 1
        for (unsigned spin = 0; spin < SPIN_CAP; ++spin) {
          MW();
          unsigned mxv = 0;
#pragma unroll
          for (int t = 0; t < 8; ++t) mxv = max(mxv, max(max(ev[t][0], ev[t][1]), max(ev[t][2], ev[t][3])));
          if (__all(mxv != POISON)) break;
          CFENCE();
          MW();
          esum();
          if (!__any(r0 != r0)) break;
        }
      }
      MW();
      asm volatile("s_mov_b64 exec, %3\n\tglobal_store_dword %0, %1, %2\n\ts_mov_b64 exec, -1" ::"v"(oo), "v"(pk2(x0 + r0, x1 + r1)), "s"(p.out), "s"(0x000F000F000F000Full) : "memory");
      asm volatile("s_waitcnt vmcnt(0)" : "+v"(f0)::"memory");
    } else if (w >= 2) {
      MW();
      if (F2AT == 1) st_sc1_32(rs, f2a, tag);
      if (wi == 0 && tid == 128) st_sc1_32(rs, WS_GEN, gen1);
    }
  }
}

extern "C" void kernel_launch(void* const* d_in, const int* in_sizes, int n_in, void* d_out, int out_size, void* d_ws,
                              size_t ws_size, hipStream_t stream) {
  Params p;
  p.x = (const u16*)d_in[0];
  p.ckv = (const u16*)d_in[1];
  p.wq = (const u16*)d_in[2];
  p.wuk = (const u16*)d_in[3];
  p.wuv = (const u16*)d_in[4];
  p.wo = (const u16*)d_in[5];
  p.wg = (const u16*)d_in[7];
  p.wu = (const u16*)d_in[8];
  p.wd = (const u16*)d_in[9];
  p.out = (u16*)d_out;
  p.ws = (unsigned char*)d_ws;
  (void)hipFuncSetAttribute((const void*)fused_kernel, hipFuncAttributeMaxDynamicSharedMemorySize, LDS_BYTES);
  fused_kernel<<<256, 512, LDS_BYTES, stream>>>(p);
}
```

```cpp
#include <hip/hip_runtime.h>
#include <stdint.h>

typedef unsigned short u16;
typedef unsigned long long u64;
typedef unsigned v4u __attribute__((ext_vector_type(4)));
typedef unsigned v2u __attribute__((ext_vector_type(2)));
typedef float f32x4 __attribute__((ext_vector_type(4)));
typedef float f32x16 __attribute__((ext_vector_type(16)));
typedef unsigned v16u __attribute__((ext_vector_type(16)));
typedef float v2f __attribute__((ext_vector_type(2)));
typedef short bf16x8 __attribute__((ext_vector_type(8)));
typedef short v4s __attribute__((ext_vector_type(4)));
typedef __bf16 v2bf __attribute__((ext_vector_type(2)));
#define GAS __attribute__((address_space(1)))
#define LAS __attribute__((address_space(3)))
#define DI __device__ __forceinline__

#define NF 12
#define R2AT (47 + NF)
#define POLL_START 250
#define POLL_GAP 4
#define Q_DLY 0
#define KV_DLY 50
#define E_DLY 10
#define T_F 450
#define T_UV 900
#define T_O 680
#define MLAT 17
#define FR4 1
#define PAUSE 1
#define QPF 0
#define WPF 2
#define BPF 0
#define BPF_DLY 0
#define WPF_DLY 0
#define FMAP 0
#define OD 6
#define V3 4
#define QPRIO 1
#define PK 8
#define WM_DLY 110
#define FPF 0
#define FLAG_AT 1
#define FL_DLY 0
#define PZ_DLY 0
#define B0W 2
#define NF2 16
#define PMIN 32
#define VD 2
#define MIDSIG 1
#define V2AT 0
#define CKAT 10
#define PPRIO 0
#define SPAT 13
#define MIX 0
#define PROG 1
#define RES 1
#define XAT 4
#define PUTAT 3
#define FLAG_FIRST 0
#define F2AT 0
#define GO_DLY 100
#define C_DLY 0
#define KPF 0
#define KPF_AT 0
#define KPF_S 1
#define FX_SLP 8
#define OSAT 11
#define PZE 0
#define PZT 0
#define PZ_AT 150
#define K1AT 1
#define CSW 1
#define FLW 0
#define FLW2 0
#define BMAP 0
#define EMAP 0
#define G0T 1
#define KPF_ST 0
#define K3S 0
#define QST 0
#define F2D 3
#define F2G 4
#define F2X 1
#define QPM 1
#define MLDP 1
#define OVTV 88.f
#define CS2 1
#define MS2 1
#define ES2 1
#define VSP 1
#define CVW 1
#define KSP 1
#define QL 4
#define YB 5
#define XR 15
#define XC 20
#define MLP 1
#define MLG 10
#define NFW 10

constexpr int DM = 512, FF = 1376;
constexpr unsigned WS_GEN = 0;
constexpr unsigned WS_FLAG = 4096;
constexpr unsigned WS_FLAG2 = WS_FLAG + 262144;
constexpr unsigned WS_QABS = WS_FLAG2 + 131072;
constexpr unsigned WS_ML = WS_QABS + 524288;
constexpr unsigned WS_EIN = WS_ML + 262144;
constexpr unsigned WS_CIN = WS_EIN + 1048576;
constexpr unsigned CIN_ROW = 256, CIN_WI = 128 * CIN_ROW;
constexpr unsigned WS_FIN = WS_CIN + 128 * CIN_WI;
constexpr unsigned WS_END = WS_FIN + 88 * 8192;
constexpr unsigned L_Q = 0;
constexpr unsigned L_P = L_Q;
constexpr unsigned L_LS = L_Q + 24576;
constexpr unsigned L_AL = L_Q + 25600;
constexpr unsigned L_SLOT = 32768;
constexpr unsigned L_XN = 0;
constexpr unsigned L_QH = 8448;
constexpr unsigned L_CL = L_QH + 2176;
constexpr unsigned L_CTX = L_CL + 2176;
constexpr unsigned L_HF = L_CTX + 2176;
constexpr unsigned L_FP = L_HF + 640;
constexpr unsigned L_WQ = 32768;
constexpr unsigned L_GU = 65536;
constexpr unsigned L_WUV = 98304;
constexpr unsigned L_CNT = 32512;
constexpr unsigned LDS_BYTES = 163840;
enum { C_P = 0, C_OVF = 1, C_ST = 2, C_FL = 3, C_MX = 5, C_PVD, C_XS, C_MASK = 8, C_N = 16 };
enum { C_QST = 0, C_GU, C_FFN, C_UV, C_GO, C_SENT };

constexpr unsigned ANY = 0x80u;
constexpr unsigned POISON = 0xFFFFFFFFu;
constexpr unsigned SPIN_CAP = 1u << 20;

struct Params {
  const u16 *x, *ckv, *wq, *wuk, *wuv, *wo, *wg, *wu, *wd;
  u16* out;
  unsigned char* ws;
};

DI float bf2f(unsigned v) { return __uint_as_float(v << 16); }
DI float bfhi(unsigned v) { return __uint_as_float(v & 0xFFFF0000u); }
DI unsigned pk2(float a, float b) {
  v2f t = {a, b};
  return __builtin_bit_cast(unsigned, __builtin_convertvector(t, v2bf));
}
DI float rbf(float a) { return bf2f(pk2(a, 0.f) & 0xFFFFu); }
DI unsigned san(unsigned v) { return min(v, 0xFFFFFFFEu); }
DI f32x4 mfma16(v4u a, v4u b, f32x4 c) {
  return __builtin_amdgcn_mfma_f32_16x16x32_bf16(__builtin_bit_cast(bf16x8, a), __builtin_bit_cast(bf16x8, b), c, 0, 0, 0);
}
DI f32x16 mfma32(v4u a, v4u b, f32x16 c) {
  return __builtin_amdgcn_mfma_f32_32x32x16_bf16(__builtin_bit_cast(bf16x8, a), __builtin_bit_cast(bf16x8, b), c, 0, 0, 0);
}
DI v4u lds128(unsigned a) { return *(LAS v4u*)(size_t)a; }
DI v2u trread(unsigned a) { return __builtin_bit_cast(v2u, __builtin_amdgcn_ds_read_tr16_b64_v4i16((LAS v4s*)(size_t)a)); }
template <class T> DI LAS T& lds(unsigned a) { return *(LAS T*)(size_t)a; }
template <int POL> DI void glds16(const void* gsrc, unsigned lds_dst) {
  unsigned keep;
  if (POL == 1) asm volatile("s_mov_b32 %0, m0\n\ts_mov_b32 m0, %2\n\ts_nop 0\n\tglobal_load_lds_dwordx4 %1, off nt\n\ts_mov_b32 m0, %0" : "=&s"(keep) : "v"(gsrc), "s"(lds_dst) : "memory");
  else if (POL == 2) asm volatile("s_mov_b32 %0, m0\n\ts_mov_b32 m0, %2\n\ts_nop 0\n\tglobal_load_lds_dwordx4 %1, off sc1\n\ts_mov_b32 m0, %0" : "=&s"(keep) : "v"(gsrc), "s"(lds_dst) : "memory");
  else asm volatile("s_mov_b32 %0, m0\n\ts_mov_b32 m0, %2\n\ts_nop 0\n\tglobal_load_lds_dwordx4 %1, off\n\ts_mov_b32 m0, %0" : "=&s"(keep) : "v"(gsrc), "s"(lds_dst) : "memory");
}
DI void glds16s(unsigned voff, const void* sbase, unsigned lds_dst) {
  unsigned keep;
  asm volatile("s_mov_b32 %0, m0\n\ts_mov_b32 m0, %3\n\ts_nop 0\n\tglobal_load_lds_dwordx4 %1, %2 nt\n\ts_mov_b32 m0, %0" : "=&s"(keep) : "v"(voff), "s"(sbase), "s"(lds_dst) : "memory");
}
DI void gld16s(v4u& d, unsigned voff, const void* sbase) {
  asm volatile("global_load_dwordx4 %0, %1, %2 nt" : "=v"(d) : "v"(voff), "s"(sbase) : "memory");
}
#define VMWAIT(n) asm volatile("s_waitcnt vmcnt(%0)" ::"i"(n) : "memory")
#define LGKM0() asm volatile("s_waitcnt lgkmcnt(0)" ::: "memory")
#define CFENCE() asm volatile("" ::: "memory")
#define SB0() __builtin_amdgcn_sched_barrier(0)
#define W8(a, o) "+v"(a[o]), "+v"(a[o + 1]), "+v"(a[o + 2]), "+v"(a[o + 3]), "+v"(a[o + 4]), "+v"(a[o + 5]), "+v"(a[o + 6]), "+v"(a[o + 7])
#define WARMUP 1
#define MK(n) asm volatile("7:\n\ts_cbranch_execz .Lwm_ret\n\t.subsection " #n "\n\ts_branch 7b\n\t.subsection 0")
#define MK_TABLE() asm volatile(".subsection 1\n.Lwm_ret:\n\ts_setpc_b64 s[96:97]\n.Lwm_w:\n\t.subsection 3\n.Lwm_q:\n\t.subsection 5\n.Lwm_p:\n\t" \
                                ".subsection 7\n.Lwm_s:\n\t.subsection 9\n.Lwm_e:\n\t.fill 128, 4, 0xBF810000\n\t.subsection 0")
#define WARM(A, B, first, step) do { u64 keep_; unsigned off_ = (first) * 4u; \
  asm volatile("s_mov_b64 %0, exec\n\ts_mov_b64 exec, 0\n\ts_getpc_b64 s[96:97]\n8:\n\t" \
               "s_cmp_ge_u32 %1, .Lwm_" #B "-.Lwm_" #A "\n\ts_cbranch_scc1 9f\n\t" \
               "s_add_u32 s94, %1, .Lwm_" #A "-8b\n\ts_add_u32 s94, s94, s96\n\ts_addc_u32 s95, s97, 0\n\t" \
               "s_add_u32 %1, %1, %2\n\ts_setpc_b64 s[94:95]\n9:\n\ts_mov_b64 exec, %0" \
               : "=&s"(keep_), "+s"(off_) : "i"((step) * 4) : "s94", "s95", "s96", "s97", "scc"); } while (0)

#define MW() MK(2)
#define MQ() MK(4)
#define MP() MK(6)
#define MS() MK(8)

DI void wg_barrier() {
  LGKM0();
  __builtin_amdgcn_s_barrier();
  CFENCE();
}
DI void cnt_addv(unsigned c, unsigned v) {
  asm volatile("s_mov_b64 exec, 1\n\tds_add_u32 %0, %1 offset:%2\n\ts_mov_b64 exec, -1" ::"v"(0u), "v"(v), "i"(L_CNT + c * 4u) : "memory");
}
DI void cnt_add(unsigned c) { cnt_addv(c, 1u); }
DI void cnt_add2(unsigned c, unsigned v0, unsigned v1) {
  asm volatile("s_mov_b64 exec, 1\n\tds_add_u64 %0, %1 offset:%2\n\ts_mov_b64 exec, -1" ::"v"(0u), "v"(v2u{v0, v1}), "i"(L_CNT + c * 4u) : "memory");
}
#define AI __attribute__((always_inline))
#define RARE(c) __builtin_expect(!!(c), 0)
DI unsigned vm_pending() {
  unsigned v;
  asm volatile("s_getreg_b32 %0, hwreg(HW_REG_IB_STS)" : "=s"(v));
  return (v & 15u) | ((v >> 18) & 48u);
}
#define STR_(x) #x
#define STR(x) STR_(x)
#define VW1(n) "s_waitcnt vmcnt(" #n ")\n\ts_branch 2f\n\t"
#define MKR "7:\n\ts_cbranch_execz .Lwm_ret\n\t.subsection 8\n\ts_branch 7b\n\t.subsection 10\n\t"
#define MKB "7:\n\ts_cbranch_execz .Lwm_ret\n\t.subsection 6\n\ts_branch 7b\n\t.subsection 11\n\t"
#define L_CNT_ 32512
#define KV_RUN_BODY() asm volatile(".subsection 10\n" \
  MKR ".Lkv_chk:\n\t" \
  "s_mov_b32 s67, 1\n\ts_mov_b32 s91, m0\n\ts_mov_b32 s90, 0xffffff\n\ts_branch 12f\n" \
  ".Lkv_runp:\n\t" \
  "s_mov_b32 s67, 1\n\ts_branch 11f\n" \
  ".Lkv_run:\n\t" \
  "s_mov_b32 s67, 2\n" \
  "11:\n\t" \
  "s_mov_b32 s91, m0\n\ts_mov_b32 s90, 0xffffff\n\ts_mov_b32 s64, -1\n" \
  "3:\n\t" \
  "v_mov_b32 v252, 0\n\t" \
  "ds_read_b128 v[252:255], v252 offset:%0\n\t" \
  "s_cmp_lg_u32 s67, 1\n\ts_cbranch_scc1 12f\n\t" \
  "v_add_u32 v249, s74, v250\n\t" \
  "s_bitcmp1_b32 s74, 12\n\ts_cbranch_scc1 14f\n\t" \
  "ds_read_b128 v[232:235], v249\n\tds_read_b128 v[236:239], v249 offset:1024\n\tds_read_b128 v[240:243], v249 offset:2048\n\tds_read_b128 v[244:247], v249 offset:3072\n\t" \
  "s_branch 12f\n" \
  "14:\n\t" \
  "ds_read_b128 v[216:219], v249\n\tds_read_b128 v[220:223], v249 offset:1024\n\tds_read_b128 v[224:227], v249 offset:2048\n\tds_read_b128 v[228:231], v249 offset:3072\n" \
  "12:\n\t" \
  "s_cmp_eq_u32 s77, 4\n\ts_cbranch_scc1 6f\n\t" \
  "s_getreg_b32 s87, hwreg(HW_REG_IB_STS)\n\t" \
  "s_lshl_b32 s89, s77, 4\n\ts_add_u32 s89, s89, 16\n\t" \
  "s_lshr_b32 s88, s87, 18\n\ts_and_b32 s88, s88, 48\n\ts_and_b32 s87, s87, 15\n\ts_or_b32 s87, s87, s88\n\t" \
  "s_add_u32 s89, s89, s87\n\ts_cmp_lt_u32 s76, s89\n\ts_cbranch_scc1 4f\n\t" \
  "s_min_u32 s88, s87, 23\n\ts_lshl_b32 s88, s88, 3\n\t" \
  "s_getpc_b64 s[94:95]\n0:\n\ts_add_u32 s88, s88, 1f-0b\n\ts_add_u32 s94, s94, s88\n\ts_addc_u32 s95, s95, 0\n\ts_setpc_b64 s[94:95]\n" \
  MKR "1:\n\t" \
  VW1(0) VW1(1) VW1(2) VW1(3) VW1(4) VW1(5) VW1(6) VW1(7) VW1(8) VW1(9) VW1(10) VW1(11) VW1(12) VW1(13) VW1(14) VW1(15) \
  VW1(16) VW1(17) VW1(18) VW1(19) VW1(20) VW1(21) VW1(22) VW1(23) \
  MKR "2:\n\t" \
  "s_lshl_b32 s88, s77, 2\n\ts_add_u32 s88, s88, s83\n\ts_lshl_b32 s88, 1, s88\n\t" \
  "v_mov_b32 v248, 0\n\tv_mov_b32 v249, s88\n\ts_mov_b64 exec, 1\n\tds_add_u32 v248, v249 offset:%1\n\ts_mov_b64 exec, -1\n\t" \
  "s_add_u32 s77, s77, 1\n" \
  "4:\n\t" \
    \
  "s_lshr_b32 s89, s86, s83\n\ts_and_b32 s89, s89, 0x11000000\n\ts_and_b32 s88, s86, 0x440000\n\ts_or_b32 s89, s89, s88\n\t" \
  "s_cmp_eq_u32 s89, 0x11440000\n\ts_cselect_b32 s88, 64, 48\n\t" \
  "s_and_b32 s89, s89, 0x1040000\n\ts_cmp_eq_u32 s89, 0x1040000\n\ts_cselect_b32 s88, s88, 32\n\t" \
  "s_and_b32 s89, s75, %2 | 0x100\n\ts_cmp_eq_u32 s89, 0x100\n\ts_cselect_b32 s89, " STR(PMIN) ", 64\n\ts_min_u32 s88, s88, s89\n\t" \
  "s_and_b32 s89, s75, %2\n\ts_cselect_b32 s89, " STR(NF2) ", " STR(NF) "\n\t" \
  "s_add_u32 s89, s89, s76\n\ts_sub_u32 s89, s89, s87\n\ts_min_u32 s88, s88, s89\n\t" \
  "s_cmp_ge_i32 s76, s88\n\ts_cbranch_scc1 6f\n" \
  "5:\n\t" \
  "v_readlane_b32 s89, v251, s76\n\ts_add_u32 s76, s76, 1\n\ts_and_b32 s87, s89, 0x1fc00\n\tv_xor_b32 v249, s89, v250\n\t" \
  "s_add_u32 m0, s87, s82\n\ts_cmp_lt_u32 s76, s88\n\tglobal_load_lds_dwordx4 v249, s[80:81] nt\n\ts_cbranch_scc1 5b\n\t" \
  "6:\n\t" MKR \
  "s_waitcnt lgkmcnt(0)\n\t" \
  "v_readfirstlane_b32 s85, v252\n\tv_readfirstlane_b32 s86, v254\n\tv_readfirstlane_b32 s84, v253\n\tv_readfirstlane_b32 s75, v255\n\t" \
  "s_bitset1_b32 s85, 7\n\ts_and_b32 s89, s86, s78\n\ts_cmp_eq_u32 s89, s78\n\ts_cbranch_scc0 9f\n\ts_and_b32 s89, s85, s79\n\ts_cbranch_scc0 9f\n\t" \
  "s_cmp_lg_u32 s67, 0\n\ts_cbranch_scc1 10f\n" \
  "9:\n\ts_cmp_eq_u32 s67, 2\n\ts_cbranch_scc1 13f\n\ts_and_b32 s89, s85, s68\n\ts_cselect_b32 s67, 1, 0\n" \
  "13:\n\ts_mov_b32 s89, s79\n\ts_cmp_eq_u32 s76, s64\n\ts_cbranch_scc1 17f\n\ts_sub_u32 s90, s90, 1\n\ts_cbranch_scc0 3b\n" \
  "10:\n\ts_mov_b32 s64, 0\n" \
  "17:\n\ts_mov_b32 m0, s91\n\ts_mov_b32 s87, s64\n" \
  "15:\n\ts_cmp_eq_u32 s77, 2\n\ts_cselect_b32 s66, 0, 15\n\ts_or_b32 s66, s66, 0xc00000\n\t" \
  "s_lshr_b32 s65, s77, 2\n\ts_cmp_eq_u32 s76, 64\n\ts_cselect_b32 s65, s65, 1\n\ts_lshl_b32 s65, s65, 8\n\ts_cmp_eq_u32 s87, 0\n\ts_setpc_b64 s[92:93]\n" \
  MKR ".Lkv_sig:\n\t" \
  "s_waitcnt vmcnt(16)\n\ts_cmp_eq_u32 s77, 2\n\ts_cbranch_scc1 16f\n\ts_waitcnt vmcnt(0)\n" \
  "16:\n\ts_lshl_b32 s88, s77, 2\n\ts_add_u32 s88, s88, s83\n\ts_lshl_b32 s88, 1, s88\n\t" \
  "v_mov_b32 v248, 0\n\tv_mov_b32 v249, s88\n\ts_mov_b64 exec, 1\n\tds_add_u32 v248, v249 offset:%1\n\ts_mov_b64 exec, -1\n\t" \
  "s_add_u32 s77, s77, 1\n\ts_mov_b32 s87, 0\n\ts_cmp_lg_u32 s77, 4\n\ts_cbranch_scc1 15b\n\ts_cmp_eq_u32 s98, 0\n\ts_cbranch_scc1 15b\n" \
    \
  ".Lkv_y3:\n\ts_mov_b32 s88, s98\n\tv_mov_b32 v248, 0\n" \
  "20:\n\tds_read_b32 v249, v248 offset:%1\n\ts_sub_u32 s88, s88, 1\n\ts_cbranch_scc1 21f\n\ts_waitcnt lgkmcnt(0)\n\t" \
  "v_and_b32 v249, 0xf000, v249\n\tv_cmp_ne_u32 vcc, 0xf000, v249\n\ts_cbranch_vccnz 20b\n\ts_sleep " STR(VD) "\n" \
  MKR ".Lkv_ldv:\n\t" \
  "v_xor_b32 v248, 0, v215\n\tv_xor_b32 v249, 32, v215\n\t" \
  "ds_read_b64_tr_b16 v[128:129], v248 offset:0\n\tds_read_b64_tr_b16 v[130:131], v249 offset:4096\n\t" \
  "ds_read_b64_tr_b16 v[132:133], v248 offset:16384\n\tds_read_b64_tr_b16 v[134:135], v249 offset:20480\n\t" \
  "ds_read_b64_tr_b16 v[136:137], v248 offset:32768\n\tds_read_b64_tr_b16 v[138:139], v249 offset:36864\n\t" \
  "ds_read_b64_tr_b16 v[140:141], v248 offset:49152\n\tds_read_b64_tr_b16 v[142:143], v249 offset:53248\n\t" \
  "v_xor_b32 v248, 64, v215\n\tv_xor_b32 v249, 96, v215\n\t" \
  "ds_read_b64_tr_b16 v[144:145], v248 offset:0\n\tds_read_b64_tr_b16 v[146:147], v249 offset:4096\n\t" \
  "ds_read_b64_tr_b16 v[148:149], v248 offset:16384\n\tds_read_b64_tr_b16 v[150:151], v249 offset:20480\n\t" \
  "ds_read_b64_tr_b16 v[152:153], v248 offset:32768\n\tds_read_b64_tr_b16 v[154:155], v249 offset:36864\n\t" \
  "ds_read_b64_tr_b16 v[156:157], v248 offset:49152\n\tds_read_b64_tr_b16 v[158:159], v249 offset:53248\n\t" \
  MKR "v_xor_b32 v248, 128, v215\n\tv_xor_b32 v249, 160, v215\n\t" \
  "ds_read_b64_tr_b16 v[160:161], v248 offset:0\n\tds_read_b64_tr_b16 v[162:163], v249 offset:4096\n\t" \
  "ds_read_b64_tr_b16 v[164:165], v248 offset:16384\n\tds_read_b64_tr_b16 v[166:167], v249 offset:20480\n\t" \
  "ds_read_b64_tr_b16 v[168:169], v248 offset:32768\n\tds_read_b64_tr_b16 v[170:171], v249 offset:36864\n\t" \
  "ds_read_b64_tr_b16 v[172:173], v248 offset:49152\n\tds_read_b64_tr_b16 v[174:175], v249 offset:53248\n\t" \
  "v_xor_b32 v248, 192, v215\n\tv_xor_b32 v249, 224, v215\n\t" \
  "ds_read_b64_tr_b16 v[176:177], v248 offset:0\n\tds_read_b64_tr_b16 v[178:179], v249 offset:4096\n\t" \
  "ds_read_b64_tr_b16 v[180:181], v248 offset:16384\n\tds_read_b64_tr_b16 v[182:183], v249 offset:20480\n\t" \
  "ds_read_b64_tr_b16 v[184:185], v248 offset:32768\n\tds_read_b64_tr_b16 v[186:187], v249 offset:36864\n\t" \
  "ds_read_b64_tr_b16 v[188:189], v248 offset:49152\n\tds_read_b64_tr_b16 v[190:191], v249 offset:53248\n\t" \
  "s_mov_b32 s99, 1\n\ts_mov_b32 s87, 0\n\ts_branch 15b\n" \
  "21:\n\ts_waitcnt lgkmcnt(0)\n\ts_mov_b32 s87, 0\n\ts_cmp_lt_u32 s98, 0x1000\n\ts_cbranch_scc1 15b\n\ts_branch .Lkv_ldv\n\t" \
  ".subsection 12\n\t.fill 64, 4, 0xBF810000\n\t.subsection 0" \
  :: "i"(L_CNT), "i"(L_CNT + C_ST * 4), "i"(RES ? 4 : 0x40))
DI unsigned cnt_add_rtn(unsigned c) {
  u64 keep;
  unsigned v;
  asm volatile("s_mov_b64 %0, exec\n\ts_mov_b64 exec, 1\n\tds_add_rtn_u32 %1, %2, %3 offset:%4\n\ts_waitcnt lgkmcnt(0)\n\ts_mov_b64 exec, %0"
               : "=&s"(keep), "=&v"(v) : "v"(c * 4u), "v"(1u), "i"(L_CNT) : "memory");
  return __builtin_amdgcn_readfirstlane(v);
}
DI unsigned cnt_get(unsigned c) {
  unsigned v;
  asm volatile("ds_read_b32 %0, %1 offset:%2\n\ts_waitcnt lgkmcnt(0)" : "=v"(v) : "v"(c * 4u), "i"(L_CNT) : "memory");
  return __builtin_amdgcn_readfirstlane(v);
}
template <int SLP = 0> DI void cnt_wait(unsigned c, unsigned target) {
#pragma unroll 1
  for (unsigned s = 0; s < SPIN_CAP * 16u; ++s) {
    if (cnt_get(c) >= target) break;
    if (SLP) __builtin_amdgcn_s_sleep(SLP);
  }
}
DI unsigned now32() { return (unsigned)__builtin_amdgcn_s_memrealtime(); }
DI void wait_until(unsigned t0, unsigned ticks) {
  if (ticks == 0) return;
#pragma unroll 1
  while (now32() - t0 < ticks) __builtin_amdgcn_s_sleep(1);
}
DI v4u ld_sc1(__amdgpu_buffer_rsrc_t rs, unsigned off) { return __builtin_amdgcn_raw_buffer_load_b128(rs, off, 0, 16); }
DI v2u ld_sc1_64(__amdgpu_buffer_rsrc_t rs, unsigned off) { return __builtin_amdgcn_raw_buffer_load_b64(rs, off, 0, 16); }
DI void st_sc1(__amdgpu_buffer_rsrc_t rs, unsigned off, v4u v) { __builtin_amdgcn_raw_buffer_store_b128(v, rs, off, 0, 16); }
DI void st_sc1_64(__amdgpu_buffer_rsrc_t rs, unsigned off, v2u v) { __builtin_amdgcn_raw_buffer_store_b64(v, rs, off, 0, 16); }
DI void st_sc1_32(__amdgpu_buffer_rsrc_t rs, unsigned off, unsigned v) { __builtin_amdgcn_raw_buffer_store_b32(v, rs, off, 0, 16); }
DI unsigned fswz(unsigned row) { return ((row & 3u) << 2) | ((0x78u >> (2u * ((row >> 2) & 3u))) & 3u); }
template <int N> DI float ror16(float v) {
  return __int_as_float(__builtin_amdgcn_update_dpp(0, __float_as_int(v), 0x120 + N, 0xF, 0xF, false));
}
DI float xor1(float v) { return __int_as_float(__builtin_amdgcn_update_dpp(0, __float_as_int(v), 0xB1, 0xF, 0xF, false)); }
DI float add16(float v) { auto r = __builtin_amdgcn_permlane16_swap(__float_as_uint(v), __float_as_uint(v), false, false); return __uint_as_float(r[0]) + __uint_as_float(r[1]); }
DI float add32(float v) { auto r = __builtin_amdgcn_permlane32_swap(__float_as_uint(v), __float_as_uint(v), false, false); return __uint_as_float(r[0]) + __uint_as_float(r[1]); }
DI float rs_add16(float a, float b) { auto r = __builtin_amdgcn_permlane16_swap(__float_as_uint(a), __float_as_uint(b), false, false); return __uint_as_float(r[0]) + __uint_as_float(r[1]); }
DI float rs_add32(float a, float b) { auto r = __builtin_amdgcn_permlane32_swap(__float_as_uint(a), __float_as_uint(b), false, false); return __uint_as_float(r[0]) + __uint_as_float(r[1]); }
DI float rs_max16(float a, float b) { auto r = __builtin_amdgcn_permlane16_swap(__float_as_uint(a), __float_as_uint(b), false, false); return fmaxf(__uint_as_float(r[0]), __uint_as_float(r[1])); }
DI float max32(float v) { auto r = __builtin_amdgcn_permlane32_swap(__float_as_uint(v), __float_as_uint(v), false, false); return fmaxf(__uint_as_float(r[0]), __uint_as_float(r[1])); }
DI float rowsum(float v) { v += ror16<8>(v); v += ror16<4>(v); v += ror16<2>(v); v += ror16<1>(v); return v; }
DI float rowmax(float v) { v = fmaxf(v, ror16<8>(v)); v = fmaxf(v, ror16<4>(v)); v = fmaxf(v, ror16<2>(v)); v = fmaxf(v, ror16<1>(v)); return v; }
DI float ex2(float v) { return __builtin_amdgcn_exp2f(v); }
constexpr float LOG2E = 1.4426950408889634f;

DI void transpose8(const v4u (&r)[8], v4u (&c)[8]) {
#pragma unroll
  for (int q = 0; q < 4; ++q) {
    if (q == 0 || q == 2) { SB0(); MW(); }
#pragma unroll
    for (int e2 = 0; e2 < 4; ++e2) {
      unsigned a = r[2 * e2][q], b = r[2 * e2 + 1][q];
      c[2 * q][e2] = __builtin_amdgcn_perm(b, a, 0x05040100u);
      c[2 * q + 1][e2] = __builtin_amdgcn_perm(b, a, 0x07060302u);
    }
    asm volatile("" : "+v"(c[2 * q]), "+v"(c[2 * q + 1]));
  }
}
#define PLD(f) asm volatile("global_atomic_add_x2 %0, %1, %2, %3 sc0 sc1" : "=&v"(f) : "v"(fo), "v"(zz), "s"(wsp) : "memory")
#define PLDM(f) asm volatile("s_not_b64 exec, %4\n\tglobal_atomic_add_x2 %0, %1, %2, %3 sc0 sc1\n\ts_mov_b64 exec, -1" : "+v"(f) : "v"(fo), "v"(zz), "s"(wsp), "s"(seen) : "memory", "scc")
#define PL1 "global_atomic_add_x2 %0, %2, %3, %4 sc0 sc1\n\t"
#define PL1S PL1 "s_sleep " STR(F2G) "\n\t"
#define PLPRE PL1S PL1S
#define F2XS "s_mov_b64 exec, vcc\n\t"
#define MLX "s_mov_b64 exec, vcc\n\t"
DI void poll_flags(const unsigned char* wsp, unsigned fo, unsigned tag, v2u& f) {
  unsigned cap = SPIN_CAP;
  asm volatile(PLPRE "1:\n\t" PL1 "s_sub_u32 %1, %1, 1\n\ts_waitcnt vmcnt(" STR(F2D) " - 1)\n\tv_cmp_ne_u64 vcc, %5, %0\n\ts_cselect_b64 vcc, 0, vcc\n\t" F2XS
               "s_cbranch_vccnz 1b\n\ts_mov_b64 exec, -1"
               : "=&v"(f), "+s"(cap) : "v"(fo), "v"(v2u{0u, 0u}), "s"(wsp), "s"((u64)tag * 0x100000001ull) : "memory", "vcc", "scc");
}

template <int N> DI unsigned fpos(unsigned l) { return N ? (l % N) * 128 + (l / N) * 8 : l * 8; }
DI void send_flags(__amdgpu_buffer_rsrc_t rs, int wi, int lane, unsigned tag) {
#pragma unroll
  for (int t = 0; t < (T_O == 0 || T_F == 0 || T_UV == 1 ? 4 : 2); ++t) st_sc1_32(rs, WS_FLAG + (64 * t + lane) * 1024 + fpos<FLW>(wi >> 1) + (wi & 1) * 4, tag);
}

#define PIN(x) asm volatile("" : "+v"(x))
__global__ void __launch_bounds__(512, 2) fused_kernel(Params p) {
  extern __shared__ __attribute__((aligned(16))) unsigned char smem[];
  const u64 t0l = __builtin_amdgcn_s_memrealtime();
  const unsigned t0 = (unsigned)t0l;
  const int tid = threadIdx.x, lane = tid & 63;
  const int w = __builtin_amdgcn_readfirstlane(tid >> 6);
  const int cu = blockIdx.x;
  const int xcd = cu & 7, r8 = cu >> 3, idx = r8 >> 1;
  const __amdgpu_buffer_rsrc_t rs = __builtin_amdgcn_make_buffer_rsrc((void*)p.ws, 0, (int)WS_END, 0x00020000);
  const int g4 = lane >> 4, i16 = lane & 15;
  unsigned gen;
  asm volatile("s_load_dword %0, %1, 0x0" : "=s"(gen) : "s"(p.ws), "s"((unsigned)(size_t)(LAS unsigned char*)smem) : "memory");
  unsigned gen1;
#define GET_TAG() (__extension__({ asm volatile("s_waitcnt lgkmcnt(0)\n\ts_add_u32 %0, %1, 1" : "=s"(gen1) : "s"(gen) : "memory", "scc"); (gen1 & 0x3FFFu) | ((gen1 << 2) & 0x3FFF0000u); }))

  MK_TABLE();
  if ((r8 & 1) == 0) {
    const int b = BMAP ? idx & 7 : xcd, s = BMAP ? xcd + (idx & 8) : idx, kvi = b * 16 + s;
    const unsigned hh = lane >> 5, hd = lane & 31;
    WARM(w, e, idx * 8 + w, 128);
    auto wait_p = [&](unsigned pm, unsigned fm) AI {
      v4u cv;
#pragma unroll 1
      for (unsigned sp = 0; sp < SPIN_CAP * 16u; ++sp) {
        asm volatile("ds_read_b128 %0, %1 offset:%2\n\ts_waitcnt lgkmcnt(0)" : "=v"(cv) : "v"(0u), "i"(L_CNT) : "memory");
        if ((__builtin_amdgcn_readfirstlane(cv[0]) & pm) && (__builtin_amdgcn_readfirstlane(cv[3]) & fm) == fm) break;
      }
      return __builtin_amdgcn_readfirstlane(cv[1]);
    };
    auto wait_bits = [&](unsigned c, unsigned m) AI {
#pragma unroll 1
      for (unsigned sp = 0; sp < SPIN_CAP * 16u; ++sp)
        if ((cnt_get(c) & m) == m) break;
    };
    if (w >= 4) {
      const int k = w - 4;
      (void)GET_TAG();
      unsigned l16 = lane * 16, ap0;
      {
        const unsigned blk = (lane >> 4) & 1, q4 = i16 >> 2, pp = lane & 3;
        const unsigned r0 = 8 * hh + q4;
        const unsigned ch = 16 * k + 2 * (pp & 1) + blk;
        ap0 = L_SLOT + r0 * 1024 + ((ch ^ fswz(r0)) * 16) + 8 * (pp >> 1);
      }
      PIN(l16); PIN(ap0);
      v4u vf[16];
      v16u vo[4];
      unsigned ap1 = ap0 + 65536, yb = 0, vin = 0;
      auto ldv1 = [&](int n) AI {
        const v2u t0_ = trread((ap0 ^ ((n >> 2) * 64)) + (n & 3) * 16384), t1_ = trread((ap0 ^ ((n >> 2) * 64 + 32)) + (n & 3) * 16384 + 4096);
        vf[n] = v4u{t0_[0], t0_[1], t1_[0], t1_[1]};
      };
      auto ldv = [&](int r) AI {
#pragma unroll
        for (int n = 0; n < 16; ++n) {
          ldv1(n);
          if ((n & 7) == 7) { if (r < 2) MS(); else MP(); }
        }
      };
      auto vfr = [&](int r, int n) AI {
        const int q = 4 * (n & 3);
        return (r & 1) ? v4u{vo[n >> 2][q], vo[n >> 2][q + 1], vo[n >> 2][q + 2], vo[n >> 2][q + 3]} : vf[n];
      };
      const char* base = (const char*)p.ckv + ((size_t)kvi * 256 + 16 * k) * 1024;
      unsigned xt = ((lane >> 4) << 16) | (i16 << 10) | (fswz(i16) << 4);
      unsigned ni = 0, sg = 0, ovf = 0, pc = 0, st = 0, fl = 0, cm = 0, cw = 0;
      KV_RUN_BODY();
      f32x16 oacc[4];
      v4u pf[2][4], cv4;
#define CALL0(L) "s_getpc_b64 s[92:93]\n\ts_cbranch_scc0 " L
#define RUN_TXT(L) "s_cmp_lg_u32 s76, s76\n\t" CALL0(L)
#define SIG_TXT "s_getreg_b32 s87, hwreg(HW_REG_IB_STS)\n\ts_and_b32 s87, s87, s66\n\ts_cmp_lg_u32 s87, s65\n\t" CALL0(".Lkv_sig")
#define RUN_OUT "+{s76}"(ni), "+{s77}"(sg), "={s84}"(ovf), "={s85}"(pc), "+{s86}"(st), "+{s75}"(fl), "+{s66}"(cm), "+{s65}"(cw)
#define RUN_IN "{s78}"(xm), "{s79}"(pm), "{s[80:81]}"(base), "{s82}"(L_SLOT + k * 16384), "{s83}"(k), "{v250}"(l16), "{v251}"(xt), "{s98}"(yb)
#define VO(c) c "{v[128:143]}"(vo[0]), c "{v[144:159]}"(vo[1]), c "{v[160:175]}"(vo[2]), c "{v[176:191]}"(vo[3])
#define SIG_OPS(c) : "+{s77}"(sg), "+{s66}"(cm), "+{s65}"(cw), "+{s99}"(vin), VO(c) : "{s76}"(ni), "{s83}"(k), "{s98}"(yb), "{v215}"(ap1) : RUN_CL, "vcc"
#define RUN_CL "s64", "s67", "s87", "s88", "s89", "s90", "s91", "s92", "s93", "s94", "s95", "v248", "v249", "scc", "memory"
      auto run = [&](unsigned xm, unsigned pm) AI { asm volatile(RUN_TXT(".Lkv_run") : RUN_OUT, "={v[252:255]}"(cv4) : RUN_IN : RUN_CL); };
#define PF0(c) c "{v[232:235]}"(pf[0][0]), c "{v[236:239]}"(pf[0][1]), c "{v[240:243]}"(pf[0][2]), c "{v[244:247]}"(pf[0][3]), c "{v[252:255]}"(cv4)
#define PF1(c) c "{v[216:219]}"(pf[1][0]), c "{v[220:223]}"(pf[1][1]), c "{v[224:227]}"(pf[1][2]), c "{v[228:231]}"(pf[1][3]), c "{v[252:255]}"(cv4)
#define RUNP_IN RUN_IN, "{s74}"(L_P + r * 4096), "{s68}"(r ? ANY : 0x7Fu)
      auto runp = [&](int r, unsigned pm, bool chk) AI {
        const unsigned xm = 0;
        if (chk) {
#define CHK_TXT(a, b, c, d) "s_cmp_eq_u32 s76, 64\n\ts_cselect_b32 s89, s79, 0\n\ts_mov_b32 s90, 0xffffff\n\ts_mov_b32 s64, 64\n3:\n\ts_waitcnt lgkmcnt(0)\n\t" \
                "v_readfirstlane_b32 s85, v252\n\tv_readfirstlane_b32 s86, v254\n\tv_readfirstlane_b32 s84, v253\n\tv_readfirstlane_b32 s75, v255\n\t" \
                "s_and_b32 s87, s89, s85\n\ts_getpc_b64 s[92:93]\n\ts_cbranch_scc0 1f\n\t.subsection 11\n\t" MKB "1:\n\t" \
                "s_cmp_eq_u32 s89, 0\n\ts_cbranch_scc1 .Lkv_chk\n\tv_mov_b32 v252, 0\n\tv_add_u32 v249, s74, v250\n\tds_read_b128 v[252:255], v252 offset:" STR(L_CNT_) "\n\t" \
                "ds_read_b128 " a ", v249\n\tds_read_b128 " b ", v249 offset:1024\n\tds_read_b128 " c ", v249 offset:2048\n\tds_read_b128 " d ", v249 offset:3072\n\t" \
                SIG_TXT "\n\ts_sub_u32 s90, s90, 1\n\ts_cselect_b32 s89, 0, s89\n\ts_cselect_b32 s64, -1, s64\n\ts_branch 3b\n\t.subsection 0"
          if (r & 1) asm volatile(CHK_TXT("v[216:219]", "v[220:223]", "v[224:227]", "v[228:231]") : RUN_OUT, PF1("+") : RUNP_IN : RUN_CL);
          else asm volatile(CHK_TXT("v[232:235]", "v[236:239]", "v[240:243]", "v[244:247]") : RUN_OUT, PF0("+") : RUNP_IN : RUN_CL);
        } else {
          if (r & 1) asm volatile(RUN_TXT(".Lkv_runp") : RUN_OUT, PF1("=") : RUNP_IN : RUN_CL);
          else asm volatile(RUN_TXT(".Lkv_runp") : RUN_OUT, PF0("=") : RUNP_IN : RUN_CL);
        }
      };
#define SPEC_TXT "v_mov_b32 v252, 0\n\tds_read_b128 %4, v252 offset:%6\n\tds_read_b128 %0, %5 offset:%7\n\tds_read_b128 %1, %5 offset:%7+1024\n\t" \
                 "ds_read_b128 %2, %5 offset:%7+2048\n\tds_read_b128 %3, %5 offset:%7+3072"
      auto spec = [&](int r) AI {
        if (r & 1) asm volatile(SPEC_TXT : PF1("=") : "v"(l16), "i"(L_CNT), "i"(L_P + r * 4096) : "memory");
        else asm volatile(SPEC_TXT : PF0("=") : "v"(l16), "i"(L_CNT), "i"(L_P + r * 4096) : "memory");
      };
      WARM(s, e, k, 4);
      wait_until(t0, KV_DLY);
      run(0u, ANY);
      st = 0; fl = 0;
      wg_barrier();
      MS();
      run(0xFu, ANY);
      ldv(0);
      cnt_addv(C_ST, 0x10000u);
      run(0xF0u, ANY);
      asm volatile(RUN_TXT(".Lkv_ldv") SIG_OPS("="));
      vin = 0;
      cnt_addv(C_ST, 0x100000u);
      auto sigchk = [&](bool y) AI {
        if (y) asm volatile(SIG_TXT SIG_OPS("+"));
        else asm volatile(SIG_TXT : "+{s77}"(sg), "+{s66}"(cm), "+{s65}"(cw) : "{s76}"(ni), "{s83}"(k), "{s98}"(yb) : RUN_CL);
      };
      auto getp = [&](int r, bool chk) AI {
        runp(r, 0xCu << (8 * r), chk);
        if (r && RARE(ovf & (7u << (8 * r)))) {
          runp(r, 8u << (8 * r), false);
          const float alpha = lds<float>(L_AL + r * 128 + hd * 4);
#pragma unroll
          for (int ct = 0; ct < 4; ++ct)
#pragma unroll
            for (int e = 0; e < 16; ++e) oacc[ct][e] *= alpha;
        }
        if (CVW) __builtin_amdgcn_s_waitcnt(0xC07F);
        MP();
      };
      unsigned st0 = 0;
      auto pv = [&](int r) AI {
#pragma unroll
        for (int n = 0; n < 16; ++n) {
          SB0();
          if (r == 0 && (n & 3) == 0) {
            f32x16 z;
#pragma unroll
            for (int e = 0; e < 16; ++e) z[e] = 0.f;
            oacc[n >> 2] = mfma32(vfr(r, n), pf[r & 1][n & 3], z);
          } else {
            oacc[n >> 2] = mfma32(vfr(r, n), pf[r & 1][n & 3], oacc[n >> 2]);
          }
          PIN(oacc[n >> 2]);
          SB0();
          if (r == 0 && n < 13) ldv1(n);
          if (r == 1 && n < 3) ldv1(13 + n);
          if (r == 1 && n == 4) cnt_addv(C_FL, (st0 & 0xF00u) == 0xF00u ? 0x1000u : 0u);
          if (n == SPAT) spec(r + 1);
          if (r ? (r < 2 ? (n & 3) == 3 : n % 3 == 1) : n == 15) sigchk(r == 2);
          if ((n & 3) == 2) MP();
        }
      };
      MP();
      if (G0T) { run(0u, 0x7Fu); spec(0); }
      getp(0, G0T);
      if (PPRIO) __builtin_amdgcn_s_setprio(PPRIO);
      st0 = st;
      pv(0);
      getp(1, true);
      pv(1);
      if (RARE((st0 & 0xF00u) != 0xF00u)) {
        MP();
        run(0xF00u, ANY);
        ldv(2);
        cnt_addv(C_FL, 0x1000u);
        spec(2);
      }
      MP();
      getp(2, true);
      yb = YB;
      asm volatile("s_cmp_lg_u32 s77, 4\n\t" CALL0(".Lkv_y3") SIG_OPS("="));
      pv(2);
      if (!vin) {
        yb = 0;
        if (RARE(ni != 64)) run(0xF000u, ANY);
        yb = SPIN_CAP * 16u;
        asm volatile("s_getpc_b64 s[92:93]\n\ts_cmp_lg_u32 s99, 0\n\ts_cbranch_scc1 1f\n\ts_cmp_eq_u32 s77, 4\n\ts_cbranch_scc1 .Lkv_y3\n\ts_branch .Lkv_sig\n1:" SIG_OPS("+"));
        spec(3);
      }
      yb = 0;
      {
        const unsigned sb = L_SLOT + k * 16384;
        unsigned sa = sb + hd * 272 + hh * 64;
        unsigned rx = sb + (lane >> 3) * 272 + (lane & 7) * 16;
        unsigned gx = WS_CIN + ((lane >> 3) * 4 + k) * CIN_WI + (kvi ^ (CSW ? (lane >> 3) * 16 : 0) ^ (CS2 ? (lane >> 3) * 2 + (k & 1) : 0)) * CIN_ROW + (lane & 7) * 16;
        MP();
        PIN(sa); PIN(rx); PIN(gx);
        getp(3, true);
        if (RARE(!(fl & 0x4000u))) wait_bits(C_FL, 0x4000u);
        auto dwd = [&](int d) AI { return pk2(oacc[d >> 3][2 * (d & 7)], oacc[d >> 3][2 * (d & 7) + 1]); };
        auto cvt1 = [&](int g) AI { lds<v4u>(sa + (g >> 2) * 128 + (g & 3) * 16) = v4u{san(dwd(4 * g)), dwd(4 * g + 1), dwd(4 * g + 2), dwd(4 * g + 3)}; };
        auto cvt = [&](int ct) AI { cvt1(2 * ct); cvt1(2 * ct + 1); };
        v4u ov[4];
        auto ord = [&](int hf) AI {
#pragma unroll
          for (int t = 0; t < 4; ++t) ov[t] = lds128(rx + t * 2176 + hf * 128);
        };
        auto ost = [&](int hf) AI {
#pragma unroll
          for (int t = 0; t < 4; ++t) __builtin_amdgcn_raw_buffer_store_b128(ov[t], rs, gx + hf * 128, t * 32 * CIN_WI, 16);
        };
#pragma unroll
        for (int n = 0; n < 16; ++n) {
          SB0();
          oacc[n >> 2] = mfma32(vfr(1, n), pf[1][n & 3], oacc[n >> 2]);
          SB0();
          if ((n & 3) == 1) MP();
          if (QST) {
            if (n >= 5 && n < 15 && ((n - 1) & 2) == 0) cvt1(2 * ((n - 5) >> 2) + ((n - 1) & 1));
            if (n == 10) cnt_addv(C_XS, 16u << k);
          } else {
          if (n == 5) cvt(0);
          if (n == 9) { cvt(1); ord(0); }
          if (n == OSAT) { ost(0); if (MLAT == 16) cnt_addv(C_XS, 1u << k); }
          if (n == 13) cvt(2);
          }
        }
        SB0();
        MP();
        if (MLAT == 17) cnt_addv(C_XS, 1u << k);
        cvt(3);
        ord(1);
        SB0();
        ost(1);
        if (MLAT == 18) cnt_addv(C_XS, 1u << k);
      }
    } else {
      const int k = w;
      v2u f0 = {0u, 0u}, f1 = f0, f2 = f0;
      if (k == 0 && lane < C_N) lds<unsigned>(L_CNT + lane * 4) = 0u;
      wg_barrier();
      const unsigned tag = GET_TAG();
      WARM(q, s, k, 4);
      if (QPRIO) __builtin_amdgcn_s_setprio(QPRIO);
      float nr0 = 0.f, nr1 = 0.f;
      v2f lsum = {0.f, 0.f};
      const float OVT = OVTV;
      unsigned nx = 0;
      unsigned aq0 = L_SLOT + (16 * k + i16) * 1024 + ((fswz(i16) ^ g4) * 16);
      unsigned po = L_P + k * 1024 + (i16 + 32 * (g4 >> 1)) * 16 + (g4 & 1) * 8;
      PIN(po); PIN(aq0);
      v4u kf[2][16];
      auto ldk = [&](int r) AI {
#pragma unroll
        for (int u = 0; u < 16; ++u) kf[r & 1][u] = lds128(((aq0 + (r & 1) * 65536) ^ ((u & 3) * 64)) + (u >> 2) * 256);
      };
      v4u sel;
#pragma unroll
      for (int q = 0; q < 4; ++q) sel[q] = ((g4 & 1) == (i16 >> 3) && ((i16 & 7) >> 1) == q) ? ((i16 & 1) ? 0x3F800000u : 0x3F80u) : 0u;
      unsigned qs = WS_QABS + (g4 >> 1) * 262144 + b * 32768 + k * 8192 + (lane & 31) * 16;
      unsigned qd = WS_QABS + b * 32768 + (16 + i16) * 1024 + (60 + g4) * 16;
      unsigned qw[8], qw15;
#pragma unroll
      for (int u = 0; u < 8; ++u) { qw[u] = L_Q + k * 8192 + ((i16 ^ fswz(8 * k + u)) * 16) + (g4 >> 1) * 256 + (g4 & 1) * 8; PIN(qw[u]); }
      qw15 = qw[7] - (k == 3 && g4 >= 2 && i16 >= 12 ? 4096u : 0u);
      unsigned qr = L_Q + i16 * 1024 + ((fswz(i16) ^ g4) * 16);
      PIN(sel); PIN(qs); PIN(qd); PIN(qw15); PIN(qr);
      unsigned mxw = L_Q + (16 + (hd >> 2)) * 1024 + 768 + ((3 ^ ((hd >> 2) & 3)) * 64) + (hd & 3) * 16 + k * 4;
      unsigned mxr = L_Q + (16 + (i16 >> 2)) * 1024 + 768 + ((3 ^ (i16 >> 2)) * 64) + (i16 & 3) * 16;
      PIN(mxw); PIN(mxr);
      wait_bits(C_ST, 1u << k);
      ldk(0);
      cnt_addv(C_ST, 1u << (24 + k));
      MQ();
      if (K1AT == 0) {
        wait_bits(C_ST, 16u << k);
        ldk(1);
        cnt_addv(C_ST, 1u << (28 + k));
        MQ();
      }
      v4u qf[32], qv[16], da, db;
      const unsigned char* wsp = p.ws;
      const unsigned char* wsp2 = p.ws + 4096;
#define QLD(t) asm volatile("global_load_dwordx4 %0, %1, %2 offset:%3 sc1" : "=v"(qv[t]) : "v"(qs), "s"((t) < 8 ? wsp : wsp2), "i"(((t) & 7) * 512) : "memory")
      if (k == 0) {
        wait_until(t0, POLL_START);
        const unsigned fo = WS_FLAG + kvi * 1024 + fpos<FLW>(lane);
        const v2u zz = {0u, 0u};
        u64 seen = 0;
        bool sent = false;
        auto chk = [&](v2u& f) AI {
          asm volatile("s_waitcnt vmcnt(2)" : "+v"(f)::"memory");
          const u64 ok = __ballot(((f[0] ^ tag) | (f[1] ^ tag)) == 0) | seen;
          if (ok != seen) {
            seen = ok;
            lds<v2u>(L_CNT + C_MASK * 4) = v2u{(unsigned)ok, (unsigned)(ok >> 32)};
            if (PAUSE && !sent && __popcll(~ok) <= PK) { cnt_addv(C_FL, 0x100u); sent = true; }
          }
          return ok == ~0ull;
        };
        MQ();
        PLD(f0); __builtin_amdgcn_s_sleep(POLL_GAP);
        PLD(f1); __builtin_amdgcn_s_sleep(POLL_GAP);
        PLD(f2);
#pragma unroll 1
        for (unsigned spin = 0; spin < SPIN_CAP; ++spin) {
          if (chk(f0)) break;
          if (QPM) PLDM(f0); else PLD(f0);
          MQ();
          if (chk(f1)) break;
          if (QPM) PLDM(f1); else PLD(f1);
          MQ();
          if (chk(f2)) break;
          if (QPM) PLDM(f2); else PLD(f2);
        }
        MQ();
#pragma unroll
        for (int t = 0; t < 16; ++t) QLD(t);
      } else {
        unsigned pend = 0xFFFFu;
#pragma unroll 1
        for (unsigned spin = 0; spin < SPIN_CAP * 16u; ++spin) {
          v2u mk;
          asm volatile("ds_read_b64 %0, %1 offset:%2\n\ts_waitcnt lgkmcnt(0)" : "=v"(mk) : "v"(C_MASK * 4u), "i"(L_CNT) : "memory");
          const unsigned m0 = __builtin_amdgcn_readfirstlane(mk[0]), m1 = __builtin_amdgcn_readfirstlane(mk[1]);
          const unsigned nw = ((k < 2 ? m0 : m1) >> (16 * (k & 1))) & pend;
          if (PROG ? nw != 0u : (m0 & m1) == ~0u) {
            pend &= ~nw;
#pragma unroll
            for (int t = 0; t < 16; ++t)
              if (nw >> t & 1u) { QLD(t); if ((t & 7) == 0) MQ(); }
          }
          if ((m0 & m1) == ~0u) break;
          __builtin_amdgcn_s_sleep(1);
        }
      }
#define QLD2() asm volatile("global_load_dwordx4 %0, %2, %3 sc1\n\tglobal_load_dwordx4 %1, %2, %4 sc1" : "=&v"(da), "=&v"(db) : "v"(qd), "s"(wsp), "s"(wsp + 262144) : "memory")
      QLD2();
      cnt_add(C_FL);
      MQ();
      if (K1AT == 1) {
        wait_bits(C_ST, 16u << k);
        ldk(1);
        cnt_addv(C_ST, 1u << (28 + k));
      }
#pragma unroll 1
      for (unsigned spin = 0; spin < SPIN_CAP; ++spin) {
        if (spin) {
#pragma unroll
          for (int t = 0; t < 16; ++t) QLD(t);
          QLD2();
        }
        MQ();
        asm volatile("s_waitcnt vmcnt(2)" : W8(qv, 0), W8(qv, 8), "+v"(f0), "+v"(f1), "+v"(f2)::"memory");
        float chk = 0.f;
        f32x4 d[16];
#pragma unroll
        for (int t = 0; t < 18; ++t) {
          if (t < 16) d[t] = mfma16(sel, qv[t], f32x4{0.f, 0.f, 0.f, 0.f});
          if (t >= 2) {
            const int u = t - 2;
            lds<v2u>((u == 15 ? qw15 : qw[u >> 1]) + (u >> 1) * 1024 + (u & 1) * 512) = v2u{pk2(d[u][0], d[u][1]), pk2(d[u][2], d[u][3])};
            chk += d[u][0];
          }
          SB0();
          if ((t & 3) == 1) MQ();
        }
        asm volatile("s_waitcnt vmcnt(0)" : "+v"(da), "+v"(db)::"memory");
        if (!__any(chk != chk || max(da[0], db[0]) == POISON)) break;
        unsigned mx = max(da[0], db[0]);
#pragma unroll
        for (int t = 0; t < 16; ++t) mx = max(mx, qv[t][0]);
        if (!__any(mx == POISON)) break;
        CFENCE();
      }
      MQ();
      cnt_addv(C_FL, 16u);
#pragma unroll
      for (int e = 0; e < 4; ++e) qf[31][e] = pk2(bf2f(da[e]) + bf2f(db[e]), bfhi(da[e]) + bfhi(db[e]));
      PIN(qf[31]);
      MQ();
      wait_bits(C_FL, 0x40u);
      MQ();
      f32x4 s0 = {0.f, 0.f, 0.f, 0.f}, s1 = s0, a0 = s0, a1 = s0, t0, t1;
      auto ldq = [&](int f) AI { qf[(f >> 1) + 16 * (f & 1)] = lds128((qr ^ (((f >> 1) & 3) * 64)) + (f >> 3) * 256 + (f & 1) * 16384); };
      v4u kn[2][16], cs;
      auto ldn = [&](int r, int u) AI { kn[r & 1][u] = lds128(((aq0 + (r & 1) * 65536) ^ ((u & 3) * 64)) + (u >> 2) * 256); };
#define CRD() asm volatile("ds_read_b128 %0, %1 offset:%2" : "=v"(cs) : "v"(0u), "i"(L_CNT) : "memory")
#define CWT() asm volatile("s_waitcnt lgkmcnt(0)" : "+v"(cs)::"memory")
#pragma unroll
      for (int f = 0; f < 12; ++f) ldq(f);
#pragma unroll
      for (int u = 0; u < 16; ++u) {
        SB0();
        s0 = mfma16(kf[0][u], qf[u], s0);
        s1 = mfma16(kf[0][u], qf[16 + u], s1);
        if (MIX && (u & 1)) a1 = mfma16(kf[1][u >> 1], qf[16 + (u >> 1)], a1);
        else if (MIX) a0 = mfma16(kf[1][u >> 1], qf[u >> 1], a0);
        SB0();
        if ((u & 3) == 0) MQ();
        if (12 + 2 * u < 31) ldq(12 + 2 * u);
        if (13 + 2 * u < 31) ldq(13 + 2 * u);
      }
      MQ();
      unsigned cx;
      f32x4 ma, mb;
      auto max4 = [&](const f32x4& v) AI { return fmaxf(fmaxf(v[0], v[1]), fmaxf(v[2], v[3])); };
      auto post_max = [&](bool first) AI {
        const float mw = max32(rs_max16(max4(s0), max4(s1)));
        lds<float>(mxw) = mw;
        if (first) cnt_addv(C_P, 0x10u);
        else { cnt_add(C_MX); nx += 4; }
      };
#define XRD(c, w) asm volatile("ds_read_b32 %0, %3 offset:%5\n\tds_read_b128 %1, %4\n\tds_read_b128 %2, %4 offset:4096" w : "=&v"(cx), "=&v"(ma), "=&v"(mb) : "v"(0u), "v"(mxr), "i"(L_CNT + (c) * 4) : "memory")
      auto exchange = [&](int r) AI {
        asm volatile("s_waitcnt lgkmcnt(0)" : "+v"(cx), "+v"(ma), "+v"(mb)::"memory");
        if (RARE(r ? __builtin_amdgcn_readfirstlane(cx) < nx : !(__builtin_amdgcn_readfirstlane(cx) & 0x40u))) {
#pragma unroll 1
          for (unsigned sp = 0; sp < SPIN_CAP * 16u; ++sp) {
            if (r == 0) MQ();
            XRD(r ? C_MX : C_P, "\n\ts_waitcnt lgkmcnt(0)");
            if (r ? __builtin_amdgcn_readfirstlane(cx) >= nx : (__builtin_amdgcn_readfirstlane(cx) & 0x40u) != 0) break;
          }
        }
        float d0 = max4(ma), d1 = max4(mb);
        if (r) {
          d0 = fmaxf(d0, 0.f); d1 = fmaxf(d1, 0.f);
          const float al0 = ex2(-d0), al1 = ex2(-d1);
          lsum[0] *= al0; lsum[1] *= al1;
          lds<float>(L_AL + r * 128 + hd * 4) = (lane & 16) ? al1 : al0;
        }
#pragma unroll
        for (int e = 0; e < 4; ++e) { s0[e] -= d0; s1[e] -= d1; }
        nr0 -= d0; nr1 -= d1;
      };
      v2f ln;
      auto do_p = [&](int r, const f32x4& x0, const f32x4& x1, unsigned ov) AI {
        float e0[4], e1[4];
#pragma unroll
        for (int e = 0; e < 4; ++e) { e0[e] = ex2(x0[e]); e1[e] = ex2(x1[e]); }
        lds<v2u>(po + r * 4096) = v2u{pk2(e0[0], e0[1]), pk2(e0[2], e0[3])};
        lds<v2u>(po + r * 4096 + 256) = v2u{pk2(e1[0], e1[1]), pk2(e1[2], e1[3])};
        cnt_add2(C_P, 1u << (8 * r), ov);
        ln = lsum + ((v2f{e0[0], e1[0]} + v2f{e0[1], e1[1]}) + (v2f{e0[2], e1[2]} + v2f{e0[3], e1[3]}));
      };
      auto redo = [&](int r) AI {
        post_max(false);
        XRD(C_MX, "");
        exchange(r);
        do_p(r, s0, s1, 0u);
      };
      auto fin = [&](int r, const f32x4& x0, const f32x4& x1) AI {
        MQ();
#pragma unroll
        for (int e = 0; e < 4; ++e) { s0[e] = x0[e] + nr0; s1[e] = x1[e] + nr1; }
        const unsigned ov = __any(fmaxf(max4(s0), max4(s1)) > OVT) ? 1u << (8 * r) : 0u;
        do_p(r, s0, s1, ov);
      };
#pragma unroll
      for (int u = 8 * MIX; u < 16; ++u) {
        SB0();
        a0 = mfma16(kf[1][u], qf[u], a0);
        a1 = mfma16(kf[1][u], qf[16 + u], a1);
        PIN(a0); PIN(a1);
        SB0();
        if (u <= 8 * MIX + XAT + 4 || u >= 13) MQ();
        if (u == 8 * MIX) post_max(true);
        if (u == 8 * MIX + XAT) XRD(C_P, "");
        if (u == 8 * MIX + 2 + XAT) exchange(0);
        if (u == 8 * MIX + 3 + XAT) {
          do_p(0, s0, s1, 0u);
          lsum = ln;
        }
        if (KSP == 0 && u == 13) {
          CRD();
#pragma unroll
          for (int v = 0; v < 16; ++v) ldn(2, v);
        }
        if (KSP == 1) { if (u == 0) CRD(); ldn(2, u); }
        if (KSP == 2 && u >= XAT + 3) {
          if (u == XAT + 3) CRD();
#pragma unroll
          for (int v = 16 * (u - XAT - 3) / (13 - XAT); v < 16 * (u - XAT - 2) / (13 - XAT); ++v) ldn(2, v);
        }
      }
      fin(1, a0, a1);
      asm volatile("s_waitcnt lgkmcnt(14)" : "+v"(cs)::"memory");
      auto refchk = [&](int r) AI {
        CWT();
        unsigned ov = __builtin_amdgcn_readfirstlane(cs[1]);
        if (RARE(!(__builtin_amdgcn_readfirstlane(cs[0]) >> (8 * r) & 12u))) { MQ(); ov = wait_p(12u << (8 * r), 0u); }
        if (RARE(ov & (7u << (8 * r)))) redo(r);
        lsum = ln;
        MQ();
      };
      MQ();
      if (RARE(!(__builtin_amdgcn_readfirstlane(cs[2]) >> (8 + k) & 1u))) {
        MQ();
        wait_bits(C_ST, 0x100u << k);
#pragma unroll
        for (int u = 0; u < 16; ++u) ldn(2, u);
        __builtin_amdgcn_s_waitcnt(0xC07F);
      }
      MQ();
      t0 = f32x4{0.f, 0.f, 0.f, 0.f}; t1 = t0;
      unsigned ak[4];
#pragma unroll
      for (int u = 0; u < 16; ++u) {
        SB0();
        t0 = mfma16(kn[0][u], qf[u], t0);
        t1 = mfma16(kn[0][u], qf[16 + u], t1);
        PIN(t0); PIN(t1);
        SB0();
        if ((u & 7) == 0) MQ();
        if (u < 4) { ak[u] = (aq0 + 65536) ^ (u * 64); PIN(ak[u]); }
        if (u == CKAT - 4) CRD();
        if (u == CKAT) refchk(1);
      }
      fin(2, t0, t1);
      CRD();
      t0 = f32x4{nr0, nr0, nr0, nr0}; t1 = f32x4{nr1, nr1, nr1, nr1};
      PIN(t0); PIN(t1); PIN(ln);
      MQ();
      unsigned bad;
      {
        unsigned cap = SPIN_CAP * 16u, tv;
        asm volatile("s_branch 2f\n1:\n\tds_read_b128 v[200:203], %3 offset:%4\n\ts_sub_u32 %2, %2, 1\n\ts_cselect_b64 exec, 0, exec\n2:\n\ts_waitcnt lgkmcnt(0)\n\t"
                     "v_and_b32 %1, %5, v202\n\tv_cmp_eq_u32 vcc, 0, %1\n\ts_cbranch_vccnz 1b\n\ts_mov_b64 exec, -1"
                     : "+{v[200:203]}"(cs), "=&v"(tv), "+s"(cap) : "v"(0u), "i"(L_CNT), "s"(0x1000u << k) : "memory", "vcc", "scc");
      }
#pragma unroll
      for (int u = 0; u < 16; ++u) {
        SB0();
        kn[1][u] = lds128(ak[u & 3] + (u >> 2) * 256);
        if (u == 7) MQ();
        if (u == 9) {
          asm volatile("" : "+v"(cs)::"memory");
          bad = __builtin_amdgcn_readfirstlane((cs[1] & 0x70000u) | ((cs[0] & 0xC0000u) == 0u ? 1u : 0u));
          asm volatile("" : "+s"(bad));
        }
      }
      SB0();
      if (RARE(bad)) {
        MQ();
        if (RARE(wait_p(0xC0000u, 0u) & 0x70000u)) {
          redo(2);
          t0 = f32x4{nr0, nr0, nr0, nr0}; t1 = f32x4{nr1, nr1, nr1, nr1};
        }
      }
      lsum = ln;
#pragma unroll
      for (int n = 0; n < 16 + QL; ++n) {
        SB0();
        if (n < 16) t0 = mfma16(kn[1][n], qf[n], t0);
        if (n >= QL) t1 = mfma16(kn[1][n - QL], qf[16 + n - QL], t1);
        SB0();
        if ((n & 7) == 0) MQ();
      }
      s0 = t0; s1 = t1;
      do_p(3, s0, s1, __any(fmaxf(max4(s0), max4(s1)) > OVT) ? 1u << 24 : 0u);
      MQ();
      if (RARE(wait_p(0xC000000u, 0u) & 0x7000000u)) redo(3);
      lsum = ln;
      {
        MQ();
        const float lt = add32(rs_add16(lsum[0], lsum[1]));
        lds<float>(L_LS + k * 128 + hd * 4) = lt;
        cnt_add(C_MX);
        nx += 4;
        cnt_wait(C_MX, nx);
        const LAS float* ls = (const LAS float*)(size_t)(L_LS + hd * 4);
        v4u mlv = {__float_as_uint((lane & 16) ? -nr1 : -nr0), tag, __float_as_uint((ls[0] + ls[32]) + (ls[64] + ls[96])), tag};
        unsigned gm = WS_ML + ((hd * 4 + k) * 128 + (kvi ^ (MS2 ? (hd & 7) * 16 : 0))) * 16;
        PIN(mlv); PIN(gm);
        MQ();
        if (QST) {
          unsigned rx = L_SLOT + k * 16384 + (lane >> 3) * 272 + (lane & 7) * 16;
          unsigned gx = WS_CIN + ((lane >> 3) * 4 + k) * CIN_WI + (kvi ^ (CSW ? (lane >> 3) * 16 : 0)) * CIN_ROW + (lane & 7) * 16;
          PIN(rx); PIN(gx);
          MQ();
          wait_bits(C_XS, 16u << k);
          v4u ov[4];
#pragma unroll
          for (int t = 0; t < 4; ++t) ov[t] = lds128(rx + t * 2176);
#pragma unroll
          for (int t = 0; t < 4; ++t) __builtin_amdgcn_raw_buffer_store_b128(ov[t], rs, gx, t * 32 * CIN_WI, 16);
          MQ();
        }
        wait_bits(C_XS, 1u << k);
        if (lane < 32) st_sc1(rs, gm, mlv);
      }
    }
  } else {
    const int h = 4 * xcd + (idx & 3), j = idx >> 2, wi = 4 * h + j;
    const bool ffn = wi < 86;
    const unsigned hh = lane >> 5;
    v4u xa, xb;
    asm volatile("s_cmp_gt_u32 %3, 3\n\ts_cbranch_scc1 1f\n\tglobal_load_dwordx4 %0, %2, %4\n\tglobal_load_dwordx4 %1, %2, %4 offset:512\n1:"
                 : "=&v"(xa), "=&v"(xb) : "v"((2 * w + hh) * 1024 + (lane & 31) * 16), "s"(w), "s"(p.x) : "memory", "scc");
    constexpr int NT = FR4 ? 2 : 4;
    const int hf = FMAP ? (idx & 7) + 8 * (xcd & 3) : h, jf = FMAP ? (idx >> 3) + 2 * (xcd >> 2) : j;
    const int fr = FR4 ? 1 : __builtin_amdgcn_readfirstlane((jf >> 1) ^ 1);
    const int dj = jf & 1, c0 = (FR4 ? (jf >> 1) * 256 : 0) + w * 16 * NT;
    unsigned qb = WS_QABS + dj * 262144 + (i16 * 32 + hf) * 1024 + (c0 + 8 * g4) * 2;
    auto ffn_pf = [&](unsigned& d) {
      const int u = idx * 8 + w, q5 = lane / 5, r5 = lane - 5 * q5;
      const char* pb = (const char*)((q5 & 1) ? p.wu : p.wg) + (4 * u + (min(q5, 7) >> 1)) * (FF * 2) + min(xcd * 512 + min(r5 * 128, 508), FF * 2 - 4);
      asm volatile("global_load_dword %0, %1, off" : "=&v"(d) : "v"(pb) : "memory");
    };
    unsigned dpf = 0;
    SB0();
    if (WPF) {
      unsigned d0, d1;
      if (WPF == 1) {
        const unsigned po = (32 * idx + 4 * w + g4) * 8192 + xcd * 1024 + i16 * 64;
        asm volatile("global_load_dword %0, %4, %5\n\tglobal_load_dword %1, %4, %6\n\ts_waitcnt vmcnt(0)" : "=&v"(d0), "=&v"(d1), "+v"(xa), "+v"(xb) : "v"(po), "s"(p.wq), "s"(p.wuk) : "memory");
      } else {
        const char* pa = (const char*)(hh ? p.wuk : p.wq) + (32 * idx + 4 * w + ((lane >> 3) & 3)) * 8192 + xcd * 1024 + (lane & 7) * 128;
        asm volatile("global_load_dword %0, %1, off" : "=&v"(d0) : "v"(pa) : "memory");
        if (PZT) asm volatile("global_load_dword %0, %1, %2" : "=&v"(dpf) : "v"(WS_CIN + wi * CIN_WI + w * 4096), "s"(p.ws) : "memory");
        if (PZE == 1) {
#pragma unroll
          for (int t = 0; t < 4; ++t) st_sc1(rs, WS_CIN + wi * CIN_WI + w * 4096 + t * 1024 + lane * 16, v4u{POISON, POISON, POISON, POISON});
          CFENCE();
        }
        if (w >= 4) {
          wait_until(t0, WM_DLY);
          WARM(w, q, w - 4, 4);
          if (PZE == 3) VMWAIT(0);
        } else if (PZE == 3) {
          wait_until(t0, PZ_AT);
#pragma unroll
          for (int t = 0; t < 8; ++t) st_sc1(rs, WS_CIN + wi * CIN_WI + w * 8192 + t * 1024 + lane * 16, v4u{POISON, POISON, POISON, POISON});
          CFENCE();
        }
        asm volatile("s_waitcnt vmcnt(%4)" : "+v"(d0), "+v"(xa), "+v"(xb), "+v"(dpf) : "i"(PZE == 1 ? 4 : PZE == 3 ? 8 : 0) : "memory");
      }
      if (WPF_DLY) __builtin_amdgcn_s_sleep(WPF_DLY);
    }
    __builtin_amdgcn_s_barrier();
    MW();
    v4u wukf[8];
    if (fr) {
      const char* src = (const char*)p.wq + hf * 256 + dj * 128 + (size_t)(64 * w) * 8192;
#pragma unroll
      for (int t = 0; t < 8; ++t) {
        glds16s((lane >> 3) * 8192 + (((lane & 7) ^ (((g4 & 1) | ((t & 1) << 1)) << 1)) * 16), src + t * 65536, L_WQ + (64 * w + 8 * t) * 128);
        if (t == 3) MW();
      }
      MW();
    }
    {
      const unsigned vk = (8 * (i16 >> 2) + (lane & 3)) * 8192 + g4 * 16;
      const char* kb = (const char*)p.wuk + hf * 256 + dj * 128 + (size_t)c0 * 8192;
#pragma unroll
      for (int u = 0; u < NT / 2; ++u)
        asm volatile("s_cmp_eq_u32 %5, 0\n\ts_cbranch_scc1 1f\n\t"
                     "global_load_dwordx4 %0, %4, %6\n\tglobal_load_dwordx4 %1, %4, %6 offset:64\n\t"
                     "global_load_dwordx4 %2, %4, %7\n\tglobal_load_dwordx4 %3, %4, %7 offset:64\n1:"
                     : "=&v"(wukf[4 * u]), "=&v"(wukf[4 * u + 1]), "=&v"(wukf[4 * u + 2]), "=&v"(wukf[4 * u + 3])
                     : "v"(vk), "s"(fr), "s"(kb + 32 * u * 8192), "s"(kb + (32 * u + 4) * 8192) : "memory", "scc");
    }
    {
      MW();
      const v4u pz = {POISON, POISON, POISON, POISON};
      if (fr && i16 < 8) {
#pragma unroll
        for (int u = 0; u < NT / 2; ++u) st_sc1(rs, qb + u * 64, pz);
      }
      if (PZE == 2) {
        if (w >= 4) {
          if (PZ_DLY) __builtin_amdgcn_s_sleep(PZ_DLY);
          MW();
#pragma unroll
          for (int t = 0; t < 2; ++t) st_sc1(rs, WS_EIN + wi * 8192 + (w - 4) * 2048 + t * 1024 + lane * 16, pz);
#pragma unroll
          for (int t = 0; t < 8; ++t) st_sc1(rs, WS_CIN + wi * CIN_WI + (w - 4) * 8192 + t * 1024 + lane * 16, pz);
          VMWAIT(2 * NT + NT / 2 + 10);
        }
      } else {
      st_sc1(rs, WS_EIN + wi * 8192 + w * 1024 + lane * 16, pz);
      if (!PZE) {
#pragma unroll
        for (int t = 0; t < 4; ++t) st_sc1(rs, WS_CIN + wi * CIN_WI + w * 4096 + t * 1024 + lane * 16, pz);
      }
      }
    }
    if (w < 4) {
      asm volatile("s_waitcnt vmcnt(%3)\n\ts_cmp_lg_u32 %2, 0\n\ts_cbranch_scc1 1f\n\ts_waitcnt vmcnt(1)\n1:" : "+v"(xa), "+v"(xb) : "s"(fr), "i"(8 + 2 * NT + NT / 2 + 1 + 8) : "memory", "scc");
      MW();
      v2f v[8];
#pragma unroll
      for (int e = 0; e < 4; ++e) { v[e] = v2f{bf2f(xa[e]), bfhi(xa[e])}; v[4 + e] = v2f{bf2f(xb[e]), bfhi(xb[e])}; }
      const unsigned p0 = __builtin_amdgcn_readlane(__float_as_uint(v[0][0]), 0), p1 = __builtin_amdgcn_readlane(__float_as_uint(v[0][0]), 32);
      const float piv = __uint_as_float(hh ? p1 : p0);
      v2f sm = {0.f, 0.f}, ss = {0.f, 0.f};
#pragma unroll
      for (int e = 0; e < 8; ++e) {
        if (e == 3) { SB0(); MW(); }
        const v2f d = v[e] - v2f{piv, piv}; sm += d; ss += d * d;
      }
      SB0();
      MW();
      float t = rowsum(rs_add16(sm[0] + sm[1], ss[0] + ss[1]));
      auto sw2 = __builtin_amdgcn_permlane16_swap(__float_as_uint(t), __float_as_uint(t), false, false);
      const float md = __uint_as_float(sw2[0]) * (1.f / DM);
      const float var = fmaxf(fmaf(-md, md, __uint_as_float(sw2[1]) * (1.f / DM)), 0.f);
      const float mb = rbf(piv + md), vb = rbf(var);
      const float rden = __builtin_amdgcn_rcpf(rbf(__builtin_amdgcn_sqrtf(rbf(vb + 1e-5f))));
      SB0();
      MW();
      v4u o4[2];
#pragma unroll
      for (int e = 0; e < 8; ++e) {
        if (e == 3 || e == 6) { SB0(); MW(); }
        const v2f d = v[e] - v2f{mb, mb};
        const unsigned u = pk2(d[0], d[1]);
        const v2f o = v2f{bf2f(u), bfhi(u)} * v2f{rden, rden};
        o4[e >> 2][e & 3] = pk2(o[0], o[1]);
      }
      const unsigned xo = L_XN + (2 * w + hh) * 1040 + (lane & 31) * 16;
      lds<v4u>(xo) = o4[0];
      lds<v4u>(xo + 512) = o4[1];
      if (tid < C_N) lds<unsigned>(L_CNT + tid * 4) = 0u;
      if (PZE == 2) VMWAIT(2 * NT + NT / 2);
    }
    MW();
    const unsigned tag = GET_TAG();
    unsigned qa = L_QH + (lane & 7) * 144 + g4 * 16;
    {
      unsigned ta = L_WQ + (8 * g4 + (i16 >> 2)) * 128 + (((2 * w + ((lane & 3) >> 1)) ^ (((i16 >> 3) | ((g4 & 1) << 1)) << 1)) * 16) + 8 * (lane & 1);
      unsigned xq = L_XN + (lane & 7) * 1040 + g4 * 16;
      unsigned qw = L_QH + (lane & 7) * 144 + (16 * w + 4 * g4) * 2;
      PIN(ta); PIN(xq); PIN(qw); PIN(qa); PIN(qb);
      if (PZE != 2) VMWAIT(2 * NT + NT / 2 + 1 + (PZE ? 0 : 4));
      wg_barrier();
      MW();
      if (fr && w < 4) {
        f32x4 c0a = {0.f, 0.f, 0.f, 0.f}, c1a = c0a;
#pragma unroll
        for (int ks = 0; ks < 16; ks += 2) {
          if (ks == 4 || ks == 10) MW();
          const v4u x0 = lds128(xq + ks * 64), x1 = lds128(xq + ks * 64 + 64);
          const v2u a0 = trread(ta + ks * 4096), a1 = trread(ta + ks * 4096 + 512), b0 = trread(ta + ks * 4096 + 4096), b1 = trread(ta + ks * 4096 + 4608);
          c0a = mfma16(v4u{a0[0], a0[1], a1[0], a1[1]}, x0, c0a);
          c1a = mfma16(v4u{b0[0], b0[1], b1[0], b1[1]}, x1, c1a);
        }
        MW();
        c0a += c1a;
        lds<v2u>(qw) = v2u{pk2(c0a[0], c0a[1]), pk2(c0a[2], c0a[3])};
      }
    }
    MW();
    wg_barrier();
    {
      const v4u q0 = lds128(qa), q1 = lds128(qa + 64);
      if (NT == 4) asm volatile("s_waitcnt vmcnt(0)" : W8(wukf, 0)::"memory");
      else asm volatile("s_waitcnt vmcnt(0)" : "+v"(wukf[0]), "+v"(wukf[1]), "+v"(wukf[2]), "+v"(wukf[3])::"memory");
      const bool last = cnt_add_rtn(C_QST) == 7u;
      if (FLAG_FIRST && last) send_flags(rs, wi, lane, tag);
      if (fr) {
        const float sc = 0.08838834764831845f * LOG2E;
#pragma unroll
        for (int u = 0; u < NT / 2; ++u) {
          f32x4 d0 = mfma16(wukf[4 * u], q0, f32x4{0.f, 0.f, 0.f, 0.f}), d1 = mfma16(wukf[4 * u + 2], q0, f32x4{0.f, 0.f, 0.f, 0.f});
          d0 = mfma16(wukf[4 * u + 1], q1, d0);
          d1 = mfma16(wukf[4 * u + 3], q1, d1);
          d0 *= sc; d1 *= sc;
          if (i16 < 8) st_sc1(rs, qb + u * 64, v4u{san(pk2(d0[0], d0[1])), pk2(d0[2], d0[3]), pk2(d1[0], d1[1]), pk2(d1[2], d1[3])});
        }
      }
      MW();
      if (!FLAG_FIRST && last) send_flags(rs, wi, lane, tag);
      if (last) cnt_add(C_SENT);
      if (KPF) {
        if (KPF_AT) wait_until(t0, KPF_AT + (w ^ 4) * KPF_ST);
        if (KPF_S) cnt_wait<1>(C_SENT, 1);
        if (KPF == 1) {
          const unsigned ko = (unsigned)(xcd * 16 + idx) * 262144u + 196608u + w * 8192 + lane * 128;
          asm volatile("global_load_dword %0, %1, %2" : "=&v"(dpf) : "v"(ko), "s"(p.ckv) : "memory");
        } else {
          const unsigned ko = min((unsigned)lane * 131072u + (w & 1) * 65536u + idx * 4096u, WS_END - 4u);
          asm volatile("s_cmp_gt_u32 %3, %4\n\ts_cbranch_scc1 1f\n\tglobal_load_dword %0, %1, %2 sc1\n1:" : "=&v"(dpf) : "v"(ko), "s"(p.ws), "s"(w), "i"(KPF - 3) : "memory", "scc");
        }
      }
      if (FPF == 3 && xcd < 6) {
        cnt_wait<1>(C_SENT, 1);
        ffn_pf(dpf);
        asm volatile("s_waitcnt vmcnt(0)" : "+v"(dpf)::"memory");
      }
    }
    if (w == 4 && (T_O == 0 || T_F == 0 || T_UV == 1)) {
      v2u f0;
      MW();
      wait_until(t0, POLL_START);
      poll_flags(p.ws, WS_FLAG + (128 + wi) * 1024 + fpos<FLW>(lane), tag, f0);
      MW();
      if (GO_DLY) wait_until(now32(), GO_DLY);
      cnt_add(C_GO);
      asm volatile("s_waitcnt vmcnt(0)" : "+v"(f0)::"memory");
    }
    MW();
    v4u wof[4][8];
    f32x4 oc[8];
    if (w >= 4) {
      const int hk = w - 4;
      if (T_O) { wait_until(t0, T_O); cnt_wait<1>(C_SENT, 1); } else cnt_wait<2>(C_GO, 1);
      v4u wor[4][8], wdr[8], wdf[8];
      {
        const unsigned vo = 8 * g4 * 1024 + i16 * 16, vd = 8 * (g4 & 1) * 1024 + i16 * 16;
        const char* so = (const char*)p.wo + (size_t)(h * 128) * 1024 + hk * 256;
        const char* sd = (const char*)p.wd + (size_t)(ffn ? 16 * wi : 0) * 1024 + hk * 256;
#pragma unroll
        for (int n = 0; n < 40; ++n) {
          if (n < 8) asm volatile("global_load_dwordx4 %0, %1, %2 nt" : "=v"(wdr[n]) : "v"(vd), "s"(sd + n * 1024) : "memory");
          else asm volatile("global_load_dwordx4 %0, %1, %2" : "=v"(wor[(n - 8) >> 3][n & 7]) : "v"(vo), "s"(so + (32 * ((((n - 8) >> 3) + j) & 3) + (n & 7)) * 1024) : "memory");
          if (n >= NFW - 1 && n < 39) VMWAIT(NFW - 1);
          if ((n & 7) == 0) MW();
        }
      }
      asm volatile("s_waitcnt vmcnt(32)" : W8(wdr, 0), "+v"(dpf)::"memory");
      transpose8(wdr, wdf);
      asm volatile("s_waitcnt vmcnt(24)" : W8(wor[0], 0)::"memory");
      transpose8(wor[0], wof[0]);
      asm volatile("s_waitcnt vmcnt(16)" : W8(wor[1], 0)::"memory");
      transpose8(wor[1], wof[1]);
      asm volatile("s_waitcnt vmcnt(8)" : W8(wor[2], 0)::"memory");
      transpose8(wor[2], wof[2]);
      asm volatile("s_waitcnt vmcnt(0)" : W8(wor[3], 0)::"memory");
      transpose8(wor[3], wof[3]);
#pragma unroll
      for (int jj = 0; jj < 8; ++jj) oc[jj] = f32x4{0.f, 0.f, 0.f, 0.f};
      if (ffn) {
        MW();
        cnt_wait<1>(C_FFN, 1);
        const v4u a = lds128(L_HF + ((lane & 7) ^ ((lane & 8) >> 2)) * 80 + g4 * 16);
#pragma unroll
        for (int jj = 0; jj < 8; ++jj) oc[jj] = mfma16(a, wdf[jj], oc[jj]);
      }
#pragma unroll
      for (int u = 0; u < 4; ++u) asm volatile("" : W8(wof[u], 0));
      asm volatile("" : W8(oc, 0));
    } else {
      MW();
      if (T_F) { wait_until(t0, T_F); cnt_wait<1>(C_SENT, 1); } else cnt_wait<2>(C_GO, 1);
      if (h < 22) {
        const char* src = (const char*)(((i16 >> 3) ^ (g4 >> 1)) ? p.wu : p.wg) + (size_t)(128 * j + 32 * w + g4) * (FF * 2) + h * 128;
#pragma unroll
        for (int t = 0; t < 8; ++t) {
          glds16<0>(src + (size_t)t * 4 * (FF * 2) + min((i16 ^ fswz(4 * t + g4)) & 7u, h == 21 ? 3u : 7u) * 16, L_GU + (32 * w + 4 * t) * 256);
          if (t & 1) MW();
        }
      }
      auto ld_uv = [&]() {
#pragma unroll
        for (int t = 0; t < 8; ++t) {
          const unsigned row = 32 * w + 4 * t + g4;
          if ((t & 1) == 0) MW();
          glds16<1>((const char*)p.wuv + (size_t)(128 * j + row) * 8192 + h * 256 + ((i16 ^ fswz(row)) * 16), L_WUV + (32 * w + 4 * t) * 256);
        }
      };
      if (!T_UV) ld_uv();
      if (h < 22) {
        if (T_UV) VMWAIT(0); else VMWAIT(8);
        MW();
        cnt_add(C_GU);
        cnt_wait(C_GU, 4);
        f32x4 ag = {0.f, 0.f, 0.f, 0.f}, au = {0.f, 0.f, 0.f, 0.f};
        const unsigned ra = 8 * g4 + (i16 >> 2), ch = 2 * w + ((lane & 3) >> 1);
        const unsigned r0 = L_GU + ra * 256 + ((ch ^ fswz(ra)) * 16) + 8 * (lane & 1), r1 = L_GU + (ra + 4) * 256 + ((ch ^ fswz(ra + 4)) * 16) + 8 * (lane & 1);
        MW();
#pragma unroll
        for (int ks = 0; ks < 4; ++ks) {
          const v4u a = lds128(L_XN + (lane & 7) * 1040 + (128 * j + 32 * ks + 8 * g4) * 2);
          const v2u b0 = trread(r0 + ks * 8192), b1 = trread(r1 + ks * 8192);
          const v2u c0 = trread((r0 ^ 128) + ks * 8192), c1 = trread((r1 ^ 128) + ks * 8192);
          if (ks == 2) MW();
          ag = mfma16(a, v4u{b0[0], b0[1], b1[0], b1[1]}, ag);
          au = mfma16(a, v4u{c0[0], c0[1], c1[0], c1[1]}, au);
        }
        MW();
        const v4u pv = __builtin_bit_cast(v4u, hh ? au : ag);
        const unsigned fo = WS_FIN + (4 * h + w) * 8192 + j * 2048 + lane * 16;
        st_sc1(rs, fo, v4u{pv[0], pv[1], pv[2], tag});
        st_sc1(rs, fo + 1024, v4u{pv[3], pv[3], pv[3], tag});
      }
      if (T_UV) {
        if (T_UV > 1) wait_until(t0, T_UV); else cnt_wait<2>(C_GO, 1);
        ld_uv();
      }
      asm volatile("s_waitcnt vmcnt(0)" : "+v"(dpf)::"memory");
      cnt_add(C_UV);
      if (ffn && w == 1) {
        v4u fv[8];
#pragma unroll 1
        for (unsigned spin = 0; spin < SPIN_CAP; ++spin) {
          MW();
          unsigned bad = 0;
#pragma unroll
          for (int t = 0; t < 8; ++t) fv[t] = ld_sc1(rs, WS_FIN + wi * 8192 + t * 1024 + lane * 16);
#pragma unroll
          for (int t = 0; t < 8; ++t) bad |= fv[t][3] ^ tag;
          if (!__any(bad != 0u)) break;
          __builtin_amdgcn_s_sleep(FX_SLP);
          CFENCE();
        }
        MW();
        float sv[4];
#pragma unroll
        for (int e = 0; e < 4; ++e) {
          const int o = e == 3, q = e % 3;
          sv[e] = (__uint_as_float(fv[o][q]) + __uint_as_float(fv[2 + o][q])) + (__uint_as_float(fv[4 + o][q]) + __uint_as_float(fv[6 + o][q]));
        }
#pragma unroll
        for (int e = 0; e < 2; ++e) {
          SB0();
          MW();
          const auto gu = __builtin_amdgcn_permlane32_swap(__float_as_uint(sv[e]), __float_as_uint(sv[e + 2]), false, false);
          const float gg = rbf(__uint_as_float(gu[0])), uu = rbf(__uint_as_float(gu[1]));
          const float inner = 0.7978845608028654f * (gg + 0.044715f * gg * gg * gg);
          const float th = 1.f - 2.f / (__expf(2.f * inner) + 1.f);
          SB0();
          MW();
          const float ge = rbf(0.5f * gg * (1.f + th));
          lds<u16>(L_HF + (4 * (g4 & 1) + 2 * hh + e) * 80 + i16 * 2) = (u16)(pk2(ge * uu, 0.f) & 0xFFFFu);
        }
        if (lane < 16) lds<v4u>(L_HF + (lane >> 1) * 80 + 32 + (lane & 1) * 16) = v4u{0u, 0u, 0u, 0u};
        MW();
        cnt_add(C_FFN);
      }
    }
    v4u uvf[4];
    MW();
    cnt_wait<2>(C_UV, 4);
#pragma unroll
    for (int ks = 0; ks < 4; ++ks) {
      const unsigned ra = 32 * (2 * (ks & 1) + (g4 >> 1)) + 16 * (ks >> 1) + 8 * (g4 & 1) + (i16 >> 2), rb = ra + 4, ch = 2 * w + ((lane & 3) >> 1);
      const v2u b0 = trread(L_WUV + ra * 256 + ((ch ^ fswz(ra)) * 16) + 8 * (lane & 1));
      const v2u b1 = trread(L_WUV + rb * 256 + ((ch ^ fswz(rb)) * 16) + 8 * (lane & 1));
      uvf[ks] = v4u{b0[0], b0[1], b1[0], b1[1]};
      PIN(uvf[ks]);
      MW();
    }
    unsigned cx = WS_CIN + wi * CIN_WI + (w ^ (CSW ? h & 7 : 0)) * 4096 + lane * 16;
    unsigned clx = L_CL + w * 272 + (((i16 >> 2) & 1) * 32 + (i16 >> 3) * 16 + (i16 & 3) * 4 + g4) * 4;
    PIN(cx); PIN(clx);
    MW();
    unsigned cla = L_CL + (lane & 7) * 272 + g4 * 16;
    unsigned cxw = L_CTX + (lane & 7) * 272 + (16 * w + 4 * g4) * 2;
    PIN(cla); PIN(cxw);
    MW();
    unsigned cxa[4];
#pragma unroll
    for (int u = 0; u < 4; ++u) { cxa[u] = L_CTX + ((lane & 7) ^ ((lane & 8) >> 2)) * 272 + (32 * ((u + j) & 3) + 8 * g4) * 2; PIN(cxa[u]); }
    unsigned eb = w >= 4 ? WS_EIN + ((4 * (g4 & 1) + 2 * hh) * 16 + 4 * (w - 4) + (i16 >> 2)) * 8192 + (wi ^ (ES2 ? (lane >> 2) * 4 : 0)) * 64 + (i16 & 3) * 16 : WS_EIN + wi * 8192 + lane * 16;
    unsigned f2a = WS_FLAG2 + (64 * (w & 1) + lane) * 1024 + fpos<FLW2>(wi >> 1) + (wi & 1) * 4;
    PIN(f2a);
    MW();
    unsigned msk[4];
#pragma unroll
    for (int q = 0; q < 4; ++q) { msk[q] = (i16 >> 1) == 2 * q ? ((i16 & 1) ? 0xFFFF0000u : 0xFFFFu) : 0u; PIN(msk[q]); }
    MW();
    PIN(cx); PIN(clx); PIN(cla); PIN(cxw); PIN(eb);
    {
      v2u m0, m1;
      unsigned ml, cap = SPIN_CAP;
      v4u dv[4];
      const unsigned ma = WS_ML + (wi * 128 + (w ^ (MS2 ? h & 7 : 0)) * 16) * 16 + ((lane & 31) ^ (CS2 ? (h & 7) * 4 + (j & 1) * 2 : 0)) * 8;
#define MLD(r) "global_atomic_add_x2 " r ", %8, %9, %10 sc0 sc1\n\t"
#define DLD "s_mov_b64 exec, -1\n\tglobal_load_dwordx4 %0, %11, %10 sc1\n\tglobal_load_dwordx4 %1, %11, %10 offset:1024 sc1\n\t" \
            "global_load_dwordx4 %2, %11, %10 offset:2048 sc1\n\tglobal_load_dwordx4 %3, %11, %10 offset:3072 sc1"
#define MLOPS : "=&v"(dv[0]), "=&v"(dv[1]), "=&v"(dv[2]), "=&v"(dv[3]), "=&{v[248:249]}"(m0), "=&{v[250:251]}"(m1), "=&v"(ml), "+s"(cap) \
              : "v"(ma), "v"(v2u{0u, 0u}), "s"(p.ws), "v"(cx), "s"(tag), "s"(0xFFFFFFFFull) : "memory", "vcc", "scc"
      if (MLDP == 2)
        asm volatile("s_mov_b64 exec, %13\n\t" MLD("v[248:249]") "s_sleep " STR(MLG) "\n1:\n\t" MLD("v[250:251]")
                     "s_waitcnt vmcnt(1)\n\tv_cmp_ne_u32 vcc, %12, v249\n\tv_mov_b32 %6, v248\n\ts_mov_b64 exec, vcc\n\ts_cbranch_vccz 2f\n\t" MLD("v[248:249]")
                     "s_sub_u32 %7, %7, 1\n\ts_waitcnt vmcnt(1)\n\tv_cmp_ne_u32 vcc, %12, v251\n\tv_mov_b32 %6, v250\n\ts_cselect_b64 vcc, 0, vcc\n\t"
                     "s_mov_b64 exec, vcc\n\ts_cbranch_vccnz 1b\n2:\n\t" DLD MLOPS);
      else
        asm volatile("s_mov_b64 exec, %13\n1:\n\t" MLD("v[248:249]") "s_sub_u32 %7, %7, 1\n\ts_waitcnt vmcnt(0)\n\t"
                     "v_cmp_ne_u32 vcc, %12, v249\n\tv_mov_b32 %6, v248\n\ts_cselect_b64 vcc, 0, vcc\n\t" MLX "s_cbranch_vccnz 1b\n\t" DLD MLOPS);
      MW();
      v4u aw[4];
      {
        const float o = xor1(__uint_as_float(ml));
        const float ms = (lane & 1) ? o : __uint_as_float(ml), lsv = (lane & 1) ? __uint_as_float(ml) : o;
        float M = rowmax(ms);
        M = rs_max16(M, M);
        const float ee = ex2(ms - M);
        const float wsv = 2.f * ee / add16(rowsum(ee * lsv));
        unsigned wb = pk2(wsv, wsv);
        PIN(wb);
        MW();
#pragma unroll
        for (int t = 0; t < 4; ++t) {
          const unsigned u = __shfl(wb, 8 * t + 2 * g4);
          aw[t] = v4u{u & msk[0], u & msk[1], u & msk[2], u & msk[3]};
          PIN(aw[t]);
          if (t & 1) MW();
        }
      }
      asm volatile("s_waitcnt vmcnt(0)" : "+v"(dv[0]), "+v"(dv[1]), "+v"(dv[2]), "+v"(dv[3]), "+{v[248:249]}"(m0), "+{v[250:251]}"(m1)::"memory");
      if (RARE(__any(max(max(dv[0][0], dv[1][0]), max(dv[2][0], dv[3][0])) == POISON))) {
#pragma unroll 1
        for (unsigned spin = 0; spin < SPIN_CAP; ++spin) {
          MW();
#pragma unroll
          for (int t = 0; t < 4; ++t) dv[t] = ld_sc1(rs, cx + t * 1024);
          if (!__any(max(max(dv[0][0], dv[1][0]), max(dv[2][0], dv[3][0])) == POISON)) break;
          CFENCE();
        }
      }
      MW();
      f32x4 dx = {0.f, 0.f, 0.f, 0.f}, dy = dx;
#pragma unroll
      for (int t = 0; t < 2; ++t) { dx = mfma16(aw[t], dv[t], dx); dy = mfma16(aw[t + 2], dv[t + 2], dy); }
      lds<unsigned>(clx) = pk2(dx[0] + dy[0], dx[1] + dy[1]);
    }
    wg_barrier();
    MW();
    if (F2AT == 0 && (w == 2 || w == 3)) st_sc1_32(rs, f2a, tag);
    {
      v4u a[4];
#pragma unroll
      for (int ks = 0; ks < 4; ++ks) a[ks] = lds128(cla + ks * 64);
      SB0();
      f32x4 c0 = mfma16(uvf[0], a[0], f32x4{0.f, 0.f, 0.f, 0.f}), c1 = mfma16(uvf[1], a[1], f32x4{0.f, 0.f, 0.f, 0.f});
      c0 = mfma16(uvf[2], a[2], c0);
      c1 = mfma16(uvf[3], a[3], c1);
      c0 += c1;
      lds<v2u>(cxw) = v2u{pk2(c0[0], c0[1]), pk2(c0[2], c0[3])};
    }
    wg_barrier();
    MW();
    if (__builtin_expect(w >= 4, 1)) {
      v4u a[4];
#pragma unroll
      for (int u = 0; u < 4; ++u) a[u] = lds128(cxa[u]);
      SB0();
#pragma unroll
      for (int u = 0; u < 4; ++u) {
#pragma unroll
        for (int jj = 0; jj < 8; ++jj) oc[jj] = mfma16(a[u], wof[u][jj], oc[jj]);
        if (u & 1) { SB0(); MW(); }
      }
#pragma unroll
      for (int e = 0; e < 2; ++e)
        __builtin_amdgcn_raw_buffer_store_b128(v4u{san(pk2(oc[0][e], oc[1][e])), san(pk2(oc[2][e], oc[3][e])), san(pk2(oc[4][e], oc[5][e])), san(pk2(oc[6][e], oc[7][e]))},
                                               rs, eb, e * 16 * 8192, 16);
    } else if (w == 0) {
      const unsigned oo = wi * 64 + (i16 & 3) * 16 + g4 * 4;
      const unsigned xf = *(const GAS unsigned*)((const char*)p.x + oo);
      const unsigned one = (i16 & 2) ? 0u : ((i16 & 1) ? 0x3F800000u : 0x3F80u);
      v4u sel;
#pragma unroll
      for (int q = 0; q < 4; ++q) sel[q] = (i16 >> 2) == q ? one : 0u;
      v2u f0;
      unsigned eb2 = eb + 4096;
      float x0 = bf2f(xf), x1 = bfhi(xf);
      PIN(sel); PIN(eb2); PIN(x0); PIN(x1);
      MW();
      poll_flags(p.ws, WS_FLAG2 + wi * 1024 + fpos<FLW2>(lane), tag, f0);
      MW();
      if (E_DLY) __builtin_amdgcn_s_sleep(E_DLY);
      float r0, r1;
      v4u ev[8];
      auto esum = [&]() AI {
#pragma unroll
        for (int t = 0; t < 8; ++t) ev[t] = ld_sc1(rs, (t < 4 ? eb : eb2) + (t & 3) * 1024);
        SB0();
        f32x4 d[4];
#pragma unroll
        for (int t = 0; t < 8; ++t) d[t & 3] = mfma16(sel, ev[t], t < 4 ? f32x4{0.f, 0.f, 0.f, 0.f} : d[t & 3]);
        MW();
        r0 = (d[0][0] + d[1][0]) + (d[2][0] + d[3][0]); r1 = (d[0][1] + d[1][1]) + (d[2][1] + d[3][1]);
        r0 += ror16<8>(r0); r1 += ror16<8>(r1);
        r0 += ror16<4>(r0); r1 += ror16<4>(r1);
      };
      esum();
      if (RARE(__any(r0 != r0))) {
#pragma unroll 1
        for (unsigned spin = 0; spin < SPIN_CAP; ++spin) {
          MW();
          unsigned mxv = 0;
#pragma unroll
          for (int t = 0; t < 8; ++t) mxv = max(mxv, max(max(ev[t][0], ev[t][1]), max(ev[t][2], ev[t][3])));
          if (__all(mxv != POISON)) break;
          CFENCE();
          MW();
          esum();
          if (!__any(r0 != r0)) break;
        }
      }
      MW();
      asm volatile("s_mov_b64 exec, %3\n\tglobal_store_dword %0, %1, %2\n\ts_mov_b64 exec, -1" ::"v"(oo), "v"(pk2(x0 + r0, x1 + r1)), "s"(p.out), "s"(0x000F000F000F000Full) : "memory");
      asm volatile("s_waitcnt vmcnt(0)" : "+v"(f0)::"memory");
    } else if (w >= 2) {
      MW();
      if (F2AT == 1) st_sc1_32(rs, f2a, tag);
      if (wi == 0 && tid == 128) st_sc1_32(rs, WS_GEN, gen1);
    }
  }
}

extern "C" void kernel_launch(void* const* d_in, const int* in_sizes, int n_in, void* d_out, int out_size, void* d_ws,
                              size_t ws_size, hipStream_t stream) {
  Params p;
  p.x = (const u16*)d_in[0];
  p.ckv = (const u16*)d_in[1];
  p.wq = (const u16*)d_in[2];
  p.wuk = (const u16*)d_in[3];
  p.wuv = (const u16*)d_in[4];
  p.wo = (const u16*)d_in[5];
  p.wg = (const u16*)d_in[7];
  p.wu = (const u16*)d_in[8];
  p.wd = (const u16*)d_in[9];
  p.out = (u16*)d_out;
  p.ws = (unsigned char*)d_ws;
  (void)hipFuncSetAttribute((const void*)fused_kernel, hipFuncAttributeMaxDynamicSharedMemorySize, LDS_BYTES);
  fused_kernel<<<256, 512, LDS_BYTES, stream>>>(p);
}
```

```cpp
#include <hip/hip_runtime.h>
#include <stdint.h>

typedef unsigned short u16;
typedef unsigned long long u64;
typedef unsigned v4u __attribute__((ext_vector_type(4)));
typedef unsigned v2u __attribute__((ext_vector_type(2)));
typedef float f32x4 __attribute__((ext_vector_type(4)));
typedef float f32x16 __attribute__((ext_vector_type(16)));
typedef unsigned v16u __attribute__((ext_vector_type(16)));
typedef float v2f __attribute__((ext_vector_type(2)));
typedef short bf16x8 __attribute__((ext_vector_type(8)));
typedef short v4s __attribute__((ext_vector_type(4)));
typedef __bf16 v2bf __attribute__((ext_vector_type(2)));
#define GAS __attribute__((address_space(1)))
#define LAS __attribute__((address_space(3)))
#define DI __device__ __forceinline__

#define NF 12
#define R2AT (47 + NF)
#define POLL_START 250
#define POLL_GAP 4
#define Q_DLY 0
#define KV_DLY 50
#define E_DLY 10
#define T_F 450
#define T_UV 900
#define T_O 680
#define MLAT 17
#define FR4 1
#define PAUSE 1
#define QPF 0
#define WPF 2
#define BPF 0
#define BPF_DLY 0
#define WPF_DLY 0
#define FMAP 0
#define OD 6
#define V3 4
#define QPRIO 1
#define PK 8
#define WM_DLY 110
#define FPF 0
#define FLAG_AT 1
#define FL_DLY 0
#define PZ_DLY 0
#define B0W 2
#define NF2 16
#define PMIN 32
#define VD 2
#define MIDSIG 1
#define V2AT 0
#define CKAT 10
#define PPRIO 0
#define SPAT 13
#define MIX 0
#define PROG 1
#define RES 1
#define XAT 4
#define PUTAT 3
#define FLAG_FIRST 0
#define F2AT 0
#define GO_DLY 100
#define C_DLY 0
#define KPF 0
#define KPF_AT 0
#define KPF_S 1
#define FX_SLP 8
#define OSAT 11
#define PZE 0
#define PZT 0
#define PZ_AT 150
#define K1AT 1
#define CSW 1
#define FLW 0
#define FLW2 0
#define BMAP 0
#define EMAP 0
#define G0T 1
#define KPF_ST 0
#define K3S 0
#define QST 0
#define F2D 3
#define F2G 4
#define F2X 1
#define QPM 1
#define MLDP 1
#define OVTV 88.f
#define CS2 1
#define MS2 1
#define ES2 1
#define VSP 1
#define CVW 1
#define KSP 1
#define QL 4
#define YB 5
#define XR 15
#define XC 20
#define MLP 1
#define MLG 10
#define NFW 10

constexpr int DM = 512, FF = 1376;
constexpr unsigned WS_GEN = 0;
constexpr unsigned WS_FLAG = 4096;
constexpr unsigned WS_FLAG2 = WS_FLAG + 262144;
constexpr unsigned WS_QABS = WS_FLAG2 + 131072;
constexpr unsigned WS_ML = WS_QABS + 524288;
constexpr unsigned WS_EIN = WS_ML + 262144;
constexpr unsigned WS_CIN = WS_EIN + 1048576;
constexpr unsigned CIN_ROW = 256, CIN_WI = 128 * CIN_ROW;
constexpr unsigned WS_FIN = WS_CIN + 128 * CIN_WI;
constexpr unsigned WS_END = WS_FIN + 88 * 8192;
constexpr unsigned L_Q = 0;
constexpr unsigned L_P = L_Q;
constexpr unsigned L_LS = L_Q + 24576;
constexpr unsigned L_AL = L_Q + 25600;
constexpr unsigned L_SLOT = 32768;
constexpr unsigned L_XN = 0;
constexpr unsigned L_QH = 8448;
constexpr unsigned L_CL = L_QH + 2176;
constexpr unsigned L_CTX = L_CL + 2176;
constexpr unsigned L_HF = L_CTX + 2176;
constexpr unsigned L_FP = L_HF + 640;
constexpr unsigned L_WQ = 32768;
constexpr unsigned L_GU = 65536;
constexpr unsigned L_WUV = 98304;
constexpr unsigned L_CNT = 32512;
constexpr unsigned LDS_BYTES = 163840;
enum { C_P = 0, C_OVF = 1, C_ST = 2, C_FL = 3, C_MX = 5, C_PVD, C_XS, C_MASK = 8, C_N = 16 };
enum { C_QST = 0, C_GU, C_FFN, C_UV, C_GO, C_SENT };

constexpr unsigned ANY = 0x80u;
constexpr unsigned POISON = 0xFFFFFFFFu;
constexpr unsigned SPIN_CAP = 1u << 20;

struct Params {
  const u16 *x, *ckv, *wq, *wuk, *wuv, *wo, *wg, *wu, *wd;
  u16* out;
  unsigned char* ws;
};

DI float bf2f(unsigned v) { return __uint_as_float(v << 16); }
DI float bfhi(unsigned v) { return __uint_as_float(v & 0xFFFF0000u); }
DI unsigned pk2(float a, float b) {
  v2f t = {a, b};
  return __builtin_bit_cast(unsigned, __builtin_convertvector(t, v2bf));
}
DI float rbf(float a) { return bf2f(pk2(a, 0.f) & 0xFFFFu); }
DI unsigned san(unsigned v) { return min(v, 0xFFFFFFFEu); }
DI f32x4 mfma16(v4u a, v4u b, f32x4 c) {
  return __builtin_amdgcn_mfma_f32_16x16x32_bf16(__builtin_bit_cast(bf16x8, a), __builtin_bit_cast(bf16x8, b), c, 0, 0, 0);
}
DI f32x16 mfma32(v4u a, v4u b, f32x16 c) {
  return __builtin_amdgcn_mfma_f32_32x32x16_bf16(__builtin_bit_cast(bf16x8, a), __builtin_bit_cast(bf16x8, b), c, 0, 0, 0);
}
DI v4u lds128(unsigned a) { return *(LAS v4u*)(size_t)a; }
DI v2u trread(unsigned a) { return __builtin_bit_cast(v2u, __builtin_amdgcn_ds_read_tr16_b64_v4i16((LAS v4s*)(size_t)a)); }
template <class T> DI LAS T& lds(unsigned a) { return *(LAS T*)(size_t)a; }
template <int POL> DI void glds16(const void* gsrc, unsigned lds_dst) {
  unsigned keep;
  if (POL == 1) asm volatile("s_mov_b32 %0, m0\n\ts_mov_b32 m0, %2\n\ts_nop 0\n\tglobal_load_lds_dwordx4 %1, off nt\n\ts_mov_b32 m0, %0" : "=&s"(keep) : "v"(gsrc), "s"(lds_dst) : "memory");
  else if (POL == 2) asm volatile("s_mov_b32 %0, m0\n\ts_mov_b32 m0, %2\n\ts_nop 0\n\tglobal_load_lds_dwordx4 %1, off sc1\n\ts_mov_b32 m0, %0" : "=&s"(keep) : "v"(gsrc), "s"(lds_dst) : "memory");
  else asm volatile("s_mov_b32 %0, m0\n\ts_mov_b32 m0, %2\n\ts_nop 0\n\tglobal_load_lds_dwordx4 %1, off\n\ts_mov_b32 m0, %0" : "=&s"(keep) : "v"(gsrc), "s"(lds_dst) : "memory");
}
DI void glds16s(unsigned voff, const void* sbase, unsigned lds_dst) {
  unsigned keep;
  asm volatile("s_mov_b32 %0, m0\n\ts_mov_b32 m0, %3\n\ts_nop 0\n\tglobal_load_lds_dwordx4 %1, %2 nt\n\ts_mov_b32 m0, %0" : "=&s"(keep) : "v"(voff), "s"(sbase), "s"(lds_dst) : "memory");
}
DI void gld16s(v4u& d, unsigned voff, const void* sbase) {
  asm volatile("global_load_dwordx4 %0, %1, %2 nt" : "=v"(d) : "v"(voff), "s"(sbase) : "memory");
}
#define VMWAIT(n) asm volatile("s_waitcnt vmcnt(%0)" ::"i"(n) : "memory")
#define LGKM0() asm volatile("s_waitcnt lgkmcnt(0)" ::: "memory")
#define CFENCE() asm volatile("" ::: "memory")
#define SB0() __builtin_amdgcn_sched_barrier(0)
#define W8(a, o) "+v"(a[o]), "+v"(a[o + 1]), "+v"(a[o + 2]), "+v"(a[o + 3]), "+v"(a[o + 4]), "+v"(a[o + 5]), "+v"(a[o + 6]), "+v"(a[o + 7])
#define WARMUP 0
#define MK(n) do {} while (0)
#define MK_TABLE() do {} while (0)
#define WARM(A, B, first, step) do {} while (0)

#define MW() MK(2)
#define MQ() MK(4)
#define MP() MK(6)
#define MS() MK(8)

DI void wg_barrier() {
  LGKM0();
  __builtin_amdgcn_s_barrier();
  CFENCE();
}
DI void cnt_addv(unsigned c, unsigned v) {
  asm volatile("s_mov_b64 exec, 1\n\tds_add_u32 %0, %1 offset:%2\n\ts_mov_b64 exec, -1" ::"v"(0u), "v"(v), "i"(L_CNT + c * 4u) : "memory");
}
DI void cnt_add(unsigned c) { cnt_addv(c, 1u); }
DI void cnt_add2(unsigned c, unsigned v0, unsigned v1) {
  asm volatile("s_mov_b64 exec, 1\n\tds_add_u64 %0, %1 offset:%2\n\ts_mov_b64 exec, -1" ::"v"(0u), "v"(v2u{v0, v1}), "i"(L_CNT + c * 4u) : "memory");
}
#define AI __attribute__((always_inline))
#define RARE(c) __builtin_expect(!!(c), 0)
DI unsigned vm_pending() {
  unsigned v;
  asm volatile("s_getreg_b32 %0, hwreg(HW_REG_IB_STS)" : "=s"(v));
  return (v & 15u) | ((v >> 18) & 48u);
}
#define STR_(x) #x
#define STR(x) STR_(x)
#define VW1(n) "s_waitcnt vmcnt(" #n ")\n\ts_branch 2f\n\t"
#define MKR
#define MKB
#define L_CNT_ 32512
#define KV_RUN_BODY() asm volatile(".subsection 10\n" \
  MKR ".Lkv_chk:\n\t" \
  "s_mov_b32 s67, 1\n\ts_mov_b32 s91, m0\n\ts_mov_b32 s90, 0xffffff\n\ts_branch 12f\n" \
  ".Lkv_runp:\n\t" \
  "s_mov_b32 s67, 1\n\ts_branch 11f\n" \
  ".Lkv_run:\n\t" \
  "s_mov_b32 s67, 2\n" \
  "11:\n\t" \
  "s_mov_b32 s91, m0\n\ts_mov_b32 s90, 0xffffff\n\ts_mov_b32 s64, -1\n" \
  "3:\n\t" \
  "v_mov_b32 v252, 0\n\t" \
  "ds_read_b128 v[252:255], v252 offset:%0\n\t" \
  "s_cmp_lg_u32 s67, 1\n\ts_cbranch_scc1 12f\n\t" \
  "v_add_u32 v249, s74, v250\n\t" \
  "s_bitcmp1_b32 s74, 12\n\ts_cbranch_scc1 14f\n\t" \
  "ds_read_b128 v[232:235], v249\n\tds_read_b128 v[236:239], v249 offset:1024\n\tds_read_b128 v[240:243], v249 offset:2048\n\tds_read_b128 v[244:247], v249 offset:3072\n\t" \
  "s_branch 12f\n" \
  "14:\n\t" \
  "ds_read_b128 v[216:219], v249\n\tds_read_b128 v[220:223], v249 offset:1024\n\tds_read_b128 v[224:227], v249 offset:2048\n\tds_read_b128 v[228:231], v249 offset:3072\n" \
  "12:\n\t" \
  "s_cmp_eq_u32 s77, 4\n\ts_cbranch_scc1 6f\n\t" \
  "s_getreg_b32 s87, hwreg(HW_REG_IB_STS)\n\t" \
  "s_lshl_b32 s89, s77, 4\n\ts_add_u32 s89, s89, 16\n\t" \
  "s_lshr_b32 s88, s87, 18\n\ts_and_b32 s88, s88, 48\n\ts_and_b32 s87, s87, 15\n\ts_or_b32 s87, s87, s88\n\t" \
  "s_add_u32 s89, s89, s87\n\ts_cmp_lt_u32 s76, s89\n\ts_cbranch_scc1 4f\n\t" \
  "s_min_u32 s88, s87, 23\n\ts_lshl_b32 s88, s88, 3\n\t" \
  "s_getpc_b64 s[94:95]\n0:\n\ts_add_u32 s88, s88, 1f-0b\n\ts_add_u32 s94, s94, s88\n\ts_addc_u32 s95, s95, 0\n\ts_setpc_b64 s[94:95]\n" \
  MKR "1:\n\t" \
  VW1(0) VW1(1) VW1(2) VW1(3) VW1(4) VW1(5) VW1(6) VW1(7) VW1(8) VW1(9) VW1(10) VW1(11) VW1(12) VW1(13) VW1(14) VW1(15) \
  VW1(16) VW1(17) VW1(18) VW1(19) VW1(20) VW1(21) VW1(22) VW1(23) \
  MKR "2:\n\t" \
  "s_lshl_b32 s88, s77, 2\n\ts_add_u32 s88, s88, s83\n\ts_lshl_b32 s88, 1, s88\n\t" \
  "v_mov_b32 v248, 0\n\tv_mov_b32 v249, s88\n\ts_mov_b64 exec, 1\n\tds_add_u32 v248, v249 offset:%1\n\ts_mov_b64 exec, -1\n\t" \
  "s_add_u32 s77, s77, 1\n" \
  "4:\n\t" \
    \
  "s_lshr_b32 s89, s86, s83\n\ts_and_b32 s89, s89, 0x11000000\n\ts_and_b32 s88, s86, 0x440000\n\ts_or_b32 s89, s89, s88\n\t" \
  "s_cmp_eq_u32 s89, 0x11440000\n\ts_cselect_b32 s88, 64, 48\n\t" \
  "s_and_b32 s89, s89, 0x1040000\n\ts_cmp_eq_u32 s89, 0x1040000\n\ts_cselect_b32 s88, s88, 32\n\t" \
  "s_and_b32 s89, s75, %2 | 0x100\n\ts_cmp_eq_u32 s89, 0x100\n\ts_cselect_b32 s89, " STR(PMIN) ", 64\n\ts_min_u32 s88, s88, s89\n\t" \
  "s_and_b32 s89, s75, %2\n\ts_cselect_b32 s89, " STR(NF2) ", " STR(NF) "\n\t" \
  "s_add_u32 s89, s89, s76\n\ts_sub_u32 s89, s89, s87\n\ts_min_u32 s88, s88, s89\n\t" \
  "s_cmp_ge_i32 s76, s88\n\ts_cbranch_scc1 6f\n" \
  "5:\n\t" \
  "v_readlane_b32 s89, v251, s76\n\ts_add_u32 s76, s76, 1\n\ts_and_b32 s87, s89, 0x1fc00\n\tv_xor_b32 v249, s89, v250\n\t" \
  "s_add_u32 m0, s87, s82\n\ts_cmp_lt_u32 s76, s88\n\tglobal_load_lds_dwordx4 v249, s[80:81] nt\n\ts_cbranch_scc1 5b\n\t" \
  "6:\n\t" MKR \
  "s_waitcnt lgkmcnt(0)\n\t" \
  "v_readfirstlane_b32 s85, v252\n\tv_readfirstlane_b32 s86, v254\n\tv_readfirstlane_b32 s84, v253\n\tv_readfirstlane_b32 s75, v255\n\t" \
  "s_bitset1_b32 s85, 7\n\ts_and_b32 s89, s86, s78\n\ts_cmp_eq_u32 s89, s78\n\ts_cbranch_scc0 9f\n\ts_and_b32 s89, s85, s79\n\ts_cbranch_scc0 9f\n\t" \
  "s_cmp_lg_u32 s67, 0\n\ts_cbranch_scc1 10f\n" \
  "9:\n\ts_cmp_eq_u32 s67, 2\n\ts_cbranch_scc1 13f\n\ts_and_b32 s89, s85, s68\n\ts_cselect_b32 s67, 1, 0\n" \
  "13:\n\ts_mov_b32 s89, s79\n\ts_cmp_eq_u32 s76, s64\n\ts_cbranch_scc1 17f\n\ts_sub_u32 s90, s90, 1\n\ts_cbranch_scc0 3b\n" \
  "10:\n\ts_mov_b32 s64, 0\n" \
  "17:\n\ts_mov_b32 m0, s91\n\ts_mov_b32 s87, s64\n" \
  "15:\n\ts_cmp_eq_u32 s77, 2\n\ts_cselect_b32 s66, 0, 15\n\ts_or_b32 s66, s66, 0xc00000\n\t" \
  "s_lshr_b32 s65, s77, 2\n\ts_cmp_eq_u32 s76, 64\n\ts_cselect_b32 s65, s65, 1\n\ts_lshl_b32 s65, s65, 8\n\ts_cmp_eq_u32 s87, 0\n\ts_setpc_b64 s[92:93]\n" \
  MKR ".Lkv_sig:\n\t" \
  "s_waitcnt vmcnt(16)\n\ts_cmp_eq_u32 s77, 2\n\ts_cbranch_scc1 16f\n\ts_waitcnt vmcnt(0)\n" \
  "16:\n\ts_lshl_b32 s88, s77, 2\n\ts_add_u32 s88, s88, s83\n\ts_lshl_b32 s88, 1, s88\n\t" \
  "v_mov_b32 v248, 0\n\tv_mov_b32 v249, s88\n\ts_mov_b64 exec, 1\n\tds_add_u32 v248, v249 offset:%1\n\ts_mov_b64 exec, -1\n\t" \
  "s_add_u32 s77, s77, 1\n\ts_mov_b32 s87, 0\n\ts_cmp_lg_u32 s77, 4\n\ts_cbranch_scc1 15b\n\ts_cmp_eq_u32 s98, 0\n\ts_cbranch_scc1 15b\n" \
    \
  ".Lkv_y3:\n\ts_mov_b32 s88, s98\n\tv_mov_b32 v248, 0\n" \
  "20:\n\tds_read_b32 v249, v248 offset:%1\n\ts_sub_u32 s88, s88, 1\n\ts_cbranch_scc1 21f\n\ts_waitcnt lgkmcnt(0)\n\t" \
  "v_and_b32 v249, 0xf000, v249\n\tv_cmp_ne_u32 vcc, 0xf000, v249\n\ts_cbranch_vccnz 20b\n\ts_sleep " STR(VD) "\n" \
  MKR ".Lkv_ldv:\n\t" \
  "v_xor_b32 v248, 0, v215\n\tv_xor_b32 v249, 32, v215\n\t" \
  "ds_read_b64_tr_b16 v[128:129], v248 offset:0\n\tds_read_b64_tr_b16 v[130:131], v249 offset:4096\n\t" \
  "ds_read_b64_tr_b16 v[132:133], v248 offset:16384\n\tds_read_b64_tr_b16 v[134:135], v249 offset:20480\n\t" \
  "ds_read_b64_tr_b16 v[136:137], v248 offset:32768\n\tds_read_b64_tr_b16 v[138:139], v249 offset:36864\n\t" \
  "ds_read_b64_tr_b16 v[140:141], v248 offset:49152\n\tds_read_b64_tr_b16 v[142:143], v249 offset:53248\n\t" \
  "v_xor_b32 v248, 64, v215\n\tv_xor_b32 v249, 96, v215\n\t" \
  "ds_read_b64_tr_b16 v[144:145], v248 offset:0\n\tds_read_b64_tr_b16 v[146:147], v249 offset:4096\n\t" \
  "ds_read_b64_tr_b16 v[148:149], v248 offset:16384\n\tds_read_b64_tr_b16 v[150:151], v249 offset:20480\n\t" \
  "ds_read_b64_tr_b16 v[152:153], v248 offset:32768\n\tds_read_b64_tr_b16 v[154:155], v249 offset:36864\n\t" \
  "ds_read_b64_tr_b16 v[156:157], v248 offset:49152\n\tds_read_b64_tr_b16 v[158:159], v249 offset:53248\n\t" \
  MKR "v_xor_b32 v248, 128, v215\n\tv_xor_b32 v249, 160, v215\n\t" \
  "ds_read_b64_tr_b16 v[160:161], v248 offset:0\n\tds_read_b64_tr_b16 v[162:163], v249 offset:4096\n\t" \
  "ds_read_b64_tr_b16 v[164:165], v248 offset:16384\n\tds_read_b64_tr_b16 v[166:167], v249 offset:20480\n\t" \
  "ds_read_b64_tr_b16 v[168:169], v248 offset:32768\n\tds_read_b64_tr_b16 v[170:171], v249 offset:36864\n\t" \
  "ds_read_b64_tr_b16 v[172:173], v248 offset:49152\n\tds_read_b64_tr_b16 v[174:175], v249 offset:53248\n\t" \
  "v_xor_b32 v248, 192, v215\n\tv_xor_b32 v249, 224, v215\n\t" \
  "ds_read_b64_tr_b16 v[176:177], v248 offset:0\n\tds_read_b64_tr_b16 v[178:179], v249 offset:4096\n\t" \
  "ds_read_b64_tr_b16 v[180:181], v248 offset:16384\n\tds_read_b64_tr_b16 v[182:183], v249 offset:20480\n\t" \
  "ds_read_b64_tr_b16 v[184:185], v248 offset:32768\n\tds_read_b64_tr_b16 v[186:187], v249 offset:36864\n\t" \
  "ds_read_b64_tr_b16 v[188:189], v248 offset:49152\n\tds_read_b64_tr_b16 v[190:191], v249 offset:53248\n\t" \
  "s_mov_b32 s99, 1\n\ts_mov_b32 s87, 0\n\ts_branch 15b\n" \
  "21:\n\ts_waitcnt lgkmcnt(0)\n\ts_mov_b32 s87, 0\n\ts_cmp_lt_u32 s98, 0x1000\n\ts_cbranch_scc1 15b\n\ts_branch .Lkv_ldv\n\t" \
  ".subsection 12\n\t.fill 64, 4, 0xBF810000\n\t.subsection 0" \
  :: "i"(L_CNT), "i"(L_CNT + C_ST * 4), "i"(RES ? 4 : 0x40))
DI unsigned cnt_add_rtn(unsigned c) {
  u64 keep;
  unsigned v;
  asm volatile("s_mov_b64 %0, exec\n\ts_mov_b64 exec, 1\n\tds_add_rtn_u32 %1, %2, %3 offset:%4\n\ts_waitcnt lgkmcnt(0)\n\ts_mov_b64 exec, %0"
               : "=&s"(keep), "=&v"(v) : "v"(c * 4u), "v"(1u), "i"(L_CNT) : "memory");
  return __builtin_amdgcn_readfirstlane(v);
}
DI unsigned cnt_get(unsigned c) {
  unsigned v;
  asm volatile("ds_read_b32 %0, %1 offset:%2\n\ts_waitcnt lgkmcnt(0)" : "=v"(v) : "v"(c * 4u), "i"(L_CNT) : "memory");
  return __builtin_amdgcn_readfirstlane(v);
}
template <int SLP = 0> DI void cnt_wait(unsigned c, unsigned target) {
#pragma unroll 1
  for (unsigned s = 0; s < SPIN_CAP * 16u; ++s) {
    if (cnt_get(c) >= target) break;
    if (SLP) __builtin_amdgcn_s_sleep(SLP);
  }
}
DI unsigned now32() { return (unsigned)__builtin_amdgcn_s_memrealtime(); }
DI void wait_until(unsigned t0, unsigned ticks) {
  if (ticks == 0) return;
#pragma unroll 1
  while (now32() - t0 < ticks) __builtin_amdgcn_s_sleep(1);
}
DI v4u ld_sc1(__amdgpu_buffer_rsrc_t rs, unsigned off) { return __builtin_amdgcn_raw_buffer_load_b128(rs, off, 0, 16); }
DI v2u ld_sc1_64(__amdgpu_buffer_rsrc_t rs, unsigned off) { return __builtin_amdgcn_raw_buffer_load_b64(rs, off, 0, 16); }
DI void st_sc1(__amdgpu_buffer_rsrc_t rs, unsigned off, v4u v) { __builtin_amdgcn_raw_buffer_store_b128(v, rs, off, 0, 16); }
DI void st_sc1_64(__amdgpu_buffer_rsrc_t rs, unsigned off, v2u v) { __builtin_amdgcn_raw_buffer_store_b64(v, rs, off, 0, 16); }
DI void st_sc1_32(__amdgpu_buffer_rsrc_t rs, unsigned off, unsigned v) { __builtin_amdgcn_raw_buffer_store_b32(v, rs, off, 0, 16); }
DI unsigned fswz(unsigned row) { return ((row & 3u) << 2) | ((0x78u >> (2u * ((row >> 2) & 3u))) & 3u); }
template <int N> DI float ror16(float v) {
  return __int_as_float(__builtin_amdgcn_update_dpp(0, __float_as_int(v), 0x120 + N, 0xF, 0xF, false));
}
DI float xor1(float v) { return __int_as_float(__builtin_amdgcn_update_dpp(0, __float_as_int(v), 0xB1, 0xF, 0xF, false)); }
DI float add16(float v) { auto r = __builtin_amdgcn_permlane16_swap(__float_as_uint(v), __float_as_uint(v), false, false); return __uint_as_float(r[0]) + __uint_as_float(r[1]); }
DI float add32(float v) { auto r = __builtin_amdgcn_permlane32_swap(__float_as_uint(v), __float_as_uint(v), false, false); return __uint_as_float(r[0]) + __uint_as_float(r[1]); }
DI float rs_add16(float a, float b) { auto r = __builtin_amdgcn_permlane16_swap(__float_as_uint(a), __float_as_uint(b), false, false); return __uint_as_float(r[0]) + __uint_as_float(r[1]); }
DI float rs_add32(float a, float b) { auto r = __builtin_amdgcn_permlane32_swap(__float_as_uint(a), __float_as_uint(b), false, false); return __uint_as_float(r[0]) + __uint_as_float(r[1]); }
DI float rs_max16(float a, float b) { auto r = __builtin_amdgcn_permlane16_swap(__float_as_uint(a), __float_as_uint(b), false, false); return fmaxf(__uint_as_float(r[0]), __uint_as_float(r[1])); }
DI float max32(float v) { auto r = __builtin_amdgcn_permlane32_swap(__float_as_uint(v), __float_as_uint(v), false, false); return fmaxf(__uint_as_float(r[0]), __uint_as_float(r[1])); }
DI float rowsum(float v) { v += ror16<8>(v); v += ror16<4>(v); v += ror16<2>(v); v += ror16<1>(v); return v; }
DI float rowmax(float v) { v = fmaxf(v, ror16<8>(v)); v = fmaxf(v, ror16<4>(v)); v = fmaxf(v, ror16<2>(v)); v = fmaxf(v, ror16<1>(v)); return v; }
DI float ex2(float v) { return __builtin_amdgcn_exp2f(v); }
constexpr float LOG2E = 1.4426950408889634f;

DI void transpose8(const v4u (&r)[8], v4u (&c)[8]) {
#pragma unroll
  for (int q = 0; q < 4; ++q) {
    if (q == 0 || q == 2) { SB0(); MW(); }
#pragma unroll
    for (int e2 = 0; e2 < 4; ++e2) {
      unsigned a = r[2 * e2][q], b = r[2 * e2 + 1][q];
      c[2 * q][e2] = __builtin_amdgcn_perm(b, a, 0x05040100u);
      c[2 * q + 1][e2] = __builtin_amdgcn_perm(b, a, 0x07060302u);
    }
    asm volatile("" : "+v"(c[2 * q]), "+v"(c[2 * q + 1]));
  }
}
#define PLD(f) asm volatile("global_atomic_add_x2 %0, %1, %2, %3 sc0 sc1" : "=&v"(f) : "v"(fo), "v"(zz), "s"(wsp) : "memory")
#define PLDM(f) asm volatile("s_not_b64 exec, %4\n\tglobal_atomic_add_x2 %0, %1, %2, %3 sc0 sc1\n\ts_mov_b64 exec, -1" : "+v"(f) : "v"(fo), "v"(zz), "s"(wsp), "s"(seen) : "memory", "scc")
#define PL1 "global_atomic_add_x2 %0, %2, %3, %4 sc0 sc1\n\t"
#define PL1S PL1 "s_sleep " STR(F2G) "\n\t"
#define PLPRE PL1S PL1S
#define F2XS "s_mov_b64 exec, vcc\n\t"
#define MLX "s_mov_b64 exec, vcc\n\t"
DI void poll_flags(const unsigned char* wsp, unsigned fo, unsigned tag, v2u& f) {
  unsigned cap = SPIN_CAP;
  asm volatile(PLPRE "1:\n\t" PL1 "s_sub_u32 %1, %1, 1\n\ts_waitcnt vmcnt(" STR(F2D) " - 1)\n\tv_cmp_ne_u64 vcc, %5, %0\n\ts_cselect_b64 vcc, 0, vcc\n\t" F2XS
               "s_cbranch_vccnz 1b\n\ts_mov_b64 exec, -1"
               : "=&v"(f), "+s"(cap) : "v"(fo), "v"(v2u{0u, 0u}), "s"(wsp), "s"((u64)tag * 0x100000001ull) : "memory", "vcc", "scc");
}

template <int N> DI unsigned fpos(unsigned l) { return N ? (l % N) * 128 + (l / N) * 8 : l * 8; }
DI void send_flags(__amdgpu_buffer_rsrc_t rs, int wi, int lane, unsigned tag) {
#pragma unroll
  for (int t = 0; t < (T_O == 0 || T_F == 0 || T_UV == 1 ? 4 : 2); ++t) st_sc1_32(rs, WS_FLAG + (64 * t + lane) * 1024 + fpos<FLW>(wi >> 1) + (wi & 1) * 4, tag);
}

#define PIN(x) asm volatile("" : "+v"(x))
__global__ void __launch_bounds__(512, 2) fused_kernel(Params p) {
  extern __shared__ __attribute__((aligned(16))) unsigned char smem[];
  const u64 t0l = __builtin_amdgcn_s_memrealtime();
  const unsigned t0 = (unsigned)t0l;
  const int tid = threadIdx.x, lane = tid & 63;
  const int w = __builtin_amdgcn_readfirstlane(tid >> 6);
  const int cu = blockIdx.x;
  const int xcd = cu & 7, r8 = cu >> 3, idx = r8 >> 1;
  const __amdgpu_buffer_rsrc_t rs = __builtin_amdgcn_make_buffer_rsrc((void*)p.ws, 0, (int)WS_END, 0x00020000);
  const int g4 = lane >> 4, i16 = lane & 15;
  unsigned gen;
  asm volatile("s_load_dword %0, %1, 0x0" : "=s"(gen) : "s"(p.ws), "s"((unsigned)(size_t)(LAS unsigned char*)smem) : "memory");
  unsigned gen1;
#define GET_TAG() (__extension__({ asm volatile("s_waitcnt lgkmcnt(0)\n\ts_add_u32 %0, %1, 1" : "=s"(gen1) : "s"(gen) : "memory", "scc"); (gen1 & 0x3FFFu) | ((gen1 << 2) & 0x3FFF0000u); }))

  MK_TABLE();
  if ((r8 & 1) == 0) {
    const int b = BMAP ? idx & 7 : xcd, s = BMAP ? xcd + (idx & 8) : idx, kvi = b * 16 + s;
    const unsigned hh = lane >> 5, hd = lane & 31;
    WARM(w, e, idx * 8 + w, 128);
    auto wait_p = [&](unsigned pm, unsigned fm) AI {
      v4u cv;
#pragma unroll 1
      for (unsigned sp = 0; sp < SPIN_CAP * 16u; ++sp) {
        asm volatile("ds_read_b128 %0, %1 offset:%2\n\ts_waitcnt lgkmcnt(0)" : "=v"(cv) : "v"(0u), "i"(L_CNT) : "memory");
        if ((__builtin_amdgcn_readfirstlane(cv[0]) & pm) && (__builtin_amdgcn_readfirstlane(cv[3]) & fm) == fm) break;
      }
      return __builtin_amdgcn_readfirstlane(cv[1]);
    };
    auto wait_bits = [&](unsigned c, unsigned m) AI {
#pragma unroll 1
      for (unsigned sp = 0; sp < SPIN_CAP * 16u; ++sp)
        if ((cnt_get(c) & m) == m) break;
    };
    if (w >= 4) {
      const int k = w - 4;
      (void)GET_TAG();
      unsigned l16 = lane * 16, ap0;
      {
        const unsigned blk = (lane >> 4) & 1, q4 = i16 >> 2, pp = lane & 3;
        const unsigned r0 = 8 * hh + q4;
        const unsigned ch = 16 * k + 2 * (pp & 1) + blk;
        ap0 = L_SLOT + r0 * 1024 + ((ch ^ fswz(r0)) * 16) + 8 * (pp >> 1);
      }
      PIN(l16); PIN(ap0);
      v4u vf[16];
      v16u vo[4];
      unsigned ap1 = ap0 + 65536, yb = 0, vin = 0;
      auto ldv1 = [&](int n) AI {
        const v2u t0_ = trread((ap0 ^ ((n >> 2) * 64)) + (n & 3) * 16384), t1_ = trread((ap0 ^ ((n >> 2) * 64 + 32)) + (n & 3) * 16384 + 4096);
        vf[n] = v4u{t0_[0], t0_[1], t1_[0], t1_[1]};
      };
      auto ldv = [&](int r) AI {
#pragma unroll
        for (int n = 0; n < 16; ++n) {
          ldv1(n);
          if ((n & 7) == 7) { if (r < 2) MS(); else MP(); }
        }
      };
      auto vfr = [&](int r, int n) AI {
        const int q = 4 * (n & 3);
        return (r & 1) ? v4u{vo[n >> 2][q], vo[n >> 2][q + 1], vo[n >> 2][q + 2], vo[n >> 2][q + 3]} : vf[n];
      };
      const char* base = (const char*)p.ckv + ((size_t)kvi * 256 + 16 * k) * 1024;
      unsigned xt = ((lane >> 4) << 16) | (i16 << 10) | (fswz(i16) << 4);
      unsigned ni = 0, sg = 0, ovf = 0, pc = 0, st = 0, fl = 0, cm = 0, cw = 0;
      KV_RUN_BODY();
      f32x16 oacc[4];
      v4u pf[2][4], cv4;
#define CALL0(L) "s_getpc_b64 s[92:93]\n\ts_cbranch_scc0 " L
#define RUN_TXT(L) "s_cmp_lg_u32 s76, s76\n\t" CALL0(L)
#define SIG_TXT "s_getreg_b32 s87, hwreg(HW_REG_IB_STS)\n\ts_and_b32 s87, s87, s66\n\ts_cmp_lg_u32 s87, s65\n\t" CALL0(".Lkv_sig")
#define RUN_OUT "+{s76}"(ni), "+{s77}"(sg), "={s84}"(ovf), "={s85}"(pc), "+{s86}"(st), "+{s75}"(fl), "+{s66}"(cm), "+{s65}"(cw)
#define RUN_IN "{s78}"(xm), "{s79}"(pm), "{s[80:81]}"(base), "{s82}"(L_SLOT + k * 16384), "{s83}"(k), "{v250}"(l16), "{v251}"(xt), "{s98}"(yb)
#define VO(c) c "{v[128:143]}"(vo[0]), c "{v[144:159]}"(vo[1]), c "{v[160:175]}"(vo[2]), c "{v[176:191]}"(vo[3])
#define SIG_OPS(c) : "+{s77}"(sg), "+{s66}"(cm), "+{s65}"(cw), "+{s99}"(vin), VO(c) : "{s76}"(ni), "{s83}"(k), "{s98}"(yb), "{v215}"(ap1) : RUN_CL, "vcc"
#define RUN_CL "s64", "s67", "s87", "s88", "s89", "s90", "s91", "s92", "s93", "s94", "s95", "v248", "v249", "scc", "memory"
      auto run = [&](unsigned xm, unsigned pm) AI { asm volatile(RUN_TXT(".Lkv_run") : RUN_OUT, "={v[252:255]}"(cv4) : RUN_IN : RUN_CL); };
#define PF0(c) c "{v[232:235]}"(pf[0][0]), c "{v[236:239]}"(pf[0][1]), c "{v[240:243]}"(pf[0][2]), c "{v[244:247]}"(pf[0][3]), c "{v[252:255]}"(cv4)
#define PF1(c) c "{v[216:219]}"(pf[1][0]), c "{v[220:223]}"(pf[1][1]), c "{v[224:227]}"(pf[1][2]), c "{v[228:231]}"(pf[1][3]), c "{v[252:255]}"(cv4)
#define RUNP_IN RUN_IN, "{s74}"(L_P + r * 4096), "{s68}"(r ? ANY : 0x7Fu)
      auto runp = [&](int r, unsigned pm, bool chk) AI {
        const unsigned xm = 0;
        if (chk) {
#define CHK_TXT(a, b, c, d) "s_cmp_eq_u32 s76, 64\n\ts_cselect_b32 s89, s79, 0\n\ts_mov_b32 s90, 0xffffff\n\ts_mov_b32 s64, 64\n3:\n\ts_waitcnt lgkmcnt(0)\n\t" \
                "v_readfirstlane_b32 s85, v252\n\tv_readfirstlane_b32 s86, v254\n\tv_readfirstlane_b32 s84, v253\n\tv_readfirstlane_b32 s75, v255\n\t" \
                "s_and_b32 s87, s89, s85\n\ts_getpc_b64 s[92:93]\n\ts_cbranch_scc0 1f\n\t.subsection 11\n\t" MKB "1:\n\t" \
                "s_cmp_eq_u32 s89, 0\n\ts_cbranch_scc1 .Lkv_chk\n\tv_mov_b32 v252, 0\n\tv_add_u32 v249, s74, v250\n\tds_read_b128 v[252:255], v252 offset:" STR(L_CNT_) "\n\t" \
                "ds_read_b128 " a ", v249\n\tds_read_b128 " b ", v249 offset:1024\n\tds_read_b128 " c ", v249 offset:2048\n\tds_read_b128 " d ", v249 offset:3072\n\t" \
                SIG_TXT "\n\ts_sub_u32 s90, s90, 1\n\ts_cselect_b32 s89, 0, s89\n\ts_cselect_b32 s64, -1, s64\n\ts_branch 3b\n\t.subsection 0"
          if (r & 1) asm volatile(CHK_TXT("v[216:219]", "v[220:223]", "v[224:227]", "v[228:231]") : RUN_OUT, PF1("+") : RUNP_IN : RUN_CL);
          else asm volatile(CHK_TXT("v[232:235]", "v[236:239]", "v[240:243]", "v[244:247]") : RUN_OUT, PF0("+") : RUNP_IN : RUN_CL);
        } else {
          if (r & 1) asm volatile(RUN_TXT(".Lkv_runp") : RUN_OUT, PF1("=") : RUNP_IN : RUN_CL);
          else asm volatile(RUN_TXT(".Lkv_runp") : RUN_OUT, PF0("=") : RUNP_IN : RUN_CL);
        }
      };
#define SPEC_TXT "v_mov_b32 v252, 0\n\tds_read_b128 %4, v252 offset:%6\n\tds_read_b128 %0, %5 offset:%7\n\tds_read_b128 %1, %5 offset:%7+1024\n\t" \
                 "ds_read_b128 %2, %5 offset:%7+2048\n\tds_read_b128 %3, %5 offset:%7+3072"
      auto spec = [&](int r) AI {
        if (r & 1) asm volatile(SPEC_TXT : PF1("=") : "v"(l16), "i"(L_CNT), "i"(L_P + r * 4096) : "memory");
        else asm volatile(SPEC_TXT : PF0("=") : "v"(l16), "i"(L_CNT), "i"(L_P + r * 4096) : "memory");
      };
      WARM(s, e, k, 4);
      wait_until(t0, KV_DLY);
      run(0u, ANY);
      st = 0; fl = 0;
      wg_barrier();
      MS();
      run(0xFu, ANY);
      ldv(0);
      cnt_addv(C_ST, 0x10000u);
      run(0xF0u, ANY);
      asm volatile(RUN_TXT(".Lkv_ldv") SIG_OPS("="));
      vin = 0;
      cnt_addv(C_ST, 0x100000u);
      auto sigchk = [&](bool y) AI {
        if (y) asm volatile(SIG_TXT SIG_OPS("+"));
        else asm volatile(SIG_TXT : "+{s77}"(sg), "+{s66}"(cm), "+{s65}"(cw) : "{s76}"(ni), "{s83}"(k), "{s98}"(yb) : RUN_CL);
      };
      auto getp = [&](int r, bool chk) AI {
        runp(r, 0xCu << (8 * r), chk);
        if (r && RARE(ovf & (7u << (8 * r)))) {
          runp(r, 8u << (8 * r), false);
          const float alpha = lds<float>(L_AL + r * 128 + hd * 4);
#pragma unroll
          for (int ct = 0; ct < 4; ++ct)
#pragma unroll
            for (int e = 0; e < 16; ++e) oacc[ct][e] *= alpha;
        }
        if (CVW) __builtin_amdgcn_s_waitcnt(0xC07F);
        MP();
      };
      unsigned st0 = 0;
      auto pv = [&](int r) AI {
#pragma unroll
        for (int n = 0; n < 16; ++n) {
          SB0();
          if (r == 0 && (n & 3) == 0) {
            f32x16 z;
#pragma unroll
            for (int e = 0; e < 16; ++e) z[e] = 0.f;
            oacc[n >> 2] = mfma32(vfr(r, n), pf[r & 1][n & 3], z);
          } else {
            oacc[n >> 2] = mfma32(vfr(r, n), pf[r & 1][n & 3], oacc[n >> 2]);
          }
          PIN(oacc[n >> 2]);
          SB0();
          if (r == 0 && n < 13) ldv1(n);
          if (r == 1 && n < 3) ldv1(13 + n);
          if (r == 1 && n == 4) cnt_addv(C_FL, (st0 & 0xF00u) == 0xF00u ? 0x1000u : 0u);
          if (n == SPAT) spec(r + 1);
          if (r ? (r < 2 ? (n & 3) == 3 : n % 3 == 1) : n == 15) sigchk(r == 2);
          if ((n & 3) == 2) MP();
        }
      };
      MP();
      if (G0T) { run(0u, 0x7Fu); spec(0); }
      getp(0, G0T);
      if (PPRIO) __builtin_amdgcn_s_setprio(PPRIO);
      st0 = st;
      pv(0);
      getp(1, true);
      pv(1);
      if (RARE((st0 & 0xF00u) != 0xF00u)) {
        MP();
        run(0xF00u, ANY);
        ldv(2);
        cnt_addv(C_FL, 0x1000u);
        spec(2);
      }
      MP();
      getp(2, true);
      yb = YB;
      asm volatile("s_cmp_lg_u32 s77, 4\n\t" CALL0(".Lkv_y3") SIG_OPS("="));
      pv(2);
      if (!vin) {
        yb = 0;
        if (RARE(ni != 64)) run(0xF000u, ANY);
        yb = SPIN_CAP * 16u;
        asm volatile("s_getpc_b64 s[92:93]\n\ts_cmp_lg_u32 s99, 0\n\ts_cbranch_scc1 1f\n\ts_cmp_eq_u32 s77, 4\n\ts_cbranch_scc1 .Lkv_y3\n\ts_branch .Lkv_sig\n1:" SIG_OPS("+"));
        spec(3);
      }
      yb = 0;
      {
        const unsigned sb = L_SLOT + k * 16384;
        unsigned sa = sb + hd * 272 + hh * 64;
        unsigned rx = sb + (lane >> 3) * 272 + (lane & 7) * 16;
        unsigned gx = WS_CIN + ((lane >> 3) * 4 + k) * CIN_WI + (kvi ^ (CSW ? (lane >> 3) * 16 : 0) ^ (CS2 ? (lane >> 3) * 2 + (k & 1) : 0)) * CIN_ROW + (lane & 7) * 16;
        MP();
        PIN(sa); PIN(rx); PIN(gx);
        getp(3, true);
        if (RARE(!(fl & 0x4000u))) wait_bits(C_FL, 0x4000u);
        auto dwd = [&](int d) AI { return pk2(oacc[d >> 3][2 * (d & 7)], oacc[d >> 3][2 * (d & 7) + 1]); };
        auto cvt1 = [&](int g) AI { lds<v4u>(sa + (g >> 2) * 128 + (g & 3) * 16) = v4u{san(dwd(4 * g)), dwd(4 * g + 1), dwd(4 * g + 2), dwd(4 * g + 3)}; };
        auto cvt = [&](int ct) AI { cvt1(2 * ct); cvt1(2 * ct + 1); };
        v4u ov[4];
        auto ord = [&](int hf) AI {
#pragma unroll
          for (int t = 0; t < 4; ++t) ov[t] = lds128(rx + t * 2176 + hf * 128);
        };
        auto ost = [&](int hf) AI {
#pragma unroll
          for (int t = 0; t < 4; ++t) __builtin_amdgcn_raw_buffer_store_b128(ov[t], rs, gx + hf * 128, t * 32 * CIN_WI, 16);
        };
#pragma unroll
        for (int n = 0; n < 16; ++n) {
          SB0();
          oacc[n >> 2] = mfma32(vfr(1, n), pf[1][n & 3], oacc[n >> 2]);
          SB0();
          if ((n & 3) == 1) MP();
          if (QST) {
            if (n >= 5 && n < 15 && ((n - 1) & 2) == 0) cvt1(2 * ((n - 5) >> 2) + ((n - 1) & 1));
            if (n == 10) cnt_addv(C_XS, 16u << k);
          } else {
          if (n == 5) cvt(0);
          if (n == 9) { cvt(1); ord(0); }
          if (n == OSAT) { ost(0); if (MLAT == 16) cnt_addv(C_XS, 1u << k); }
          if (n == 13) cvt(2);
          }
        }
        SB0();
        MP();
        if (MLAT == 17) cnt_addv(C_XS, 1u << k);
        cvt(3);
        ord(1);
        SB0();
        ost(1);
        if (MLAT == 18) cnt_addv(C_XS, 1u << k);
      }
    } else {
      const int k = w;
      v2u f0 = {0u, 0u}, f1 = f0, f2 = f0;
      if (k == 0 && lane < C_N) lds<unsigned>(L_CNT + lane * 4) = 0u;
      wg_barrier();
      const unsigned tag = GET_TAG();
      WARM(q, s, k, 4);
      if (QPRIO) __builtin_amdgcn_s_setprio(QPRIO);
      float nr0 = 0.f, nr1 = 0.f;
      v2f lsum = {0.f, 0.f};
      const float OVT = OVTV;
      unsigned nx = 0;
      unsigned aq0 = L_SLOT + (16 * k + i16) * 1024 + ((fswz(i16) ^ g4) * 16);
      unsigned po = L_P + k * 1024 + (i16 + 32 * (g4 >> 1)) * 16 + (g4 & 1) * 8;
      PIN(po); PIN(aq0);
      v4u kf[2][16];
      auto ldk = [&](int r) AI {
#pragma unroll
        for (int u = 0; u < 16; ++u) kf[r & 1][u] = lds128(((aq0 + (r & 1) * 65536) ^ ((u & 3) * 64)) + (u >> 2) * 256);
      };
      v4u sel;
#pragma unroll
      for (int q = 0; q < 4; ++q) sel[q] = ((g4 & 1) == (i16 >> 3) && ((i16 & 7) >> 1) == q) ? ((i16 & 1) ? 0x3F800000u : 0x3F80u) : 0u;
      unsigned qs = WS_QABS + (g4 >> 1) * 262144 + b * 32768 + k * 8192 + (lane & 31) * 16;
      unsigned qd = WS_QABS + b * 32768 + (16 + i16) * 1024 + (60 + g4) * 16;
      unsigned qw[8], qw15;
#pragma unroll
      for (int u = 0; u < 8; ++u) { qw[u] = L_Q + k * 8192 + ((i16 ^ fswz(8 * k + u)) * 16) + (g4 >> 1) * 256 + (g4 & 1) * 8; PIN(qw[u]); }
      qw15 = qw[7] - (k == 3 && g4 >= 2 && i16 >= 12 ? 4096u : 0u);
      unsigned qr = L_Q + i16 * 1024 + ((fswz(i16) ^ g4) * 16);
      PIN(sel); PIN(qs); PIN(qd); PIN(qw15); PIN(qr);
      unsigned mxw = L_Q + (16 + (hd >> 2)) * 1024 + 768 + ((3 ^ ((hd >> 2) & 3)) * 64) + (hd & 3) * 16 + k * 4;
      unsigned mxr = L_Q + (16 + (i16 >> 2)) * 1024 + 768 + ((3 ^ (i16 >> 2)) * 64) + (i16 & 3) * 16;
      PIN(mxw); PIN(mxr);
      wait_bits(C_ST, 1u << k);
      ldk(0);
      cnt_addv(C_ST, 1u << (24 + k));
      MQ();
      if (K1AT == 0) {
        wait_bits(C_ST, 16u << k);
        ldk(1);
        cnt_addv(C_ST, 1u << (28 + k));
        MQ();
      }
      v4u qf[32], qv[16], da, db;
      const unsigned char* wsp = p.ws;
      const unsigned char* wsp2 = p.ws + 4096;
#define QLD(t) asm volatile("global_load_dwordx4 %0, %1, %2 offset:%3 sc1" : "=v"(qv[t]) : "v"(qs), "s"((t) < 8 ? wsp : wsp2), "i"(((t) & 7) * 512) : "memory")
      if (k == 0) {
        wait_until(t0, POLL_START);
        const unsigned fo = WS_FLAG + kvi * 1024 + fpos<FLW>(lane);
        const v2u zz = {0u, 0u};
        u64 seen = 0;
        bool sent = false;
        auto chk = [&](v2u& f) AI {
          asm volatile("s_waitcnt vmcnt(2)" : "+v"(f)::"memory");
          const u64 ok = __ballot(((f[0] ^ tag) | (f[1] ^ tag)) == 0) | seen;
          if (ok != seen) {
            seen = ok;
            lds<v2u>(L_CNT + C_MASK * 4) = v2u{(unsigned)ok, (unsigned)(ok >> 32)};
            if (PAUSE && !sent && __popcll(~ok) <= PK) { cnt_addv(C_FL, 0x100u); sent = true; }
          }
          return ok == ~0ull;
        };
        MQ();
        PLD(f0); __builtin_amdgcn_s_sleep(POLL_GAP);
        PLD(f1); __builtin_amdgcn_s_sleep(POLL_GAP);
        PLD(f2);
#pragma unroll 1
        for (unsigned spin = 0; spin < SPIN_CAP; ++spin) {
          if (chk(f0)) break;
          if (QPM) PLDM(f0); else PLD(f0);
          MQ();
          if (chk(f1)) break;
          if (QPM) PLDM(f1); else PLD(f1);
          MQ();
          if (chk(f2)) break;
          if (QPM) PLDM(f2); else PLD(f2);
        }
        MQ();
#pragma unroll
        for (int t = 0; t < 16; ++t) QLD(t);
      } else {
        unsigned pend = 0xFFFFu;
#pragma unroll 1
        for (unsigned spin = 0; spin < SPIN_CAP * 16u; ++spin) {
          v2u mk;
          asm volatile("ds_read_b64 %0, %1 offset:%2\n\ts_waitcnt lgkmcnt(0)" : "=v"(mk) : "v"(C_MASK * 4u), "i"(L_CNT) : "memory");
          const unsigned m0 = __builtin_amdgcn_readfirstlane(mk[0]), m1 = __builtin_amdgcn_readfirstlane(mk[1]);
          const unsigned nw = ((k < 2 ? m0 : m1) >> (16 * (k & 1))) & pend;
          if (PROG ? nw != 0u : (m0 & m1) == ~0u) {
            pend &= ~nw;
#pragma unroll
            for (int t = 0; t < 16; ++t)
              if (nw >> t & 1u) { QLD(t); if ((t & 7) == 0) MQ(); }
          }
          if ((m0 & m1) == ~0u) break;
          __builtin_amdgcn_s_sleep(1);
        }
      }
#define QLD2() asm volatile("global_load_dwordx4 %0, %2, %3 sc1\n\tglobal_load_dwordx4 %1, %2, %4 sc1" : "=&v"(da), "=&v"(db) : "v"(qd), "s"(wsp), "s"(wsp + 262144) : "memory")
      QLD2();
      cnt_add(C_FL);
      MQ();
      if (K1AT == 1) {
        wait_bits(C_ST, 16u << k);
        ldk(1);
        cnt_addv(C_ST, 1u << (28 + k));
      }
#pragma unroll 1
      for (unsigned spin = 0; spin < SPIN_CAP; ++spin) {
        if (spin) {
#pragma unroll
          for (int t = 0; t < 16; ++t) QLD(t);
          QLD2();
        }
        MQ();
        asm volatile("s_waitcnt vmcnt(2)" : W8(qv, 0), W8(qv, 8), "+v"(f0), "+v"(f1), "+v"(f2)::"memory");
        float chk = 0.f;
        f32x4 d[16];
#pragma unroll
        for (int t = 0; t < 18; ++t) {
          if (t < 16) d[t] = mfma16(sel, qv[t], f32x4{0.f, 0.f, 0.f, 0.f});
          if (t >= 2) {
            const int u = t - 2;
            lds<v2u>((u == 15 ? qw15 : qw[u >> 1]) + (u >> 1) * 1024 + (u & 1) * 512) = v2u{pk2(d[u][0], d[u][1]), pk2(d[u][2], d[u][3])};
            chk += d[u][0];
          }
          SB0();
          if ((t & 3) == 1) MQ();
        }
        asm volatile("s_waitcnt vmcnt(0)" : "+v"(da), "+v"(db)::"memory");
        if (!__any(chk != chk || max(da[0], db[0]) == POISON)) break;
        unsigned mx = max(da[0], db[0]);
#pragma unroll
        for (int t = 0; t < 16; ++t) mx = max(mx, qv[t][0]);
        if (!__any(mx == POISON)) break;
        CFENCE();
      }
      MQ();
      cnt_addv(C_FL, 16u);
#pragma unroll
      for (int e = 0; e < 4; ++e) qf[31][e] = pk2(bf2f(da[e]) + bf2f(db[e]), bfhi(da[e]) + bfhi(db[e]));
      PIN(qf[31]);
      MQ();
      wait_bits(C_FL, 0x40u);
      MQ();
      f32x4 s0 = {0.f, 0.f, 0.f, 0.f}, s1 = s0, a0 = s0, a1 = s0, t0, t1;
      auto ldq = [&](int f) AI { qf[(f >> 1) + 16 * (f & 1)] = lds128((qr ^ (((f >> 1) & 3) * 64)) + (f >> 3) * 256 + (f & 1) * 16384); };
      v4u kn[2][16], cs;
      auto ldn = [&](int r, int u) AI { kn[r & 1][u] = lds128(((aq0 + (r & 1) * 65536) ^ ((u & 3) * 64)) + (u >> 2) * 256); };
#define CRD() asm volatile("ds_read_b128 %0, %1 offset:%2" : "=v"(cs) : "v"(0u), "i"(L_CNT) : "memory")
#define CWT() asm volatile("s_waitcnt lgkmcnt(0)" : "+v"(cs)::"memory")
#pragma unroll
      for (int f = 0; f < 12; ++f) ldq(f);
#pragma unroll
      for (int u = 0; u < 16; ++u) {
        SB0();
        s0 = mfma16(kf[0][u], qf[u], s0);
        s1 = mfma16(kf[0][u], qf[16 + u], s1);
        if (MIX && (u & 1)) a1 = mfma16(kf[1][u >> 1], qf[16 + (u >> 1)], a1);
        else if (MIX) a0 = mfma16(kf[1][u >> 1], qf[u >> 1], a0);
        SB0();
        if ((u & 3) == 0) MQ();
        if (12 + 2 * u < 31) ldq(12 + 2 * u);
        if (13 + 2 * u < 31) ldq(13 + 2 * u);
      }
      MQ();
      unsigned cx;
      f32x4 ma, mb;
      auto max4 = [&](const f32x4& v) AI { return fmaxf(fmaxf(v[0], v[1]), fmaxf(v[2], v[3])); };
      auto post_max = [&](bool first) AI {
        const float mw = max32(rs_max16(max4(s0), max4(s1)));
        lds<float>(mxw) = mw;
        if (first) cnt_addv(C_P, 0x10u);
        else { cnt_add(C_MX); nx += 4; }
      };
#define XRD(c, w) asm volatile("ds_read_b32 %0, %3 offset:%5\n\tds_read_b128 %1, %4\n\tds_read_b128 %2, %4 offset:4096" w : "=&v"(cx), "=&v"(ma), "=&v"(mb) : "v"(0u), "v"(mxr), "i"(L_CNT + (c) * 4) : "memory")
      auto exchange = [&](int r) AI {
        asm volatile("s_waitcnt lgkmcnt(0)" : "+v"(cx), "+v"(ma), "+v"(mb)::"memory");
        if (RARE(r ? __builtin_amdgcn_readfirstlane(cx) < nx : !(__builtin_amdgcn_readfirstlane(cx) & 0x40u))) {
#pragma unroll 1
          for (unsigned sp = 0; sp < SPIN_CAP * 16u; ++sp) {
            if (r == 0) MQ();
            XRD(r ? C_MX : C_P, "\n\ts_waitcnt lgkmcnt(0)");
            if (r ? __builtin_amdgcn_readfirstlane(cx) >= nx : (__builtin_amdgcn_readfirstlane(cx) & 0x40u) != 0) break;
          }
        }
        float d0 = max4(ma), d1 = max4(mb);
        if (r) {
          d0 = fmaxf(d0, 0.f); d1 = fmaxf(d1, 0.f);
          const float al0 = ex2(-d0), al1 = ex2(-d1);
          lsum[0] *= al0; lsum[1] *= al1;
          lds<float>(L_AL + r * 128 + hd * 4) = (lane & 16) ? al1 : al0;
        }
#pragma unroll
        for (int e = 0; e < 4; ++e) { s0[e] -= d0; s1[e] -= d1; }
        nr0 -= d0; nr1 -= d1;
      };
      v2f ln;
      auto do_p = [&](int r, const f32x4& x0, const f32x4& x1, unsigned ov) AI {
        float e0[4], e1[4];
#pragma unroll
        for (int e = 0; e < 4; ++e) { e0[e] = ex2(x0[e]); e1[e] = ex2(x1[e]); }
        lds<v2u>(po + r * 4096) = v2u{pk2(e0[0], e0[1]), pk2(e0[2], e0[3])};
        lds<v2u>(po + r * 4096 + 256) = v2u{pk2(e1[0], e1[1]), pk2(e1[2], e1[3])};
        cnt_add2(C_P, 1u << (8 * r), ov);
        ln = lsum + ((v2f{e0[0], e1[0]} + v2f{e0[1], e1[1]}) + (v2f{e0[2], e1[2]} + v2f{e0[3], e1[3]}));
      };
      auto redo = [&](int r) AI {
        post_max(false);
        XRD(C_MX, "");
        exchange(r);
        do_p(r, s0, s1, 0u);
      };
      auto fin = [&](int r, const f32x4& x0, const f32x4& x1) AI {
        MQ();
#pragma unroll
        for (int e = 0; e < 4; ++e) { s0[e] = x0[e] + nr0; s1[e] = x1[e] + nr1; }
        const unsigned ov = __any(fmaxf(max4(s0), max4(s1)) > OVT) ? 1u << (8 * r) : 0u;
        do_p(r, s0, s1, ov);
      };
#pragma unroll
      for (int u = 8 * MIX; u < 16; ++u) {
        SB0();
        a0 = mfma16(kf[1][u], qf[u], a0);
        a1 = mfma16(kf[1][u], qf[16 + u], a1);
        PIN(a0); PIN(a1);
        SB0();
        if (u <= 8 * MIX + XAT + 4 || u >= 13) MQ();
        if (u == 8 * MIX) post_max(true);
        if (u == 8 * MIX + XAT) XRD(C_P, "");
        if (u == 8 * MIX + 2 + XAT) exchange(0);
        if (u == 8 * MIX + 3 + XAT) {
          do_p(0, s0, s1, 0u);
          lsum = ln;
        }
        if (KSP == 0 && u == 13) {
          CRD();
#pragma unroll
          for (int v = 0; v < 16; ++v) ldn(2, v);
        }
        if (KSP == 1) { if (u == 0) CRD(); ldn(2, u); }
        if (KSP == 2 && u >= XAT + 3) {
          if (u == XAT + 3) CRD();
#pragma unroll
          for (int v = 16 * (u - XAT - 3) / (13 - XAT); v < 16 * (u - XAT - 2) / (13 - XAT); ++v) ldn(2, v);
        }
      }
      fin(1, a0, a1);
      asm volatile("s_waitcnt lgkmcnt(14)" : "+v"(cs)::"memory");
      auto refchk = [&](int r) AI {
        CWT();
        unsigned ov = __builtin_amdgcn_readfirstlane(cs[1]);
        if (RARE(!(__builtin_amdgcn_readfirstlane(cs[0]) >> (8 * r) & 12u))) { MQ(); ov = wait_p(12u << (8 * r), 0u); }
        if (RARE(ov & (7u << (8 * r)))) redo(r);
        lsum = ln;
        MQ();
      };
      MQ();
      if (RARE(!(__builtin_amdgcn_readfirstlane(cs[2]) >> (8 + k) & 1u))) {
        MQ();
        wait_bits(C_ST, 0x100u << k);
#pragma unroll
        for (int u = 0; u < 16; ++u) ldn(2, u);
        __builtin_amdgcn_s_waitcnt(0xC07F);
      }
      MQ();
      t0 = f32x4{0.f, 0.f, 0.f, 0.f}; t1 = t0;
      unsigned ak[4];
#pragma unroll
      for (int u = 0; u < 16; ++u) {
        SB0();
        t0 = mfma16(kn[0][u], qf[u], t0);
        t1 = mfma16(kn[0][u], qf[16 + u], t1);
        PIN(t0); PIN(t1);
        SB0();
        if ((u & 7) == 0) MQ();
        if (u < 4) { ak[u] = (aq0 + 65536) ^ (u * 64); PIN(ak[u]); }
        if (u == CKAT - 4) CRD();
        if (u == CKAT) refchk(1);
      }
      fin(2, t0, t1);
      CRD();
      t0 = f32x4{nr0, nr0, nr0, nr0}; t1 = f32x4{nr1, nr1, nr1, nr1};
      PIN(t0); PIN(t1); PIN(ln);
      MQ();
      unsigned bad;
      {
        unsigned cap = SPIN_CAP * 16u, tv;
        asm volatile("s_branch 2f\n1:\n\tds_read_b128 v[200:203], %3 offset:%4\n\ts_sub_u32 %2, %2, 1\n\ts_cselect_b64 exec, 0, exec\n2:\n\ts_waitcnt lgkmcnt(0)\n\t"
                     "v_and_b32 %1, %5, v202\n\tv_cmp_eq_u32 vcc, 0, %1\n\ts_cbranch_vccnz 1b\n\ts_mov_b64 exec, -1"
                     : "+{v[200:203]}"(cs), "=&v"(tv), "+s"(cap) : "v"(0u), "i"(L_CNT), "s"(0x1000u << k) : "memory", "vcc", "scc");
      }
#pragma unroll
      for (int u = 0; u < 16; ++u) {
        SB0();
        kn[1][u] = lds128(ak[u & 3] + (u >> 2) * 256);
        if (u == 7) MQ();
        if (u == 9) {
          asm volatile("" : "+v"(cs)::"memory");
          bad = __builtin_amdgcn_readfirstlane((cs[1] & 0x70000u) | ((cs[0] & 0xC0000u) == 0u ? 1u : 0u));
          asm volatile("" : "+s"(bad));
        }
      }
      SB0();
      if (RARE(bad)) {
        MQ();
        if (RARE(wait_p(0xC0000u, 0u) & 0x70000u)) {
          redo(2);
          t0 = f32x4{nr0, nr0, nr0, nr0}; t1 = f32x4{nr1, nr1, nr1, nr1};
        }
      }
      lsum = ln;
#pragma unroll
      for (int n = 0; n < 16 + QL; ++n) {
        SB0();
        if (n < 16) t0 = mfma16(kn[1][n], qf[n], t0);
        if (n >= QL) t1 = mfma16(kn[1][n - QL], qf[16 + n - QL], t1);
        SB0();
        if ((n & 7) == 0) MQ();
      }
      s0 = t0; s1 = t1;
      do_p(3, s0, s1, __any(fmaxf(max4(s0), max4(s1)) > OVT) ? 1u << 24 : 0u);
      MQ();
      if (RARE(wait_p(0xC000000u, 0u) & 0x7000000u)) redo(3);
      lsum = ln;
      {
        MQ();
        const float lt = add32(rs_add16(lsum[0], lsum[1]));
        lds<float>(L_LS + k * 128 + hd * 4) = lt;
        cnt_add(C_MX);
        nx += 4;
        cnt_wait(C_MX, nx);
        const LAS float* ls = (const LAS float*)(size_t)(L_LS + hd * 4);
        v4u mlv = {__float_as_uint((lane & 16) ? -nr1 : -nr0), tag, __float_as_uint((ls[0] + ls[32]) + (ls[64] + ls[96])), tag};
        unsigned gm = WS_ML + ((hd * 4 + k) * 128 + (kvi ^ (MS2 ? (hd & 7) * 16 : 0))) * 16;
        PIN(mlv); PIN(gm);
        MQ();
        if (QST) {
          unsigned rx = L_SLOT + k * 16384 + (lane >> 3) * 272 + (lane & 7) * 16;
          unsigned gx = WS_CIN + ((lane >> 3) * 4 + k) * CIN_WI + (kvi ^ (CSW ? (lane >> 3) * 16 : 0)) * CIN_ROW + (lane & 7) * 16;
          PIN(rx); PIN(gx);
          MQ();
          wait_bits(C_XS, 16u << k);
          v4u ov[4];
#pragma unroll
          for (int t = 0; t < 4; ++t) ov[t] = lds128(rx + t * 2176);
#pragma unroll
          for (int t = 0; t < 4; ++t) __builtin_amdgcn_raw_buffer_store_b128(ov[t], rs, gx, t * 32 * CIN_WI, 16);
          MQ();
        }
        wait_bits(C_XS, 1u << k);
        if (lane < 32) st_sc1(rs, gm, mlv);
      }
    }
  } else {
    const int h = 4 * xcd + (idx & 3), j = idx >> 2, wi = 4 * h + j;
    const bool ffn = wi < 86;
    const unsigned hh = lane >> 5;
    v4u xa, xb;
    asm volatile("s_cmp_gt_u32 %3, 3\n\ts_cbranch_scc1 1f\n\tglobal_load_dwordx4 %0, %2, %4\n\tglobal_load_dwordx4 %1, %2, %4 offset:512\n1:"
                 : "=&v"(xa), "=&v"(xb) : "v"((2 * w + hh) * 1024 + (lane & 31) * 16), "s"(w), "s"(p.x) : "memory", "scc");
    constexpr int NT = FR4 ? 2 : 4;
    const int hf = FMAP ? (idx & 7) + 8 * (xcd & 3) : h, jf = FMAP ? (idx >> 3) + 2 * (xcd >> 2) : j;
    const int fr = FR4 ? 1 : __builtin_amdgcn_readfirstlane((jf >> 1) ^ 1);
    const int dj = jf & 1, c0 = (FR4 ? (jf >> 1) * 256 : 0) + w * 16 * NT;
    unsigned qb = WS_QABS + dj * 262144 + (i16 * 32 + hf) * 1024 + (c0 + 8 * g4) * 2;
    auto ffn_pf = [&](unsigned& d) {
      const int u = idx * 8 + w, q5 = lane / 5, r5 = lane - 5 * q5;
      const char* pb = (const char*)((q5 & 1) ? p.wu : p.wg) + (4 * u + (min(q5, 7) >> 1)) * (FF * 2) + min(xcd * 512 + min(r5 * 128, 508), FF * 2 - 4);
      asm volatile("global_load_dword %0, %1, off" : "=&v"(d) : "v"(pb) : "memory");
    };
    unsigned dpf = 0;
    SB0();
    if (WPF) {
      unsigned d0, d1;
      if (WPF == 1) {
        const unsigned po = (32 * idx + 4 * w + g4) * 8192 + xcd * 1024 + i16 * 64;
        asm volatile("global_load_dword %0, %4, %5\n\tglobal_load_dword %1, %4, %6\n\ts_waitcnt vmcnt(0)" : "=&v"(d0), "=&v"(d1), "+v"(xa), "+v"(xb) : "v"(po), "s"(p.wq), "s"(p.wuk) : "memory");
      } else {
        const char* pa = (const char*)(hh ? p.wuk : p.wq) + (32 * idx + 4 * w + ((lane >> 3) & 3)) * 8192 + xcd * 1024 + (lane & 7) * 128;
        asm volatile("global_load_dword %0, %1, off" : "=&v"(d0) : "v"(pa) : "memory");
        if (PZT) asm volatile("global_load_dword %0, %1, %2" : "=&v"(dpf) : "v"(WS_CIN + wi * CIN_WI + w * 4096), "s"(p.ws) : "memory");
        if (PZE == 1) {
#pragma unroll
          for (int t = 0; t < 4; ++t) st_sc1(rs, WS_CIN + wi * CIN_WI + w * 4096 + t * 1024 + lane * 16, v4u{POISON, POISON, POISON, POISON});
          CFENCE();
        }
        if (w >= 4) {
          wait_until(t0, WM_DLY);
          WARM(w, q, w - 4, 4);
          if (PZE == 3) VMWAIT(0);
        } else if (PZE == 3) {
          wait_until(t0, PZ_AT);
#pragma unroll
          for (int t = 0; t < 8; ++t) st_sc1(rs, WS_CIN + wi * CIN_WI + w * 8192 + t * 1024 + lane * 16, v4u{POISON, POISON, POISON, POISON});
          CFENCE();
        }
        asm volatile("s_waitcnt vmcnt(%4)" : "+v"(d0), "+v"(xa), "+v"(xb), "+v"(dpf) : "i"(PZE == 1 ? 4 : PZE == 3 ? 8 : 0) : "memory");
      }
      if (WPF_DLY) __builtin_amdgcn_s_sleep(WPF_DLY);
    }
    __builtin_amdgcn_s_barrier();
    MW();
    v4u wukf[8];
    if (fr) {
      const char* src = (const char*)p.wq + hf * 256 + dj * 128 + (size_t)(64 * w) * 8192;
#pragma unroll
      for (int t = 0; t < 8; ++t) {
        glds16s((lane >> 3) * 8192 + (((lane & 7) ^ (((g4 & 1) | ((t & 1) << 1)) << 1)) * 16), src + t * 65536, L_WQ + (64 * w + 8 * t) * 128);
        if (t == 3) MW();
      }
      MW();
    }
    {
      const unsigned vk = (8 * (i16 >> 2) + (lane & 3)) * 8192 + g4 * 16;
      const char* kb = (const char*)p.wuk + hf * 256 + dj * 128 + (size_t)c0 * 8192;
#pragma unroll
      for (int u = 0; u < NT / 2; ++u)
        asm volatile("s_cmp_eq_u32 %5, 0\n\ts_cbranch_scc1 1f\n\t"
                     "global_load_dwordx4 %0, %4, %6\n\tglobal_load_dwordx4 %1, %4, %6 offset:64\n\t"
                     "global_load_dwordx4 %2, %4, %7\n\tglobal_load_dwordx4 %3, %4, %7 offset:64\n1:"
                     : "=&v"(wukf[4 * u]), "=&v"(wukf[4 * u + 1]), "=&v"(wukf[4 * u + 2]), "=&v"(wukf[4 * u + 3])
                     : "v"(vk), "s"(fr), "s"(kb + 32 * u * 8192), "s"(kb + (32 * u + 4) * 8192) : "memory", "scc");
    }
    {
      MW();
      const v4u pz = {POISON, POISON, POISON, POISON};
      if (fr && i16 < 8) {
#pragma unroll
        for (int u = 0; u < NT / 2; ++u) st_sc1(rs, qb + u * 64, pz);
      }
      if (PZE == 2) {
        if (w >= 4) {
          if (PZ_DLY) __builtin_amdgcn_s_sleep(PZ_DLY);
          MW();
#pragma unroll
          for (int t = 0; t < 2; ++t) st_sc1(rs, WS_EIN + wi * 8192 + (w - 4) * 2048 + t * 1024 + lane * 16, pz);
#pragma unroll
          for (int t = 0; t < 8; ++t) st_sc1(rs, WS_CIN + wi * CIN_WI + (w - 4) * 8192 + t * 1024 + lane * 16, pz);
          VMWAIT(2 * NT + NT / 2 + 10);
        }
      } else {
      st_sc1(rs, WS_EIN + wi * 8192 + w * 1024 + lane * 16, pz);
      if (!PZE) {
#pragma unroll
        for (int t = 0; t < 4; ++t) st_sc1(rs, WS_CIN + wi * CIN_WI + w * 4096 + t * 1024 + lane * 16, pz);
      }
      }
    }
    if (w < 4) {
      asm volatile("s_waitcnt vmcnt(%3)\n\ts_cmp_lg_u32 %2, 0\n\ts_cbranch_scc1 1f\n\ts_waitcnt vmcnt(1)\n1:" : "+v"(xa), "+v"(xb) : "s"(fr), "i"(8 + 2 * NT + NT / 2 + 1 + 8) : "memory", "scc");
      MW();
      v2f v[8];
#pragma unroll
      for (int e = 0; e < 4; ++e) { v[e] = v2f{bf2f(xa[e]), bfhi(xa[e])}; v[4 + e] = v2f{bf2f(xb[e]), bfhi(xb[e])}; }
      const unsigned p0 = __builtin_amdgcn_readlane(__float_as_uint(v[0][0]), 0), p1 = __builtin_amdgcn_readlane(__float_as_uint(v[0][0]), 32);
      const float piv = __uint_as_float(hh ? p1 : p0);
      v2f sm = {0.f, 0.f}, ss = {0.f, 0.f};
#pragma unroll
      for (int e = 0; e < 8; ++e) {
        if (e == 3) { SB0(); MW(); }
        const v2f d = v[e] - v2f{piv, piv}; sm += d; ss += d * d;
      }
      SB0();
      MW();
      float t = rowsum(rs_add16(sm[0] + sm[1], ss[0] + ss[1]));
      auto sw2 = __builtin_amdgcn_permlane16_swap(__float_as_uint(t), __float_as_uint(t), false, false);
      const float md = __uint_as_float(sw2[0]) * (1.f / DM);
      const float var = fmaxf(fmaf(-md, md, __uint_as_float(sw2[1]) * (1.f / DM)), 0.f);
      const float mb = rbf(piv + md), vb = rbf(var);
      const float rden = __builtin_amdgcn_rcpf(rbf(__builtin_amdgcn_sqrtf(rbf(vb + 1e-5f))));
      SB0();
      MW();
      v4u o4[2];
#pragma unroll
      for (int e = 0; e < 8; ++e) {
        if (e == 3 || e == 6) { SB0(); MW(); }
        const v2f d = v[e] - v2f{mb, mb};
        const unsigned u = pk2(d[0], d[1]);
        const v2f o = v2f{bf2f(u), bfhi(u)} * v2f{rden, rden};
        o4[e >> 2][e & 3] = pk2(o[0], o[1]);
      }
      const unsigned xo = L_XN + (2 * w + hh) * 1040 + (lane & 31) * 16;
      lds<v4u>(xo) = o4[0];
      lds<v4u>(xo + 512) = o4[1];
      if (tid < C_N) lds<unsigned>(L_CNT + tid * 4) = 0u;
      if (PZE == 2) VMWAIT(2 * NT + NT / 2);
    }
    MW();
    const unsigned tag = GET_TAG();
    unsigned qa = L_QH + (lane & 7) * 144 + g4 * 16;
    {
      unsigned ta = L_WQ + (8 * g4 + (i16 >> 2)) * 128 + (((2 * w + ((lane & 3) >> 1)) ^ (((i16 >> 3) | ((g4 & 1) << 1)) << 1)) * 16) + 8 * (lane & 1);
      unsigned xq = L_XN + (lane & 7) * 1040 + g4 * 16;
      unsigned qw = L_QH + (lane & 7) * 144 + (16 * w + 4 * g4) * 2;
      PIN(ta); PIN(xq); PIN(qw); PIN(qa); PIN(qb);
      if (PZE != 2) VMWAIT(2 * NT + NT / 2 + 1 + (PZE ? 0 : 4));
      wg_barrier();
      MW();
      if (fr && w < 4) {
        f32x4 c0a = {0.f, 0.f, 0.f, 0.f}, c1a = c0a;
#pragma unroll
        for (int ks = 0; ks < 16; ks += 2) {
          if (ks == 4 || ks == 10) MW();
          const v4u x0 = lds128(xq + ks * 64), x1 = lds128(xq + ks * 64 + 64);
          const v2u a0 = trread(ta + ks * 4096), a1 = trread(ta + ks * 4096 + 512), b0 = trread(ta + ks * 4096 + 4096), b1 = trread(ta + ks * 4096 + 4608);
          c0a = mfma16(v4u{a0[0], a0[1], a1[0], a1[1]}, x0, c0a);
          c1a = mfma16(v4u{b0[0], b0[1], b1[0], b1[1]}, x1, c1a);
        }
        MW();
        c0a += c1a;
        lds<v2u>(qw) = v2u{pk2(c0a[0], c0a[1]), pk2(c0a[2], c0a[3])};
      }
    }
    MW();
    wg_barrier();
    {
      const v4u q0 = lds128(qa), q1 = lds128(qa + 64);
      if (NT == 4) asm volatile("s_waitcnt vmcnt(0)" : W8(wukf, 0)::"memory");
      else asm volatile("s_waitcnt vmcnt(0)" : "+v"(wukf[0]), "+v"(wukf[1]), "+v"(wukf[2]), "+v"(wukf[3])::"memory");
      const bool last = cnt_add_rtn(C_QST) == 7u;
      if (FLAG_FIRST && last) send_flags(rs, wi, lane, tag);
      if (fr) {
        const float sc = 0.08838834764831845f * LOG2E;
#pragma unroll
        for (int u = 0; u < NT / 2; ++u) {
          f32x4 d0 = mfma16(wukf[4 * u], q0, f32x4{0.f, 0.f, 0.f, 0.f}), d1 = mfma16(wukf[4 * u + 2], q0, f32x4{0.f, 0.f, 0.f, 0.f});
          d0 = mfma16(wukf[4 * u + 1], q1, d0);
          d1 = mfma16(wukf[4 * u + 3], q1, d1);
          d0 *= sc; d1 *= sc;
          if (i16 < 8) st_sc1(rs, qb + u * 64, v4u{san(pk2(d0[0], d0[1])), pk2(d0[2], d0[3]), pk2(d1[0], d1[1]), pk2(d1[2], d1[3])});
        }
      }
      MW();
      if (!FLAG_FIRST && last) send_flags(rs, wi, lane, tag);
      if (last) cnt_add(C_SENT);
      if (KPF) {
        if (KPF_AT) wait_until(t0, KPF_AT + (w ^ 4) * KPF_ST);
        if (KPF_S) cnt_wait<1>(C_SENT, 1);
        if (KPF == 1) {
          const unsigned ko = (unsigned)(xcd * 16 + idx) * 262144u + 196608u + w * 8192 + lane * 128;
          asm volatile("global_load_dword %0, %1, %2" : "=&v"(dpf) : "v"(ko), "s"(p.ckv) : "memory");
        } else {
          const unsigned ko = min((unsigned)lane * 131072u + (w & 1) * 65536u + idx * 4096u, WS_END - 4u);
          asm volatile("s_cmp_gt_u32 %3, %4\n\ts_cbranch_scc1 1f\n\tglobal_load_dword %0, %1, %2 sc1\n1:" : "=&v"(dpf) : "v"(ko), "s"(p.ws), "s"(w), "i"(KPF - 3) : "memory", "scc");
        }
      }
      if (FPF == 3 && xcd < 6) {
        cnt_wait<1>(C_SENT, 1);
        ffn_pf(dpf);
        asm volatile("s_waitcnt vmcnt(0)" : "+v"(dpf)::"memory");
      }
    }
    if (w == 4 && (T_O == 0 || T_F == 0 || T_UV == 1)) {
      v2u f0;
      MW();
      wait_until(t0, POLL_START);
      poll_flags(p.ws, WS_FLAG + (128 + wi) * 1024 + fpos<FLW>(lane), tag, f0);
      MW();
      if (GO_DLY) wait_until(now32(), GO_DLY);
      cnt_add(C_GO);
      asm volatile("s_waitcnt vmcnt(0)" : "+v"(f0)::"memory");
    }
    MW();
    v4u wof[4][8];
    f32x4 oc[8];
    if (w >= 4) {
      const int hk = w - 4;
      if (T_O) { wait_until(t0, T_O); cnt_wait<1>(C_SENT, 1); } else cnt_wait<2>(C_GO, 1);
      v4u wor[4][8], wdr[8], wdf[8];
      {
        const unsigned vo = 8 * g4 * 1024 + i16 * 16, vd = 8 * (g4 & 1) * 1024 + i16 * 16;
        const char* so = (const char*)p.wo + (size_t)(h * 128) * 1024 + hk * 256;
        const char* sd = (const char*)p.wd + (size_t)(ffn ? 16 * wi : 0) * 1024 + hk * 256;
#pragma unroll
        for (int n = 0; n < 40; ++n) {
          if (n < 8) asm volatile("global_load_dwordx4 %0, %1, %2 nt" : "=v"(wdr[n]) : "v"(vd), "s"(sd + n * 1024) : "memory");
          else asm volatile("global_load_dwordx4 %0, %1, %2" : "=v"(wor[(n - 8) >> 3][n & 7]) : "v"(vo), "s"(so + (32 * ((((n - 8) >> 3) + j) & 3) + (n & 7)) * 1024) : "memory");
          if (n >= NFW - 1 && n < 39) VMWAIT(NFW - 1);
          if ((n & 7) == 0) MW();
        }
      }
      asm volatile("s_waitcnt vmcnt(32)" : W8(wdr, 0), "+v"(dpf)::"memory");
      transpose8(wdr, wdf);
      asm volatile("s_waitcnt vmcnt(24)" : W8(wor[0], 0)::"memory");
      transpose8(wor[0], wof[0]);
      asm volatile("s_waitcnt vmcnt(16)" : W8(wor[1], 0)::"memory");
      transpose8(wor[1], wof[1]);
      asm volatile("s_waitcnt vmcnt(8)" : W8(wor[2], 0)::"memory");
      transpose8(wor[2], wof[2]);
      asm volatile("s_waitcnt vmcnt(0)" : W8(wor[3], 0)::"memory");
      transpose8(wor[3], wof[3]);
#pragma unroll
      for (int jj = 0; jj < 8; ++jj) oc[jj] = f32x4{0.f, 0.f, 0.f, 0.f};
      if (ffn) {
        MW();
        cnt_wait<1>(C_FFN, 1);
        const v4u a = lds128(L_HF + ((lane & 7) ^ ((lane & 8) >> 2)) * 80 + g4 * 16);
#pragma unroll
        for (int jj = 0; jj < 8; ++jj) oc[jj] = mfma16(a, wdf[jj], oc[jj]);
      }
#pragma unroll
      for (int u = 0; u < 4; ++u) asm volatile("" : W8(wof[u], 0));
      asm volatile("" : W8(oc, 0));
    } else {
      MW();
      if (T_F) { wait_until(t0, T_F); cnt_wait<1>(C_SENT, 1); } else cnt_wait<2>(C_GO, 1);
      if (h < 22) {
        const char* src = (const char*)(((i16 >> 3) ^ (g4 >> 1)) ? p.wu : p.wg) + (size_t)(128 * j + 32 * w + g4) * (FF * 2) + h * 128;
#pragma unroll
        for (int t = 0; t < 8; ++t) {
          glds16<0>(src + (size_t)t * 4 * (FF * 2) + min((i16 ^ fswz(4 * t + g4)) & 7u, h == 21 ? 3u : 7u) * 16, L_GU + (32 * w + 4 * t) * 256);
          if (t & 1) MW();
        }
      }
      auto ld_uv = [&]() {
#pragma unroll
        for (int t = 0; t < 8; ++t) {
          const unsigned row = 32 * w + 4 * t + g4;
          if ((t & 1) == 0) MW();
          glds16<1>((const char*)p.wuv + (size_t)(128 * j + row) * 8192 + h * 256 + ((i16 ^ fswz(row)) * 16), L_WUV + (32 * w + 4 * t) * 256);
        }
      };
      if (!T_UV) ld_uv();
      if (h < 22) {
        if (T_UV) VMWAIT(0); else VMWAIT(8);
        MW();
        cnt_add(C_GU);
        cnt_wait(C_GU, 4);
        f32x4 ag = {0.f, 0.f, 0.f, 0.f}, au = {0.f, 0.f, 0.f, 0.f};
        const unsigned ra = 8 * g4 + (i16 >> 2), ch = 2 * w + ((lane & 3) >> 1);
        const unsigned r0 = L_GU + ra * 256 + ((ch ^ fswz(ra)) * 16) + 8 * (lane & 1), r1 = L_GU + (ra + 4) * 256 + ((ch ^ fswz(ra + 4)) * 16) + 8 * (lane & 1);
        MW();
#pragma unroll
        for (int ks = 0; ks < 4; ++ks) {
          const v4u a = lds128(L_XN + (lane & 7) * 1040 + (128 * j + 32 * ks + 8 * g4) * 2);
          const v2u b0 = trread(r0 + ks * 8192), b1 = trread(r1 + ks * 8192);
          const v2u c0 = trread((r0 ^ 128) + ks * 8192), c1 = trread((r1 ^ 128) + ks * 8192);
          if (ks == 2) MW();
          ag = mfma16(a, v4u{b0[0], b0[1], b1[0], b1[1]}, ag);
          au = mfma16(a, v4u{c0[0], c0[1], c1[0], c1[1]}, au);
        }
        MW();
        const v4u pv = __builtin_bit_cast(v4u, hh ? au : ag);
        const unsigned fo = WS_FIN + (4 * h + w) * 8192 + j * 2048 + lane * 16;
        st_sc1(rs, fo, v4u{pv[0], pv[1], pv[2], tag});
        st_sc1(rs, fo + 1024, v4u{pv[3], pv[3], pv[3], tag});
      }
      if (T_UV) {
        if (T_UV > 1) wait_until(t0, T_UV); else cnt_wait<2>(C_GO, 1);
        ld_uv();
      }
      asm volatile("s_waitcnt vmcnt(0)" : "+v"(dpf)::"memory");
      cnt_add(C_UV);
      if (ffn && w == 1) {
        v4u fv[8];
#pragma unroll 1
        for (unsigned spin = 0; spin < SPIN_CAP; ++spin) {
          MW();
          unsigned bad = 0;
#pragma unroll
          for (int t = 0; t < 8; ++t) fv[t] = ld_sc1(rs, WS_FIN + wi * 8192 + t * 1024 + lane * 16);
#pragma unroll
          for (int t = 0; t < 8; ++t) bad |= fv[t][3] ^ tag;
          if (!__any(bad != 0u)) break;
          __builtin_amdgcn_s_sleep(FX_SLP);
          CFENCE();
        }
        MW();
        float sv[4];
#pragma unroll
        for (int e = 0; e < 4; ++e) {
          const int o = e == 3, q = e % 3;
          sv[e] = (__uint_as_float(fv[o][q]) + __uint_as_float(fv[2 + o][q])) + (__uint_as_float(fv[4 + o][q]) + __uint_as_float(fv[6 + o][q]));
        }
#pragma unroll
        for (int e = 0; e < 2; ++e) {
          SB0();
          MW();
          const auto gu = __builtin_amdgcn_permlane32_swap(__float_as_uint(sv[e]), __float_as_uint(sv[e + 2]), false, false);
          const float gg = rbf(__uint_as_float(gu[0])), uu = rbf(__uint_as_float(gu[1]));
          const float inner = 0.7978845608028654f * (gg + 0.044715f * gg * gg * gg);
          const float th = 1.f - 2.f / (__expf(2.f * inner) + 1.f);
          SB0();
          MW();
          const float ge = rbf(0.5f * gg * (1.f + th));
          lds<u16>(L_HF + (4 * (g4 & 1) + 2 * hh + e) * 80 + i16 * 2) = (u16)(pk2(ge * uu, 0.f) & 0xFFFFu);
        }
        if (lane < 16) lds<v4u>(L_HF + (lane >> 1) * 80 + 32 + (lane & 1) * 16) = v4u{0u, 0u, 0u, 0u};
        MW();
        cnt_add(C_FFN);
      }
    }
    v4u uvf[4];
    MW();
    cnt_wait<2>(C_UV, 4);
#pragma unroll
    for (int ks = 0; ks < 4; ++ks) {
      const unsigned ra = 32 * (2 * (ks & 1) + (g4 >> 1)) + 16 * (ks >> 1) + 8 * (g4 & 1) + (i16 >> 2), rb = ra + 4, ch = 2 * w + ((lane & 3) >> 1);
      const v2u b0 = trread(L_WUV + ra * 256 + ((ch ^ fswz(ra)) * 16) + 8 * (lane & 1));
      const v2u b1 = trread(L_WUV + rb * 256 + ((ch ^ fswz(rb)) * 16) + 8 * (lane & 1));
      uvf[ks] = v4u{b0[0], b0[1], b1[0], b1[1]};
      PIN(uvf[ks]);
      MW();
    }
    unsigned cx = WS_CIN + wi * CIN_WI + (w ^ (CSW ? h & 7 : 0)) * 4096 + lane * 16;
    unsigned clx = L_CL + w * 272 + (((i16 >> 2) & 1) * 32 + (i16 >> 3) * 16 + (i16 & 3) * 4 + g4) * 4;
    PIN(cx); PIN(clx);
    MW();
    unsigned cla = L_CL + (lane & 7) * 272 + g4 * 16;
    unsigned cxw = L_CTX + (lane & 7) * 272 + (16 * w + 4 * g4) * 2;
    PIN(cla); PIN(cxw);
    MW();
    unsigned cxa[4];
#pragma unroll
    for (int u = 0; u < 4; ++u) { cxa[u] = L_CTX + ((lane & 7) ^ ((lane & 8) >> 2)) * 272 + (32 * ((u + j) & 3) + 8 * g4) * 2; PIN(cxa[u]); }
    unsigned eb = w >= 4 ? WS_EIN + ((4 * (g4 & 1) + 2 * hh) * 16 + 4 * (w - 4) + (i16 >> 2)) * 8192 + (wi ^ (ES2 ? (lane >> 2) * 4 : 0)) * 64 + (i16 & 3) * 16 : WS_EIN + wi * 8192 + lane * 16;
    unsigned f2a = WS_FLAG2 + (64 * (w & 1) + lane) * 1024 + fpos<FLW2>(wi >> 1) + (wi & 1) * 4;
    PIN(f2a);
    MW();
    unsigned msk[4];
#pragma unroll
    for (int q = 0; q < 4; ++q) { msk[q] = (i16 >> 1) == 2 * q ? ((i16 & 1) ? 0xFFFF0000u : 0xFFFFu) : 0u; PIN(msk[q]); }
    MW();
    PIN(cx); PIN(clx); PIN(cla); PIN(cxw); PIN(eb);
    {
      v2u m0, m1;
      unsigned ml, cap = SPIN_CAP;
      v4u dv[4];
      const unsigned ma = WS_ML + (wi * 128 + (w ^ (MS2 ? h & 7 : 0)) * 16) * 16 + ((lane & 31) ^ (CS2 ? (h & 7) * 4 + (j & 1) * 2 : 0)) * 8;
#define MLD(r) "global_atomic_add_x2 " r ", %8, %9, %10 sc0 sc1\n\t"
#define DLD "s_mov_b64 exec, -1\n\tglobal_load_dwordx4 %0, %11, %10 sc1\n\tglobal_load_dwordx4 %1, %11, %10 offset:1024 sc1\n\t" \
            "global_load_dwordx4 %2, %11, %10 offset:2048 sc1\n\tglobal_load_dwordx4 %3, %11, %10 offset:3072 sc1"
#define MLOPS : "=&v"(dv[0]), "=&v"(dv[1]), "=&v"(dv[2]), "=&v"(dv[3]), "=&{v[248:249]}"(m0), "=&{v[250:251]}"(m1), "=&v"(ml), "+s"(cap) \
              : "v"(ma), "v"(v2u{0u, 0u}), "s"(p.ws), "v"(cx), "s"(tag), "s"(0xFFFFFFFFull) : "memory", "vcc", "scc"
      if (MLDP == 2)
        asm volatile("s_mov_b64 exec, %13\n\t" MLD("v[248:249]") "s_sleep " STR(MLG) "\n1:\n\t" MLD("v[250:251]")
                     "s_waitcnt vmcnt(1)\n\tv_cmp_ne_u32 vcc, %12, v249\n\tv_mov_b32 %6, v248\n\ts_mov_b64 exec, vcc\n\ts_cbranch_vccz 2f\n\t" MLD("v[248:249]")
                     "s_sub_u32 %7, %7, 1\n\ts_waitcnt vmcnt(1)\n\tv_cmp_ne_u32 vcc, %12, v251\n\tv_mov_b32 %6, v250\n\ts_cselect_b64 vcc, 0, vcc\n\t"
                     "s_mov_b64 exec, vcc\n\ts_cbranch_vccnz 1b\n2:\n\t" DLD MLOPS);
      else
        asm volatile("s_mov_b64 exec, %13\n1:\n\t" MLD("v[248:249]") "s_sub_u32 %7, %7, 1\n\ts_waitcnt vmcnt(0)\n\t"
                     "v_cmp_ne_u32 vcc, %12, v249\n\tv_mov_b32 %6, v248\n\ts_cselect_b64 vcc, 0, vcc\n\t" MLX "s_cbranch_vccnz 1b\n\t" DLD MLOPS);
      MW();
      v4u aw[4];
      {
        const float o = xor1(__uint_as_float(ml));
        const float ms = (lane & 1) ? o : __uint_as_float(ml), lsv = (lane & 1) ? __uint_as_float(ml) : o;
        float M = rowmax(ms);
        M = rs_max16(M, M);
        const float ee = ex2(ms - M);
        const float wsv = 2.f * ee / add16(rowsum(ee * lsv));
        unsigned wb = pk2(wsv, wsv);
        PIN(wb);
        MW();
#pragma unroll
        for (int t = 0; t < 4; ++t) {
          const unsigned u = __shfl(wb, 8 * t + 2 * g4);
          aw[t] = v4u{u & msk[0], u & msk[1], u & msk[2], u & msk[3]};
          PIN(aw[t]);
          if (t & 1) MW();
        }
      }
      asm volatile("s_waitcnt vmcnt(0)" : "+v"(dv[0]), "+v"(dv[1]), "+v"(dv[2]), "+v"(dv[3]), "+{v[248:249]}"(m0), "+{v[250:251]}"(m1)::"memory");
      if (RARE(__any(max(max(dv[0][0], dv[1][0]), max(dv[2][0], dv[3][0])) == POISON))) {
#pragma unroll 1
        for (unsigned spin = 0; spin < SPIN_CAP; ++spin) {
          MW();
#pragma unroll
          for (int t = 0; t < 4; ++t) dv[t] = ld_sc1(rs, cx + t * 1024);
          if (!__any(max(max(dv[0][0], dv[1][0]), max(dv[2][0], dv[3][0])) == POISON)) break;
          CFENCE();
        }
      }
      MW();
      f32x4 dx = {0.f, 0.f, 0.f, 0.f}, dy = dx;
#pragma unroll
      for (int t = 0; t < 2; ++t) { dx = mfma16(aw[t], dv[t], dx); dy = mfma16(aw[t + 2], dv[t + 2], dy); }
      lds<unsigned>(clx) = pk2(dx[0] + dy[0], dx[1] + dy[1]);
    }
    wg_barrier();
    MW();
    if (F2AT == 0 && (w == 2 || w == 3)) st_sc1_32(rs, f2a, tag);
    {
      v4u a[4];
#pragma unroll
      for (int ks = 0; ks < 4; ++ks) a[ks] = lds128(cla + ks * 64);
      SB0();
      f32x4 c0 = mfma16(uvf[0], a[0], f32x4{0.f, 0.f, 0.f, 0.f}), c1 = mfma16(uvf[1], a[1], f32x4{0.f, 0.f, 0.f, 0.f});
      c0 = mfma16(uvf[2], a[2], c0);
      c1 = mfma16(uvf[3], a[3], c1);
      c0 += c1;
      lds<v2u>(cxw) = v2u{pk2(c0[0], c0[1]), pk2(c0[2], c0[3])};
    }
    wg_barrier();
    MW();
    if (__builtin_expect(w >= 4, 1)) {
      v4u a[4];
#pragma unroll
      for (int u = 0; u < 4; ++u) a[u] = lds128(cxa[u]);
      SB0();
#pragma unroll
      for (int u = 0; u < 4; ++u) {
#pragma unroll
        for (int jj = 0; jj < 8; ++jj) oc[jj] = mfma16(a[u], wof[u][jj], oc[jj]);
        if (u & 1) { SB0(); MW(); }
      }
#pragma unroll
      for (int e = 0; e < 2; ++e)
        __builtin_amdgcn_raw_buffer_store_b128(v4u{san(pk2(oc[0][e], oc[1][e])), san(pk2(oc[2][e], oc[3][e])), san(pk2(oc[4][e], oc[5][e])), san(pk2(oc[6][e], oc[7][e]))},
                                               rs, eb, e * 16 * 8192, 16);
    } else if (w == 0) {
      const unsigned oo = wi * 64 + (i16 & 3) * 16 + g4 * 4;
      const unsigned xf = *(const GAS unsigned*)((const char*)p.x + oo);
      const unsigned one = (i16 & 2) ? 0u : ((i16 & 1) ? 0x3F800000u : 0x3F80u);
      v4u sel;
#pragma unroll
      for (int q = 0; q < 4; ++q) sel[q] = (i16 >> 2) == q ? one : 0u;
      v2u f0;
      unsigned eb2 = eb + 4096;
      float x0 = bf2f(xf), x1 = bfhi(xf);
      PIN(sel); PIN(eb2); PIN(x0); PIN(x1);
      MW();
      poll_flags(p.ws, WS_FLAG2 + wi * 1024 + fpos<FLW2>(lane), tag, f0);
      MW();
      if (E_DLY) __builtin_amdgcn_s_sleep(E_DLY);
      float r0, r1;
      v4u ev[8];
      auto esum = [&]() AI {
#pragma unroll
        for (int t = 0; t < 8; ++t) ev[t] = ld_sc1(rs, (t < 4 ? eb : eb2) + (t & 3) * 1024);
        SB0();
        f32x4 d[4];
#pragma unroll
        for (int t = 0; t < 8; ++t) d[t & 3] = mfma16(sel, ev[t], t < 4 ? f32x4{0.f, 0.f, 0.f, 0.f} : d[t & 3]);
        MW();
        r0 = (d[0][0] + d[1][0]) + (d[2][0] + d[3][0]); r1 = (d[0][1] + d[1][1]) + (d[2][1] + d[3][1]);
        r0 += ror16<8>(r0); r1 += ror16<8>(r1);
        r0 += ror16<4>(r0); r1 += ror16<4>(r1);
      };
      esum();
      if (RARE(__any(r0 != r0))) {
#pragma unroll 1
        for (unsigned spin = 0; spin < SPIN_CAP; ++spin) {
          MW();
          unsigned mxv = 0;
#pragma unroll
          for (int t = 0; t < 8; ++t) mxv = max(mxv, max(max(ev[t][0], ev[t][1]), max(ev[t][2], ev[t][3])));
          if (__all(mxv != POISON)) break;
          CFENCE();
          MW();
          esum();
          if (!__any(r0 != r0)) break;
        }
      }
      MW();
      asm volatile("s_mov_b64 exec, %3\n\tglobal_store_dword %0, %1, %2\n\ts_mov_b64 exec, -1" ::"v"(oo), "v"(pk2(x0 + r0, x1 + r1)), "s"(p.out), "s"(0x000F000F000F000Full) : "memory");
      asm volatile("s_waitcnt vmcnt(0)" : "+v"(f0)::"memory");
    } else if (w >= 2) {
      MW();
      if (F2AT == 1) st_sc1_32(rs, f2a, tag);
      if (wi == 0 && tid == 128) st_sc1_32(rs, WS_GEN, gen1);
    }
  }
}

extern "C" void kernel_launch(void* const* d_in, const int* in_sizes, int n_in, void* d_out, int out_size, void* d_ws,
                              size_t ws_size, hipStream_t stream) {
  Params p;
  p.x = (const u16*)d_in[0];
  p.ckv = (const u16*)d_in[1];
  p.wq = (const u16*)d_in[2];
  p.wuk = (const u16*)d_in[3];
  p.wuv = (const u16*)d_in[4];
  p.wo = (const u16*)d_in[5];
  p.wg = (const u16*)d_in[7];
  p.wu = (const u16*)d_in[8];
  p.wd = (const u16*)d_in[9];
  p.out = (u16*)d_out;
  p.ws = (unsigned char*)d_ws;
  (void)hipFuncSetAttribute((const void*)fused_kernel, hipFuncAttributeMaxDynamicSharedMemorySize, LDS_BYTES);
  fused_kernel<<<256, 512, LDS_BYTES, stream>>>(p);
}
```

```cpp
#include <hip/hip_runtime.h>
#include <stdint.h>

typedef unsigned short u16;
typedef unsigned long long u64;
typedef unsigned v4u __attribute__((ext_vector_type(4)));
typedef unsigned v2u __attribute__((ext_vector_type(2)));
typedef float f32x4 __attribute__((ext_vector_type(4)));
typedef float f32x16 __attribute__((ext_vector_type(16)));
typedef unsigned v16u __attribute__((ext_vector_type(16)));
typedef float v2f __attribute__((ext_vector_type(2)));
typedef short bf16x8 __attribute__((ext_vector_type(8)));
typedef short v4s __attribute__((ext_vector_type(4)));
typedef __bf16 v2bf __attribute__((ext_vector_type(2)));
#define GAS __attribute__((address_space(1)))
#define LAS __attribute__((address_space(3)))
#define DI __device__ __forceinline__

#define NF 12
#define R2AT (47 + NF)
#define POLL_START 250
#define POLL_GAP 4
#define Q_DLY 0
#define KV_DLY 50
#define E_DLY 10
#define T_F 450
#define T_UV 900
#define T_O 680
#define MLAT 15
#define FR4 1
#define PAUSE 1
#define QPF 0
#define WPF 2
#define BPF 0
#define BPF_DLY 0
#define WPF_DLY 0
#define FMAP 0
#define OD 6
#define V3 4
#define QPRIO 1
#define PK 8
#define WM_DLY 0
#define FPF 0
#define FLAG_AT 1
#define FL_DLY 0
#define PZ_DLY 0
#define B0W 2
#define NF2 16
#define PMIN 32
#define VD 2
#define MIDSIG 1
#define V2AT 0
#define CKAT 10
#define PPRIO 0
#define SPAT 13
#define MIX 0
#define PROG 1
#define RES 1
#define XAT 4
#define PUTAT 3
#define FLAG_FIRST 0
#define F2AT 0
#define GO_DLY 100
#define C_DLY 0
#define KPF 0
#define KPF_AT 0
#define KPF_S 1
#define FX_SLP 8
#define OSAT 11
#define PZE 0
#define PZT 0
#define PZ_AT 150
#define K1AT 1
#define CSW 1
#define FLW 0
#define FLW2 0
#define BMAP 0
#define EMAP 0
#define G0T 1
#define KPF_ST 0
#define K3S 0
#define QST 0
#define F2D 3
#define F2G 4
#define F2X 1
#define QPM 1
#define MLDP 1
#define OVTV 88.f
#define CS2 1
#define MS2 1
#define ES2 1
#define VSP 1
#define CVW 1
#define KSP 1
#define QL 4
#define YB 5
#define CPA 4
#define CPN 1
#define PFT 0
#define KPD 0
#define GR 13
#define FA 0
#define CK0 12
#define UVTV 100.f
#define REF0 70.f
#define NF3 12
#define ISS 0
#define Y2N 0
#define SPK 0
#define F1AT 0
#define TPF 0
#define TPD 0
#define WPM 0
#define WPN ""
#define NTO 0
#define NTF 0
#define NTK 0
#define NTP 0
#define NT_O ""
#define NT_K ""
#define OROT 1
#define NTQ 1
#define NT_Q " nt"
#define OPF 0
#define OPD 0
#define NTC 1
#define NTU 1
#define NTD 1
#define NT_C " nt"
#define NT_D " nt"
#define YE 2
#define XR 15
#define XC 20
#define MLP 1
#define MLG 10
#define NFW 10

constexpr int DM = 512, FF = 1376;
constexpr unsigned WS_GEN = 0;
constexpr unsigned WS_FLAG = 4096;
constexpr unsigned WS_FLAG2 = WS_FLAG + 262144;
constexpr unsigned WS_QABS = WS_FLAG2 + 131072;
constexpr unsigned WS_ML = WS_QABS + 524288;
constexpr unsigned WS_EIN = WS_ML + 262144;
constexpr unsigned WS_CIN = WS_EIN + 1048576;
constexpr unsigned CIN_ROW = 256, CIN_WI = 128 * CIN_ROW;
constexpr unsigned WS_FIN = WS_CIN + 128 * CIN_WI;
constexpr unsigned WS_END = WS_FIN + 88 * 8192;
constexpr unsigned L_Q = 0;
constexpr unsigned L_P = L_Q;
constexpr unsigned L_LS = L_Q + 24576;
constexpr unsigned L_AL = L_Q + 25600;
constexpr unsigned L_SLOT = 32768;
constexpr unsigned L_XN = 0;
constexpr unsigned L_QH = 8448;
constexpr unsigned L_CL = L_QH + 2176;
constexpr unsigned L_CTX = L_CL + 2176;
constexpr unsigned L_HF = L_CTX + 2176;
constexpr unsigned L_FP = L_HF + 640;
constexpr unsigned L_WQ = 32768;
constexpr unsigned L_GU = 65536;
constexpr unsigned L_WUV = 98304;
constexpr unsigned L_CNT = 32512;
constexpr unsigned LDS_BYTES = 163840;
enum { C_P = 0, C_OVF = 1, C_ST = 2, C_FL = 3, C_MX = 5, C_PVD, C_XS, C_MASK = 8, C_N = 16 };
enum { C_QST = 0, C_GU, C_FFN, C_UV, C_GO, C_SENT };

constexpr unsigned ANY = 0x80u;
constexpr unsigned POISON = 0xFFFFFFFFu;
constexpr unsigned SPIN_CAP = 1u << 20;

struct Params {
  const u16 *x, *ckv, *wq, *wuk, *wuv, *wo, *wg, *wu, *wd;
  u16* out;
  unsigned char* ws;
};

DI float bf2f(unsigned v) { return __uint_as_float(v << 16); }
DI float bfhi(unsigned v) { return __uint_as_float(v & 0xFFFF0000u); }
DI unsigned pk2(float a, float b) {
  v2f t = {a, b};
  return __builtin_bit_cast(unsigned, __builtin_convertvector(t, v2bf));
}
DI float rbf(float a) { return bf2f(pk2(a, 0.f) & 0xFFFFu); }
DI unsigned san(unsigned v) { return min(v, 0xFFFFFFFEu); }
DI f32x4 mfma16(v4u a, v4u b, f32x4 c) {
  return __builtin_amdgcn_mfma_f32_16x16x32_bf16(__builtin_bit_cast(bf16x8, a), __builtin_bit_cast(bf16x8, b), c, 0, 0, 0);
}
DI f32x16 mfma32(v4u a, v4u b, f32x16 c) {
  return __builtin_amdgcn_mfma_f32_32x32x16_bf16(__builtin_bit_cast(bf16x8, a), __builtin_bit_cast(bf16x8, b), c, 0, 0, 0);
}
DI v4u lds128(unsigned a) { return *(LAS v4u*)(size_t)a; }
DI v2u trread(unsigned a) { return __builtin_bit_cast(v2u, __builtin_amdgcn_ds_read_tr16_b64_v4i16((LAS v4s*)(size_t)a)); }
template <class T> DI LAS T& lds(unsigned a) { return *(LAS T*)(size_t)a; }
template <int POL> DI void glds16(const void* gsrc, unsigned lds_dst) {
  unsigned keep;
  if (POL == 1) asm volatile("s_mov_b32 %0, m0\n\ts_mov_b32 m0, %2\n\ts_nop 0\n\tglobal_load_lds_dwordx4 %1, off nt\n\ts_mov_b32 m0, %0" : "=&s"(keep) : "v"(gsrc), "s"(lds_dst) : "memory");
  else if (POL == 2) asm volatile("s_mov_b32 %0, m0\n\ts_mov_b32 m0, %2\n\ts_nop 0\n\tglobal_load_lds_dwordx4 %1, off sc1\n\ts_mov_b32 m0, %0" : "=&s"(keep) : "v"(gsrc), "s"(lds_dst) : "memory");
  else asm volatile("s_mov_b32 %0, m0\n\ts_mov_b32 m0, %2\n\ts_nop 0\n\tglobal_load_lds_dwordx4 %1, off\n\ts_mov_b32 m0, %0" : "=&s"(keep) : "v"(gsrc), "s"(lds_dst) : "memory");
}
DI void glds16s(unsigned voff, const void* sbase, unsigned lds_dst) {
  unsigned keep;
  asm volatile("s_mov_b32 %0, m0\n\ts_mov_b32 m0, %3\n\ts_nop 0\n\tglobal_load_lds_dwordx4 %1, %2" NT_Q "\n\ts_mov_b32 m0, %0" : "=&s"(keep) : "v"(voff), "s"(sbase), "s"(lds_dst) : "memory");
}
DI void gld16s(v4u& d, unsigned voff, const void* sbase) {
  asm volatile("global_load_dwordx4 %0, %1, %2 nt" : "=v"(d) : "v"(voff), "s"(sbase) : "memory");
}
#define VMWAIT(n) asm volatile("s_waitcnt vmcnt(%0)" ::"i"(n) : "memory")
#define LGKM0() asm volatile("s_waitcnt lgkmcnt(0)" ::: "memory")
#define CFENCE() asm volatile("" ::: "memory")
#define SB0() __builtin_amdgcn_sched_barrier(0)
#define W8(a, o) "+v"(a[o]), "+v"(a[o + 1]), "+v"(a[o + 2]), "+v"(a[o + 3]), "+v"(a[o + 4]), "+v"(a[o + 5]), "+v"(a[o + 6]), "+v"(a[o + 7])
#define WARMUP 2
#define MK(n) do {} while (0)
#define WARM(A, B, first, step) do {} while (0)
#define MK_TABLE() asm volatile(".Lk_beg:\n\t.subsection 12\n.Lk_end:\n\t.subsection 0")
#define CODE_PF(chunk, ldsdst) do { unsigned keep_; \
  asm volatile("s_getpc_b64 s[94:95]\n8:\n\ts_sub_u32 s94, s94, 8b-.Lk_beg\n\ts_subb_u32 s95, s95, 0\n\tv_cmp_gt_u32 vcc, .Lk_end-.Lk_beg, %1\n\t" \
               "s_mov_b64 exec, vcc\n\ts_mov_b32 %0, m0\n\ts_mov_b32 m0, %2\n\ts_nop 0\n\tglobal_load_lds_dword %1, s[94:95]\n\ts_mov_b32 m0, %0\n\ts_mov_b64 exec, -1" \
               : "=&s"(keep_) : "v"((chunk) * 8192u + lane * 128u), "s"(ldsdst) : "s94", "s95", "vcc", "scc", "memory"); } while (0)
#define CODE_PF3(grp, msk) do { unsigned keep_, t_, o2_; \
  asm volatile("s_and_b32 %1, %4, %5\n\ts_cbranch_scc1 9f\n\ts_getpc_b64 s[94:95]\n8:\n\tv_readfirstlane_b32 %1, %3\n\tv_lshlrev_b32 %2, 7, %3\n\t" \
               "s_mov_b32 %0, m0\n\tv_cmp_gt_u32 vcc, 0x8000, %2\n\ts_lshl_b32 m0, %1, 7\n\ts_mov_b64 exec, vcc\n\t" \
               "global_load_lds_dword %2, s[94:95]\n\tv_add_u32 %2, 0x8000, %2\n\tv_cmp_gt_u32 vcc, .Lk_end-8b, %2\n\ts_mov_b64 exec, vcc\n\t" \
               "global_load_lds_dword %2, s[94:95]\n\ts_mov_b32 m0, %0\n\ts_mov_b64 exec, -1\n9:" \
               : "=&s"(keep_), "=&s"(t_), "=&v"(o2_) : "v"(threadIdx.x), "s"(grp), "i"(msk) : "s94", "s95", "vcc", "scc", "memory"); } while (0)
#define CODE_PF2(sel, ldsdst) do { unsigned keep_, o2_; \
  asm volatile("s_cmp_gt_u32 %4, 3\n\ts_cbranch_scc1 9f\n\ts_getpc_b64 s[94:95]\n8:\n\ts_sub_u32 s94, s94, 8b-.Lk_beg\n\ts_subb_u32 s95, s95, 0\n\t" \
               "v_lshl_add_u32 %1, %4, 13, %2\n\ts_mov_b32 %0, m0\n\ts_mov_b32 m0, %3\n\tv_cmp_gt_u32 vcc, .Lk_end-.Lk_beg, %1\n\ts_mov_b64 exec, vcc\n\t" \
               "global_load_lds_dword %1, s[94:95]\n\ts_mov_b64 exec, -1\n\tv_add_u32 %1, 0x8000, %1\n\tv_cmp_gt_u32 vcc, .Lk_end-.Lk_beg, %1\n\ts_mov_b64 exec, vcc\n\t" \
               "global_load_lds_dword %1, s[94:95]\n\ts_mov_b32 m0, %0\n\ts_mov_b64 exec, -1\n9:" \
               : "=&s"(keep_), "=&v"(o2_) : "v"(lane * 128u), "s"(ldsdst), "s"(sel) : "s94", "s95", "vcc", "scc", "memory"); } while (0)

#define MW() MK(2)
#define MQ() MK(4)
#define MP() MK(6)
#define MS() MK(8)

DI void wg_barrier() {
  LGKM0();
  __builtin_amdgcn_s_barrier();
  CFENCE();
}
DI void cnt_addv(unsigned c, unsigned v) {
  asm volatile("s_mov_b64 exec, 1\n\tds_add_u32 %0, %1 offset:%2\n\ts_mov_b64 exec, -1" ::"v"(0u), "v"(v), "i"(L_CNT + c * 4u) : "memory");
}
DI void cnt_add(unsigned c) { cnt_addv(c, 1u); }
DI void cnt_add2(unsigned c, unsigned v0, unsigned v1) {
  asm volatile("s_mov_b64 exec, 1\n\tds_add_u64 %0, %1 offset:%2\n\ts_mov_b64 exec, -1" ::"v"(0u), "v"(v2u{v0, v1}), "i"(L_CNT + c * 4u) : "memory");
}
#define AI __attribute__((always_inline))
#define RARE(c) __builtin_expect(!!(c), 0)
DI unsigned vm_pending() {
  unsigned v;
  asm volatile("s_getreg_b32 %0, hwreg(HW_REG_IB_STS)" : "=s"(v));
  return (v & 15u) | ((v >> 18) & 48u);
}
#define STR_(x) #x
#define STR(x) STR_(x)
#define VW1(n) "s_waitcnt vmcnt(" #n ")\n\ts_branch 2f\n\t"
#define MKR
#define MKB
#define L_CNT_ 32512
#define KV_RUN_BODY() asm volatile(".subsection 10\n" \
  MKR ".Lkv_chk:\n\t" \
  "s_mov_b32 s67, 1\n\ts_mov_b32 s91, m0\n\ts_mov_b32 s90, 0xffffff\n\ts_branch 12f\n" \
  ".Lkv_runp:\n\t" \
  "s_mov_b32 s67, 1\n\ts_branch 11f\n" \
  ".Lkv_run:\n\t" \
  "s_mov_b32 s67, 2\n" \
  "11:\n\t" \
  "s_mov_b32 s91, m0\n\ts_mov_b32 s90, 0xffffff\n\ts_mov_b32 s64, -1\n" \
  "3:\n\t" \
  "v_mov_b32 v252, 0\n\t" \
  "ds_read_b128 v[252:255], v252 offset:%0\n\t" \
  "s_cmp_lg_u32 s67, 1\n\ts_cbranch_scc1 12f\n\t" \
  "v_add_u32 v249, s74, v250\n\t" \
  "s_bitcmp1_b32 s74, 12\n\ts_cbranch_scc1 14f\n\t" \
  "ds_read_b128 v[232:235], v249\n\tds_read_b128 v[236:239], v249 offset:1024\n\tds_read_b128 v[240:243], v249 offset:2048\n\tds_read_b128 v[244:247], v249 offset:3072\n\t" \
  "s_branch 12f\n" \
  "14:\n\t" \
  "ds_read_b128 v[216:219], v249\n\tds_read_b128 v[220:223], v249 offset:1024\n\tds_read_b128 v[224:227], v249 offset:2048\n\tds_read_b128 v[228:231], v249 offset:3072\n" \
  "12:\n\t" \
  "s_cmp_eq_u32 s77, 4\n\ts_cbranch_scc1 6f\n\t" \
  "s_getreg_b32 s87, hwreg(HW_REG_IB_STS)\n\t" \
  "s_lshl_b32 s89, s77, 4\n\ts_add_u32 s89, s89, 16\n\t" \
  "s_lshr_b32 s88, s87, 18\n\ts_and_b32 s88, s88, 48\n\ts_and_b32 s87, s87, 15\n\ts_or_b32 s87, s87, s88\n\t" \
  "s_add_u32 s89, s89, s87\n\ts_cmp_lt_u32 s76, s89\n\ts_cbranch_scc1 4f\n\t" \
  "s_min_u32 s88, s87, 23\n\ts_lshl_b32 s88, s88, 3\n\t" \
  "s_getpc_b64 s[94:95]\n0:\n\ts_add_u32 s88, s88, 1f-0b\n\ts_add_u32 s94, s94, s88\n\ts_addc_u32 s95, s95, 0\n\ts_setpc_b64 s[94:95]\n" \
  MKR "1:\n\t" \
  VW1(0) VW1(1) VW1(2) VW1(3) VW1(4) VW1(5) VW1(6) VW1(7) VW1(8) VW1(9) VW1(10) VW1(11) VW1(12) VW1(13) VW1(14) VW1(15) \
  VW1(16) VW1(17) VW1(18) VW1(19) VW1(20) VW1(21) VW1(22) VW1(23) \
  MKR "2:\n\t" \
  "s_lshl_b32 s88, s77, 2\n\ts_add_u32 s88, s88, s83\n\ts_lshl_b32 s88, 1, s88\n\t" \
  "v_mov_b32 v248, 0\n\tv_mov_b32 v249, s88\n\ts_mov_b64 exec, 1\n\tds_add_u32 v248, v249 offset:%1\n\ts_mov_b64 exec, -1\n\t" \
  "s_add_u32 s77, s77, 1\n" \
  "4:\n\t" \
    \
  "s_lshr_b32 s89, s86, s83\n\ts_and_b32 s89, s89, 0x11000000\n\ts_and_b32 s88, s86, 0x440000\n\ts_or_b32 s89, s89, s88\n\t" \
  "s_cmp_eq_u32 s89, 0x11440000\n\ts_cselect_b32 s88, 64, 48\n\t" \
  "s_and_b32 s89, s89, 0x1040000\n\ts_cmp_eq_u32 s89, 0x1040000\n\ts_cselect_b32 s88, s88, 32\n\t" \
  "s_and_b32 s89, s75, %2 | 0x100\n\ts_cmp_eq_u32 s89, 0x100\n\ts_cselect_b32 s89, " STR(PMIN) ", 64\n\ts_min_u32 s88, s88, s89\n\t" \
  "s_and_b32 s89, s75, %2\n\ts_cselect_b32 s89, " STR(NF2) ", " STR(NF) "\n\t" \
  "s_add_u32 s89, s89, s76\n\ts_sub_u32 s89, s89, s87\n\ts_min_u32 s88, s88, s89\n\t" \
  "s_cmp_ge_i32 s76, s88\n\ts_cbranch_scc1 6f\n" \
  "5:\n\t" \
  "v_readlane_b32 s89, v251, s76\n\ts_add_u32 s76, s76, 1\n\ts_and_b32 s87, s89, 0x1fc00\n\tv_xor_b32 v249, s89, v250\n\t" \
  "s_add_u32 m0, s87, s82\n\ts_cmp_lt_u32 s76, s88\n\tglobal_load_lds_dwordx4 v249, s[80:81]" NT_C "\n\ts_cbranch_scc1 5b\n\t" \
  "6:\n\t" MKR \
  "s_waitcnt lgkmcnt(0)\n\t" \
  "v_readfirstlane_b32 s85, v252\n\tv_readfirstlane_b32 s86, v254\n\tv_readfirstlane_b32 s84, v253\n\tv_readfirstlane_b32 s75, v255\n\t" \
  "s_bitset1_b32 s85, 7\n\ts_and_b32 s89, s86, s78\n\ts_cmp_eq_u32 s89, s78\n\ts_cbranch_scc0 9f\n\ts_and_b32 s89, s85, s79\n\ts_cbranch_scc0 9f\n\t" \
  "s_cmp_lg_u32 s67, 0\n\ts_cbranch_scc1 10f\n" \
  "9:\n\ts_cmp_eq_u32 s67, 2\n\ts_cbranch_scc1 13f\n\ts_and_b32 s89, s85, s68\n\ts_cselect_b32 s67, 1, 0\n" \
  "13:\n\ts_mov_b32 s89, s79\n\ts_cmp_eq_u32 s76, s64\n\ts_cbranch_scc1 17f\n\ts_sub_u32 s90, s90, 1\n\ts_cbranch_scc0 3b\n" \
  "10:\n\ts_mov_b32 s64, 0\n" \
  "17:\n\ts_mov_b32 m0, s91\n\ts_mov_b32 s87, s64\n" \
  "15:\n\ts_cmp_eq_u32 s77, 2\n\ts_cselect_b32 s66, 0, 15\n\ts_or_b32 s66, s66, 0xc00000\n\t" \
  "s_lshr_b32 s65, s77, 2\n\ts_cmp_eq_u32 s76, 64\n\ts_cselect_b32 s65, s65, 1\n\ts_lshl_b32 s65, s65, 8\n\ts_cmp_eq_u32 s87, 0\n\ts_setpc_b64 s[92:93]\n" \
    \
  ".Lkv_iss:\n\ts_cmp_le_u32 s76, 48\n\ts_cbranch_scc1 22f\n\ts_getreg_b32 s87, hwreg(HW_REG_IB_STS)\n\t" \
  "s_lshr_b32 s88, s87, 18\n\ts_and_b32 s88, s88, 48\n\ts_and_b32 s87, s87, 15\n\ts_or_b32 s87, s87, s88\n\t" \
  "s_add_u32 s88, s76, " STR(NF3) "\n\ts_sub_u32 s88, s88, s87\n\ts_min_u32 s88, s88, 64\n\ts_cmp_ge_i32 s76, s88\n\ts_cbranch_scc1 22f\n\ts_mov_b32 s91, m0\n" \
  "23:\n\tv_readlane_b32 s89, v251, s76\n\ts_add_u32 s76, s76, 1\n\ts_and_b32 s87, s89, 0x1fc00\n\tv_xor_b32 v249, s89, v250\n\t" \
  "s_add_u32 m0, s87, s82\n\ts_cmp_lt_u32 s76, s88\n\tglobal_load_lds_dwordx4 v249, s[80:81]" NT_C "\n\ts_cbranch_scc1 23b\n\t" \
  "s_mov_b32 m0, s91\n\ts_mov_b32 s87, 0\n\ts_branch 15b\n" \
  "22:\n\ts_setpc_b64 s[92:93]\n" \
  MKR ".Lkv_sig:\n\t" \
  "s_waitcnt vmcnt(16)\n\ts_cmp_eq_u32 s77, 2\n\ts_cbranch_scc1 16f\n\ts_waitcnt vmcnt(0)\n" \
  "16:\n\ts_lshl_b32 s88, s77, 2\n\ts_add_u32 s88, s88, s83\n\ts_lshl_b32 s88, 1, s88\n\t" \
  "v_mov_b32 v248, 0\n\tv_mov_b32 v249, s88\n\ts_mov_b64 exec, 1\n\tds_add_u32 v248, v249 offset:%1\n\ts_mov_b64 exec, -1\n\t" \
  "s_add_u32 s77, s77, 1\n\ts_mov_b32 s87, 0\n\ts_cmp_lg_u32 s77, 4\n\ts_cbranch_scc1 15b\n\ts_cmp_eq_u32 s98, 0\n\ts_cbranch_scc1 15b\n" \
    \
  ".Lkv_y3:\n\ts_mov_b32 s88, s98\n\tv_mov_b32 v248, 0\n" \
  "20:\n\tds_read_b32 v249, v248 offset:%1\n\ts_sub_u32 s88, s88, 1\n\ts_cbranch_scc1 21f\n\ts_waitcnt lgkmcnt(0)\n\t" \
  "v_and_b32 v249, 0xf000, v249\n\tv_cmp_ne_u32 vcc, 0xf000, v249\n\ts_cbranch_vccnz 20b\n\ts_sleep " STR(VD) "\n" \
  MKR ".Lkv_ldv:\n\t" \
  "v_xor_b32 v248, 0, v215\n\tv_xor_b32 v249, 32, v215\n\t" \
  "ds_read_b64_tr_b16 v[128:129], v248 offset:0\n\tds_read_b64_tr_b16 v[130:131], v249 offset:4096\n\t" \
  "ds_read_b64_tr_b16 v[132:133], v248 offset:16384\n\tds_read_b64_tr_b16 v[134:135], v249 offset:20480\n\t" \
  "ds_read_b64_tr_b16 v[136:137], v248 offset:32768\n\tds_read_b64_tr_b16 v[138:139], v249 offset:36864\n\t" \
  "ds_read_b64_tr_b16 v[140:141], v248 offset:49152\n\tds_read_b64_tr_b16 v[142:143], v249 offset:53248\n\t" \
  "v_xor_b32 v248, 64, v215\n\tv_xor_b32 v249, 96, v215\n\t" \
  "ds_read_b64_tr_b16 v[144:145], v248 offset:0\n\tds_read_b64_tr_b16 v[146:147], v249 offset:4096\n\t" \
  "ds_read_b64_tr_b16 v[148:149], v248 offset:16384\n\tds_read_b64_tr_b16 v[150:151], v249 offset:20480\n\t" \
  "ds_read_b64_tr_b16 v[152:153], v248 offset:32768\n\tds_read_b64_tr_b16 v[154:155], v249 offset:36864\n\t" \
  "ds_read_b64_tr_b16 v[156:157], v248 offset:49152\n\tds_read_b64_tr_b16 v[158:159], v249 offset:53248\n\t" \
  MKR "v_xor_b32 v248, 128, v215\n\tv_xor_b32 v249, 160, v215\n\t" \
  "ds_read_b64_tr_b16 v[160:161], v248 offset:0\n\tds_read_b64_tr_b16 v[162:163], v249 offset:4096\n\t" \
  "ds_read_b64_tr_b16 v[164:165], v248 offset:16384\n\tds_read_b64_tr_b16 v[166:167], v249 offset:20480\n\t" \
  "ds_read_b64_tr_b16 v[168:169], v248 offset:32768\n\tds_read_b64_tr_b16 v[170:171], v249 offset:36864\n\t" \
  "ds_read_b64_tr_b16 v[172:173], v248 offset:49152\n\tds_read_b64_tr_b16 v[174:175], v249 offset:53248\n\t" \
  "v_xor_b32 v248, 192, v215\n\tv_xor_b32 v249, 224, v215\n\t" \
  "ds_read_b64_tr_b16 v[176:177], v248 offset:0\n\tds_read_b64_tr_b16 v[178:179], v249 offset:4096\n\t" \
  "ds_read_b64_tr_b16 v[180:181], v248 offset:16384\n\tds_read_b64_tr_b16 v[182:183], v249 offset:20480\n\t" \
  "ds_read_b64_tr_b16 v[184:185], v248 offset:32768\n\tds_read_b64_tr_b16 v[186:187], v249 offset:36864\n\t" \
  "ds_read_b64_tr_b16 v[188:189], v248 offset:49152\n\tds_read_b64_tr_b16 v[190:191], v249 offset:53248\n\t" \
  "s_mov_b32 s99, 1\n\ts_mov_b32 s87, 0\n\ts_branch 15b\n" \
  "21:\n\ts_waitcnt lgkmcnt(0)\n\ts_mov_b32 s87, 0\n\ts_cmp_lt_u32 s98, 0x1000\n\ts_cbranch_scc1 15b\n\ts_branch .Lkv_ldv\n\t" \
  ".subsection 12\n\t.fill 64, 4, 0xBF810000\n\t.subsection 0" \
  :: "i"(L_CNT), "i"(L_CNT + C_ST * 4), "i"(RES ? 4 : 0x40))
DI unsigned cnt_add_rtn(unsigned c) {
  u64 keep;
  unsigned v;
  asm volatile("s_mov_b64 %0, exec\n\ts_mov_b64 exec, 1\n\tds_add_rtn_u32 %1, %2, %3 offset:%4\n\ts_waitcnt lgkmcnt(0)\n\ts_mov_b64 exec, %0"
               : "=&s"(keep), "=&v"(v) : "v"(c * 4u), "v"(1u), "i"(L_CNT) : "memory");
  return __builtin_amdgcn_readfirstlane(v);
}
DI unsigned cnt_get(unsigned c) {
  unsigned v;
  asm volatile("ds_read_b32 %0, %1 offset:%2\n\ts_waitcnt lgkmcnt(0)" : "=v"(v) : "v"(c * 4u), "i"(L_CNT) : "memory");
  return __builtin_amdgcn_readfirstlane(v);
}
template <int SLP = 0> DI void cnt_wait(unsigned c, unsigned target) {
#pragma unroll 1
  for (unsigned s = 0; s < SPIN_CAP * 16u; ++s) {
    if (cnt_get(c) >= target) break;
    if (SLP) __builtin_amdgcn_s_sleep(SLP);
  }
}
DI unsigned now32() { return (unsigned)__builtin_amdgcn_s_memrealtime(); }
DI void wait_until(unsigned t0, unsigned ticks) {
  if (ticks == 0) return;
#pragma unroll 1
  while (now32() - t0 < ticks) __builtin_amdgcn_s_sleep(1);
}
DI v4u ld_sc1(__amdgpu_buffer_rsrc_t rs, unsigned off) { return __builtin_amdgcn_raw_buffer_load_b128(rs, off, 0, 16); }
DI v2u ld_sc1_64(__amdgpu_buffer_rsrc_t rs, unsigned off) { return __builtin_amdgcn_raw_buffer_load_b64(rs, off, 0, 16); }
DI void st_sc1(__amdgpu_buffer_rsrc_t rs, unsigned off, v4u v) { __builtin_amdgcn_raw_buffer_store_b128(v, rs, off, 0, 16); }
DI void st_pz(__amdgpu_buffer_rsrc_t rs, unsigned off, v4u v) { __builtin_amdgcn_raw_buffer_store_b128(v, rs, off, 0, NTP ? 18 : 16); }
DI void st_sc1_64(__amdgpu_buffer_rsrc_t rs, unsigned off, v2u v) { __builtin_amdgcn_raw_buffer_store_b64(v, rs, off, 0, 16); }
DI void st_sc1_32(__amdgpu_buffer_rsrc_t rs, unsigned off, unsigned v) { __builtin_amdgcn_raw_buffer_store_b32(v, rs, off, 0, 16); }
DI unsigned fswz(unsigned row) { return ((row & 3u) << 2) | ((0x78u >> (2u * ((row >> 2) & 3u))) & 3u); }
template <int N> DI float ror16(float v) {
  return __int_as_float(__builtin_amdgcn_update_dpp(0, __float_as_int(v), 0x120 + N, 0xF, 0xF, false));
}
DI float xor1(float v) { return __int_as_float(__builtin_amdgcn_update_dpp(0, __float_as_int(v), 0xB1, 0xF, 0xF, false)); }
DI float add16(float v) { auto r = __builtin_amdgcn_permlane16_swap(__float_as_uint(v), __float_as_uint(v), false, false); return __uint_as_float(r[0]) + __uint_as_float(r[1]); }
DI float add32(float v) { auto r = __builtin_amdgcn_permlane32_swap(__float_as_uint(v), __float_as_uint(v), false, false); return __uint_as_float(r[0]) + __uint_as_float(r[1]); }
DI float rs_add16(float a, float b) { auto r = __builtin_amdgcn_permlane16_swap(__float_as_uint(a), __float_as_uint(b), false, false); return __uint_as_float(r[0]) + __uint_as_float(r[1]); }
DI float rs_add32(float a, float b) { auto r = __builtin_amdgcn_permlane32_swap(__float_as_uint(a), __float_as_uint(b), false, false); return __uint_as_float(r[0]) + __uint_as_float(r[1]); }
DI float rs_max16(float a, float b) { auto r = __builtin_amdgcn_permlane16_swap(__float_as_uint(a), __float_as_uint(b), false, false); return fmaxf(__uint_as_float(r[0]), __uint_as_float(r[1])); }
DI float max32(float v) { auto r = __builtin_amdgcn_permlane32_swap(__float_as_uint(v), __float_as_uint(v), false, false); return fmaxf(__uint_as_float(r[0]), __uint_as_float(r[1])); }
DI float rowsum(float v) { v += ror16<8>(v); v += ror16<4>(v); v += ror16<2>(v); v += ror16<1>(v); return v; }
DI float rowmax(float v) { v = fmaxf(v, ror16<8>(v)); v = fmaxf(v, ror16<4>(v)); v = fmaxf(v, ror16<2>(v)); v = fmaxf(v, ror16<1>(v)); return v; }
DI float ex2(float v) { return __builtin_amdgcn_exp2f(v); }
constexpr float LOG2E = 1.4426950408889634f;

DI void transpose8(const v4u (&r)[8], v4u (&c)[8]) {
#pragma unroll
  for (int q = 0; q < 4; ++q) {
    if (q == 0 || q == 2) { SB0(); MW(); }
#pragma unroll
    for (int e2 = 0; e2 < 4; ++e2) {
      unsigned a = r[2 * e2][q], b = r[2 * e2 + 1][q];
      c[2 * q][e2] = __builtin_amdgcn_perm(b, a, 0x05040100u);
      c[2 * q + 1][e2] = __builtin_amdgcn_perm(b, a, 0x07060302u);
    }
    asm volatile("" : "+v"(c[2 * q]), "+v"(c[2 * q + 1]));
  }
}
#define PLD(f) asm volatile("global_atomic_add_x2 %0, %1, %2, %3 sc0 sc1" : "=&v"(f) : "v"(fo), "v"(zz), "s"(wsp) : "memory")
#define PLDM(f) asm volatile("s_not_b64 exec, %4\n\tglobal_atomic_add_x2 %0, %1, %2, %3 sc0 sc1\n\ts_mov_b64 exec, -1" : "+v"(f) : "v"(fo), "v"(zz), "s"(wsp), "s"(seen) : "memory", "scc")
#define PL1 "global_atomic_add_x2 %0, %2, %3, %4 sc0 sc1\n\t"
#define PL1S PL1 "s_sleep " STR(F2G) "\n\t"
#define PLPRE PL1S PL1S
#define F2XS "s_mov_b64 exec, vcc\n\t"
#define MLX "s_mov_b64 exec, vcc\n\t"
DI void poll_flags(const unsigned char* wsp, unsigned fo, unsigned tag, v2u& f) {
  unsigned cap = SPIN_CAP;
  asm volatile(PLPRE "1:\n\t" PL1 "s_sub_u32 %1, %1, 1\n\ts_waitcnt vmcnt(" STR(F2D) " - 1)\n\tv_cmp_ne_u64 vcc, %5, %0\n\ts_cselect_b64 vcc, 0, vcc\n\t" F2XS
               "s_cbranch_vccnz 1b\n\ts_mov_b64 exec, -1"
               : "=&v"(f), "+s"(cap) : "v"(fo), "v"(v2u{0u, 0u}), "s"(wsp), "s"((u64)tag * 0x100000001ull) : "memory", "vcc", "scc");
}

template <int N> DI unsigned fpos(unsigned l) { return N ? (l % N) * 128 + (l / N) * 8 : l * 8; }
DI void send_flags(__amdgpu_buffer_rsrc_t rs, int wi, int lane, unsigned tag) {
#pragma unroll
  for (int t = 0; t < (T_O == 0 || T_F == 0 || T_UV == 1 ? 4 : 2); ++t) st_sc1_32(rs, WS_FLAG + (64 * t + lane) * 1024 + fpos<FLW>(wi >> 1) + (wi & 1) * 4, tag);
}

#define PIN(x) asm volatile("" : "+v"(x))
__global__ void __launch_bounds__(512, 2) fused_kernel(Params p) {
  extern __shared__ __attribute__((aligned(16))) unsigned char smem[];
  if (CPA == 4) CODE_PF3(blockIdx.x, ~(7 | (CPN - 1) * 16));
  const int tid = threadIdx.x, lane = tid & 63;
  const int w = __builtin_amdgcn_readfirstlane(tid >> 6);
  const int cu = blockIdx.x;
  if (CPA == 3) {
    MK_TABLE();
    CODE_PF2(w + (cu & 8) + (cu >= 16 * CPN ? 8 : 0), L_P + (w & 3) * 1024);
  }
  const u64 t0l = __builtin_amdgcn_s_memrealtime();
  const unsigned t0 = (unsigned)t0l;
  const int xcd = cu & 7, r8 = cu >> 3, idx = r8 >> 1;
  const __amdgpu_buffer_rsrc_t rs = __builtin_amdgcn_make_buffer_rsrc((void*)p.ws, 0, (int)WS_END, 0x00020000);
  const int g4 = lane >> 4, i16 = lane & 15;
  unsigned gen;
  asm volatile("s_load_dword %0, %1, 0x0" : "=s"(gen) : "s"(p.ws), "s"((unsigned)(size_t)(LAS unsigned char*)smem) : "memory");
  unsigned gen1;
#define GET_TAG() (__extension__({ asm volatile("s_waitcnt lgkmcnt(0)\n\ts_add_u32 %0, %1, 1" : "=s"(gen1) : "s"(gen) : "memory", "scc"); (gen1 & 0x3FFFu) | ((gen1 << 2) & 0x3FFF0000u); }))

  if (CPA != 3) MK_TABLE();
  if (CPA == 2) CODE_PF2((r8 & 1) == 0 && idx < CPN ? w : 8, L_P + (w & 3) * 1024);
  if ((r8 & 1) == 0) {
    if (CPA == 1) CODE_PF2(idx < CPN ? w : 8, L_P + (w & 3) * 1024);
    const int b = BMAP ? idx & 7 : xcd, s = BMAP ? xcd + (idx & 8) : idx, kvi = b * 16 + s;
    const unsigned hh = lane >> 5, hd = lane & 31;
    WARM(w, e, idx * 8 + w, 128);
    auto wait_p = [&](unsigned pm, unsigned fm) AI {
      v4u cv;
#pragma unroll 1
      for (unsigned sp = 0; sp < SPIN_CAP * 16u; ++sp) {
        asm volatile("ds_read_b128 %0, %1 offset:%2\n\ts_waitcnt lgkmcnt(0)" : "=v"(cv) : "v"(0u), "i"(L_CNT) : "memory");
        if ((__builtin_amdgcn_readfirstlane(cv[0]) & pm) && (__builtin_amdgcn_readfirstlane(cv[3]) & fm) == fm) break;
      }
      return __builtin_amdgcn_readfirstlane(cv[1]);
    };
    auto wait_bits = [&](unsigned c, unsigned m) AI {
#pragma unroll 1
      for (unsigned sp = 0; sp < SPIN_CAP * 16u; ++sp)
        if ((cnt_get(c) & m) == m) break;
    };
    if (w >= 4) {
      const int k = w - 4;
      (void)GET_TAG();
      unsigned l16 = lane * 16, ap0;
      {
        const unsigned blk = (lane >> 4) & 1, q4 = i16 >> 2, pp = lane & 3;
        const unsigned r0 = 8 * hh + q4;
        const unsigned ch = 16 * k + 2 * (pp & 1) + blk;
        ap0 = L_SLOT + r0 * 1024 + ((ch ^ fswz(r0)) * 16) + 8 * (pp >> 1);
      }
      PIN(l16); PIN(ap0);
      v4u vf[16];
      v16u vo[4];
      unsigned ap1 = ap0 + 65536, yb = 0, vin = 0;
      auto ldv1 = [&](int n) AI {
        const v2u t0_ = trread((ap0 ^ ((n >> 2) * 64)) + (n & 3) * 16384), t1_ = trread((ap0 ^ ((n >> 2) * 64 + 32)) + (n & 3) * 16384 + 4096);
        vf[n] = v4u{t0_[0], t0_[1], t1_[0], t1_[1]};
      };
      auto ldv = [&](int r) AI {
#pragma unroll
        for (int n = 0; n < 16; ++n) {
          ldv1(n);
          if ((n & 7) == 7) { if (r < 2) MS(); else MP(); }
        }
      };
      auto vfr = [&](int r, int n) AI {
        const int q = 4 * (n & 3);
        return (r & 1) ? v4u{vo[n >> 2][q], vo[n >> 2][q + 1], vo[n >> 2][q + 2], vo[n >> 2][q + 3]} : vf[n];
      };
      const char* base = (const char*)p.ckv + ((size_t)kvi * 256 + 16 * k) * 1024;
      unsigned xt = ((lane >> 4) << 16) | (i16 << 10) | (fswz(i16) << 4);
      unsigned ni = 0, sg = 0, ovf = 0, pc = 0, st = 0, fl = 0, cm = 0, cw = 0;
      KV_RUN_BODY();
      f32x16 oacc[4];
      v4u pf[2][4], cv4;
#define CALL0(L) "s_getpc_b64 s[92:93]\n\ts_cbranch_scc0 " L
#define RUN_TXT(L) "s_cmp_lg_u32 s76, s76\n\t" CALL0(L)
#define SIG_TXT "s_getreg_b32 s87, hwreg(HW_REG_IB_STS)\n\ts_and_b32 s87, s87, s66\n\ts_cmp_lg_u32 s87, s65\n\t" CALL0(".Lkv_sig")
#define ISS_TXT "s_cmp_eq_u32 s76, 64\n\t" CALL0(".Lkv_iss")
#define RUN_OUT "+{s76}"(ni), "+{s77}"(sg), "={s84}"(ovf), "={s85}"(pc), "+{s86}"(st), "+{s75}"(fl), "+{s66}"(cm), "+{s65}"(cw)
#define RUN_IN "{s78}"(xm), "{s79}"(pm), "{s[80:81]}"(base), "{s82}"(L_SLOT + k * 16384), "{s83}"(k), "{v250}"(l16), "{v251}"(xt), "{s98}"(yb)
#define VO(c) c "{v[128:143]}"(vo[0]), c "{v[144:159]}"(vo[1]), c "{v[160:175]}"(vo[2]), c "{v[176:191]}"(vo[3])
#define SIG_OPS(c) : "+{s77}"(sg), "+{s66}"(cm), "+{s65}"(cw), "+{s99}"(vin), VO(c) : "{s76}"(ni), "{s83}"(k), "{s98}"(yb), "{v215}"(ap1) : RUN_CL, "vcc"
#define RUN_CL "s64", "s67", "s87", "s88", "s89", "s90", "s91", "s92", "s93", "s94", "s95", "v248", "v249", "scc", "memory"
      auto run = [&](unsigned xm, unsigned pm) AI { asm volatile(RUN_TXT(".Lkv_run") : RUN_OUT, "={v[252:255]}"(cv4) : RUN_IN : RUN_CL); };
#define PF0(c) c "{v[232:235]}"(pf[0][0]), c "{v[236:239]}"(pf[0][1]), c "{v[240:243]}"(pf[0][2]), c "{v[244:247]}"(pf[0][3]), c "{v[252:255]}"(cv4)
#define PF1(c) c "{v[216:219]}"(pf[1][0]), c "{v[220:223]}"(pf[1][1]), c "{v[224:227]}"(pf[1][2]), c "{v[228:231]}"(pf[1][3]), c "{v[252:255]}"(cv4)
#define RUNP_IN RUN_IN, "{s74}"(L_P + r * 4096), "{s68}"(r ? ANY : 0x7Fu)
#define CHK_N 63
      auto runp = [&](int r, unsigned pm, bool chk) AI {
        const unsigned xm = 0;
        if (chk) {
#define CHK_TXT(a, b, c, d) "s_cmp_gt_u32 s76, " STR(CHK_N) "\n\ts_cselect_b32 s89, s79, 0\n\ts_mov_b32 s90, 0xffffff\n\ts_mov_b32 s64, 64\n3:\n\ts_waitcnt lgkmcnt(0)\n\t" \
                "v_readfirstlane_b32 s85, v252\n\tv_readfirstlane_b32 s86, v254\n\tv_readfirstlane_b32 s84, v253\n\tv_readfirstlane_b32 s75, v255\n\t" \
                "s_and_b32 s87, s89, s85\n\ts_getpc_b64 s[92:93]\n\ts_cbranch_scc0 1f\n\t.subsection 11\n\t" MKB "1:\n\t" \
                "s_cmp_eq_u32 s89, 0\n\ts_cbranch_scc1 .Lkv_chk\n\tv_mov_b32 v252, 0\n\tv_add_u32 v249, s74, v250\n\tds_read_b128 v[252:255], v252 offset:" STR(L_CNT_) "\n\t" \
                "ds_read_b128 " a ", v249\n\tds_read_b128 " b ", v249 offset:1024\n\tds_read_b128 " c ", v249 offset:2048\n\tds_read_b128 " d ", v249 offset:3072\n\t" \
                ISS_TXT "\n\t" SIG_TXT "\n\ts_sub_u32 s90, s90, 1\n\ts_cselect_b32 s89, 0, s79\n\ts_cselect_b32 s64, -1, s64\n\ts_branch 3b\n\t.subsection 0"
          if (r & 1) asm volatile(CHK_TXT("v[216:219]", "v[220:223]", "v[224:227]", "v[228:231]") : RUN_OUT, PF1("+") : RUNP_IN : RUN_CL);
          else if (r == 2 && YE) asm volatile(CHK_TXT("v[232:235]", "v[236:239]", "v[240:243]", "v[244:247]") : RUN_OUT, PF0("+"), "+{s99}"(vin), VO("+") : RUNP_IN, "{v215}"(ap1) : RUN_CL, "vcc");
          else asm volatile(CHK_TXT("v[232:235]", "v[236:239]", "v[240:243]", "v[244:247]") : RUN_OUT, PF0("+") : RUNP_IN : RUN_CL);
        } else {
          if (r & 1) asm volatile(RUN_TXT(".Lkv_runp") : RUN_OUT, PF1("=") : RUNP_IN : RUN_CL);
          else asm volatile(RUN_TXT(".Lkv_runp") : RUN_OUT, PF0("=") : RUNP_IN : RUN_CL);
        }
      };
#define SPEC_TXT "v_mov_b32 v252, 0\n\tds_read_b128 %4, v252 offset:%6\n\tds_read_b128 %0, %5 offset:%7\n\tds_read_b128 %1, %5 offset:%7+1024\n\t" \
                 "ds_read_b128 %2, %5 offset:%7+2048\n\tds_read_b128 %3, %5 offset:%7+3072"
      auto spec = [&](int r) AI {
        if (r & 1) asm volatile(SPEC_TXT : PF1("=") : "v"(l16), "i"(L_CNT), "i"(L_P + r * 4096) : "memory");
        else asm volatile(SPEC_TXT : PF0("=") : "v"(l16), "i"(L_CNT), "i"(L_P + r * 4096) : "memory");
      };
      WARM(s, e, k, 4);
      wait_until(t0, KV_DLY);
      run(0u, ANY);
      st = 0; fl = 0;
      wg_barrier();
      MS();
      run(0xFu, ANY);
      ldv(0);
      cnt_addv(C_ST, 0x10000u);
      run(0xF0u, ANY);
      asm volatile(RUN_TXT(".Lkv_ldv") SIG_OPS("="));
      vin = 0;
      cnt_addv(C_ST, 0x100000u);
      auto sigchk = [&](bool y) AI {
        if (y) asm volatile(SIG_TXT SIG_OPS("+"));
        else asm volatile(SIG_TXT : "+{s77}"(sg), "+{s66}"(cm), "+{s65}"(cw) : "{s76}"(ni), "{s83}"(k), "{s98}"(yb) : RUN_CL);
      };
      auto iss = [&]() AI {
        asm volatile(ISS_TXT : "+{s76}"(ni), "+{s66}"(cm), "+{s65}"(cw) : "{s77}"(sg), "{s[80:81]}"(base), "{s82}"(L_SLOT + k * 16384), "{v250}"(l16), "{v251}"(xt) : RUN_CL);
      };
      auto getp = [&](int r, bool chk) AI {
        runp(r, 0xCu << (8 * r), chk);
        if (RARE(ovf & (7u << (8 * r)))) {
          runp(r, 8u << (8 * r), false);
          if (r) {
            const float alpha = lds<float>(L_AL + r * 128 + hd * 4);
#pragma unroll
            for (int ct = 0; ct < 4; ++ct)
#pragma unroll
              for (int e = 0; e < 16; ++e) oacc[ct][e] *= alpha;
          }
        }
        if (CVW) __builtin_amdgcn_s_waitcnt(0xC07F);
        MP();
      };
      unsigned st0 = 0, sa, rx, gx;
      const unsigned sb = L_SLOT + k * 16384;
      auto pv = [&](int r) AI {
#pragma unroll
        for (int n = 0; n < 16; ++n) {
          SB0();
          if (r == 0 && (n & 3) == 0) {
            f32x16 z;
#pragma unroll
            for (int e = 0; e < 16; ++e) z[e] = 0.f;
            oacc[n >> 2] = mfma32(vfr(r, n), pf[r & 1][n & 3], z);
          } else {
            oacc[n >> 2] = mfma32(vfr(r, n), pf[r & 1][n & 3], oacc[n >> 2]);
          }
          PIN(oacc[n >> 2]);
          SB0();
          if (r == 0 && n < 13) ldv1(n);
          if (r == 1 && n < 3) ldv1(13 + n);
          if (r == 1 && n == 4) cnt_addv(C_FL, (st0 & 0xF00u) == 0xF00u ? 0x1000u : 0u);
          if (n == SPAT) spec(r + 1);
          if (r == 2 && n == 2) { PIN(l16); const unsigned ln_ = l16 >> 4; sa = sb + (ln_ & 31) * 272 + (ln_ >> 5) * 64; rx = sb + (ln_ >> 3) * 272 + (ln_ & 7) * 16; PIN(sa); PIN(rx); }
          if (r == 2 && n == 3) {
            PIN(l16);
            const unsigned l8 = l16 >> 7;
            gx = WS_CIN + (l8 * 4 + k) * CIN_WI + (kvi ^ (CSW ? l8 * 16 : 0) ^ (CS2 ? l8 * 2 + (k & 1) : 0)) * CIN_ROW + (l16 & 112u);
            PIN(gx);
          }
          if (ISS && r < 2 && n % ISS == 1) iss();
          if (r ? (r < 2 ? (n & 3) == 3 : n % 3 == 1) : n == 15) sigchk(r == 2);
          if (r == 2 && Y2N && n == Y2N) {
            yb = YB;
            asm volatile("s_cmp_lg_u32 s77, 4\n\t" CALL0(".Lkv_y3") SIG_OPS("+"));
          }
          if ((n & 3) == 2) MP();
        }
      };
      MP();
      if (G0T) { run(0u, 0x7Fu); spec(0); }
      getp(0, G0T);
      if (PPRIO) __builtin_amdgcn_s_setprio(PPRIO);
      st0 = st;
      pv(0);
      getp(1, true);
      pv(1);
      if (RARE((st0 & 0xF00u) != 0xF00u)) {
        MP();
        run(0xF00u, ANY);
        ldv(2);
        cnt_addv(C_FL, 0x1000u);
        spec(2);
      }
      MP();
      if (YE) {
        yb = YE;
        asm volatile("s_cmp_lg_u32 s77, 4\n\t" CALL0(".Lkv_y3") SIG_OPS("="));
      }
      getp(2, true);
      yb = YB;
      if (YE) asm volatile("s_lshl2_add_u32 s87, s99, s77\n\ts_cmp_lg_u32 s87, 4\n\t" CALL0(".Lkv_y3") SIG_OPS("+"));
      else asm volatile("s_cmp_lg_u32 s77, 4\n\t" CALL0(".Lkv_y3") SIG_OPS("="));
      pv(2);
      if (!vin) {
        yb = 0;
        if (RARE(ni != 64)) run(0xF000u, ANY);
        yb = SPIN_CAP * 16u;
        asm volatile("s_getpc_b64 s[92:93]\n\ts_cmp_lg_u32 s99, 0\n\ts_cbranch_scc1 1f\n\ts_cmp_eq_u32 s77, 4\n\ts_cbranch_scc1 .Lkv_y3\n\ts_branch .Lkv_sig\n1:" SIG_OPS("+"));
        spec(3);
      }
      yb = 0;
      {
        MP();
        getp(3, true);
        if (RARE(!(fl & 0x4000u))) wait_bits(C_FL, 0x4000u);
#define GRAN(a, b, c, d) asm volatile("v_cvt_pk_bf16_f32 v" #a ", %0, %1\n\tv_cvt_pk_bf16_f32 v" #b ", %2, %3\n\tv_min_u32 v" #a ", -2, v" #a "\n\t" \
          "v_cvt_pk_bf16_f32 v" #c ", %4, %5\n\tv_cvt_pk_bf16_f32 v" #d ", %6, %7\n\tds_write_b128 %8, v[" #a ":" #d "] offset:%9" \
          :: "v"(o[q]), "v"(o[q + 1]), "v"(o[q + 2]), "v"(o[q + 3]), "v"(o[q + 4]), "v"(o[q + 5]), "v"(o[q + 6]), "v"(o[q + 7]), "v"(sa), \
             "i"((g >> 2) * 128 + (g & 3) * 16) : "v" #a, "v" #b, "v" #c, "v" #d, "memory")
        auto cvt1 = [&](int g) AI {
          const f32x16& o = oacc[g >> 1];
          const int q = 8 * (g & 1);
          if ((g & 3) == 0) GRAN(232, 233, 234, 235);
          if ((g & 3) == 1) GRAN(236, 237, 238, 239);
          if ((g & 3) == 2) GRAN(240, 241, 242, 243);
          if ((g & 3) == 3) GRAN(244, 245, 246, 247);
        };
        v16u ov[2];
#define ORD(a, b, c, d) "ds_read_b128 " a ", %1 offset:%2\n\tds_read_b128 " b ", %1 offset:%2+2176\n\tds_read_b128 " c ", %1 offset:%2+4352\n\tds_read_b128 " d ", %1 offset:%2+6528"
        auto ord = [&](int hf) AI {
          if (hf) asm volatile(ORD("v[144:147]", "v[148:151]", "v[152:155]", "v[156:159]") : "={v[144:159]}"(ov[1]) : "v"(rx), "i"(128) : "memory");
          else asm volatile(ORD("v[128:131]", "v[132:135]", "v[136:139]", "v[140:143]") : "={v[128:143]}"(ov[0]) : "v"(rx), "i"(0) : "memory");
        };
#define OST(a, b, R, hf) asm volatile("s_waitcnt lgkmcnt(%4)\n\tglobal_store_dwordx4 %0, " a ", %1 offset:%5 sc1\n\ts_waitcnt lgkmcnt(%4-1)\n\tglobal_store_dwordx4 %0, " b ", %2 offset:%5 sc1" \
          :: "v"(gx), "s"(p.ws + t * 32 * CIN_WI), "s"(p.ws + (t + 1) * 32 * CIN_WI), R(ov[hf]), "i"(3 - t + c), "i"(hf * 128) : "memory")
        auto ost2 = [&](int hf, int t, int c) AI {
          if (hf == 0 && t == 0) OST("v[128:131]", "v[132:135]", "{v[128:143]}", 0);
          if (hf == 0 && t == 2) OST("v[136:139]", "v[140:143]", "{v[128:143]}", 0);
          if (hf == 1 && t == 0) OST("v[144:147]", "v[148:151]", "{v[144:159]}", 1);
          if (hf == 1 && t == 2) OST("v[152:155]", "v[156:159]", "{v[144:159]}", 1);
        };
#pragma unroll
        for (int n = 0; n < 16; ++n) {
          SB0();
          oacc[n >> 2] = mfma32(vfr(1, n), pf[1][n & 3], oacc[n >> 2]);
          PIN(oacc[n >> 2]);
          SB0();
          if (n >= 5 && n < 15 && ((n - 1) & 2) == 0) cvt1(2 * ((n - 5) >> 2) + ((n - 1) & 1));
          if (n == 10) ord(0);
          SB0();
          if (n == OSAT || n == OSAT + 1) ost2(0, 2 * (n - OSAT), (n >= 13) + (n >= 14) + (MLAT >= 10 && MLAT < n));
          if (n == MLAT) cnt_addv(C_XS, 1u << k);
        }
        SB0();
        if (MLAT == 17) cnt_addv(C_XS, 1u << k);
        asm volatile("s_nop %0" ::"i"(11 - (MLAT == 17 || MLAT == 15 ? 3 : 0) - (OSAT == 14 ? 2 : 0)));
        cvt1(6);
        cvt1(7);
        ord(1);
        SB0();
        ost2(1, 0, 0);
        ost2(1, 2, 0);
        if (MLAT == 18) cnt_addv(C_XS, 1u << k);
      }
    } else {
      const int k = w;
      v2u f0 = {0u, 0u}, f1 = f0, f2 = f0;
      if (CPA == 0) CODE_PF(idx * 4 + k, L_P + k * 1024);
      unsigned tp = 0;
      if (TPF) {
        const unsigned t = idx + 16 * lane + (3 - k) * 64, H = 524288u;
        const char* ta = t < 8 ? (const char*)p.wuv + t * H : t < 10 ? (const char*)p.wo + xcd * H + (t - 8) * (H - 4)
                       : t < 14 ? (const char*)p.wg + min((t - 10) * H, DM * FF * 2u - 4) : t < 18 ? (const char*)p.wu + min((t - 14) * H, DM * FF * 2u - 4)
                       : t < 20 ? (const char*)p.wd + min(xcd * (H / 2) + (t - 18) * (H / 2 - 4), DM * FF * 2u - 4) : t < 21 ? (const char*)p.out
                       : (const char*)p.ws + min((t - 21) * H, WS_END - 4);
        if (TPD) wait_until(t0, TPD);
        asm volatile("v_cmp_gt_u32 vcc, 37, %2\n\ts_mov_b64 exec, vcc\n\tglobal_load_dword %0, %1, off sc1\n\ts_mov_b64 exec, -1" : "=&v"(tp) : "v"(ta), "v"(t) : "memory", "vcc");
      }
      if (WPF == 4) {
        const unsigned po = (32 * idx + 8 * k + (lane >> 3)) * 8192 + xcd * 1024 + (lane & 7) * 128;
        unsigned keep;
        if (KPD) wait_until(t0, KPD);
        asm volatile("s_mov_b32 %0, m0\n\ts_mov_b32 m0, %3\n\ts_nop 0\n\tglobal_load_lds_dword %1, %2\n\ts_mov_b32 m0, %0"
                     : "=&s"(keep) : "v"(po), "s"(p.wuk), "s"(L_P + k * 1024) : "memory");
      }
      if (WPF == 3) {
        const char* pa = (const char*)(hh ? p.wuk : p.wq) + (32 * idx + 8 * k + ((lane >> 3) & 3)) * 8192 + xcd * 1024 + (lane & 7) * 128;
        unsigned keep;
        asm volatile("s_mov_b32 %0, m0\n\ts_mov_b32 m0, %3\n\ts_nop 0\n\tglobal_load_lds_dword %1, off\n\tglobal_load_lds_dword %2, off\n\ts_mov_b32 m0, %0"
                     : "=&s"(keep) : "v"(pa), "v"(pa + 32768), "s"(L_P + k * 1024) : "memory");
      }
      if (k == 0 && lane < C_N) lds<unsigned>(L_CNT + lane * 4) = 0u;
      wg_barrier();
      const unsigned tag = GET_TAG();
      WARM(q, s, k, 4);
      if (QPRIO) __builtin_amdgcn_s_setprio(QPRIO);
      float nr0 = -REF0, nr1 = -REF0;
      v2f lsum = {0.f, 0.f};
      const float OVT = OVTV;
      unsigned nx = 0;
      unsigned aq0 = L_SLOT + (16 * k + i16) * 1024 + ((fswz(i16) ^ g4) * 16);
      unsigned po = L_P + k * 1024 + (i16 + 32 * (g4 >> 1)) * 16 + (g4 & 1) * 8;
      PIN(po); PIN(aq0);
      v4u kf[2][16];
      auto ldk = [&](int r) AI {
#pragma unroll
        for (int u = 0; u < 16; ++u) kf[r & 1][u] = lds128(((aq0 + (r & 1) * 65536) ^ ((u & 3) * 64)) + (u >> 2) * 256);
      };
      v4u sel;
#pragma unroll
      for (int q = 0; q < 4; ++q) sel[q] = ((g4 & 1) == (i16 >> 3) && ((i16 & 7) >> 1) == q) ? ((i16 & 1) ? 0x3F800000u : 0x3F80u) : 0u;
      unsigned qs = WS_QABS + (g4 >> 1) * 262144 + b * 32768 + k * 8192 + (lane & 31) * 16;
      unsigned qd = WS_QABS + b * 32768 + (16 + i16) * 1024 + (60 + g4) * 16;
      unsigned qw[8], qw15;
#pragma unroll
      for (int u = 0; u < 8; ++u) { qw[u] = L_Q + k * 8192 + ((i16 ^ fswz(8 * k + u)) * 16) + (g4 >> 1) * 256 + (g4 & 1) * 8; PIN(qw[u]); }
      qw15 = qw[7] - (k == 3 && g4 >= 2 && i16 >= 12 ? 4096u : 0u);
      unsigned qr = L_Q + i16 * 1024 + ((fswz(i16) ^ g4) * 16);
      PIN(sel); PIN(qs); PIN(qd); PIN(qw15); PIN(qr);
      unsigned mxw = L_Q + (16 + (hd >> 2)) * 1024 + 768 + ((3 ^ ((hd >> 2) & 3)) * 64) + (hd & 3) * 16 + k * 4;
      unsigned mxr = L_Q + (16 + (i16 >> 2)) * 1024 + 768 + ((3 ^ (i16 >> 2)) * 64) + (i16 & 3) * 16;
      PIN(mxw); PIN(mxr);
      wait_bits(C_ST, 1u << k);
      ldk(0);
      cnt_addv(C_ST, 1u << (24 + k));
      MQ();
      if (K1AT == 0) {
        wait_bits(C_ST, 16u << k);
        ldk(1);
        cnt_addv(C_ST, 1u << (28 + k));
        MQ();
      }
      v4u qf[32], qv[16], da, db;
      const unsigned char* wsp = p.ws;
      const unsigned char* wsp2 = p.ws + 4096;
#define QLD(t) asm volatile("global_load_dwordx4 %0, %1, %2 offset:%3 sc1" : "=v"(qv[t]) : "v"(qs), "s"((t) < 8 ? wsp : wsp2), "i"(((t) & 7) * 512) : "memory")
      if (k == 0) {
        wait_until(t0, POLL_START);
        const unsigned fo = WS_FLAG + kvi * 1024 + fpos<FLW>(lane);
        const v2u zz = {0u, 0u};
        u64 seen = 0, seen1 = 0;
        bool sent = false;
        auto chk = [&](v2u& f) AI {
          asm volatile("s_waitcnt vmcnt(2)" : "+v"(f)::"memory");
          const unsigned x = (f[0] ^ tag) | (f[1] ^ tag);
          u64 ok = __ballot(x == 0) | seen;
          if (SPK) {
            seen1 |= __ballot((x & ~0x8000u) == 0);
            if (seen1 == ~0ull && __popcll(~ok) <= SPK) ok = ~0ull;
          }
          if (ok != seen) {
            seen = ok;
            lds<v2u>(L_CNT + C_MASK * 4) = v2u{(unsigned)ok, (unsigned)(ok >> 32)};
            if (PAUSE && !sent && __popcll(~ok) <= PK) { cnt_addv(C_FL, 0x100u); sent = true; }
          }
          return ok == ~0ull;
        };
        MQ();
        PLD(f0); __builtin_amdgcn_s_sleep(POLL_GAP);
        PLD(f1); __builtin_amdgcn_s_sleep(POLL_GAP);
        PLD(f2);
#pragma unroll 1
        for (unsigned spin = 0; spin < SPIN_CAP; ++spin) {
          if (chk(f0)) break;
          if (QPM) PLDM(f0); else PLD(f0);
          MQ();
          if (chk(f1)) break;
          if (QPM) PLDM(f1); else PLD(f1);
          MQ();
          if (chk(f2)) break;
          if (QPM) PLDM(f2); else PLD(f2);
        }
        MQ();
#pragma unroll
        for (int t = 0; t < 16; ++t) QLD(t);
      } else {
        unsigned pend = 0xFFFFu;
#pragma unroll 1
        for (unsigned spin = 0; spin < SPIN_CAP * 16u; ++spin) {
          v2u mk;
          asm volatile("ds_read_b64 %0, %1 offset:%2\n\ts_waitcnt lgkmcnt(0)" : "=v"(mk) : "v"(C_MASK * 4u), "i"(L_CNT) : "memory");
          const unsigned m0 = __builtin_amdgcn_readfirstlane(mk[0]), m1 = __builtin_amdgcn_readfirstlane(mk[1]);
          const unsigned nw = ((k < 2 ? m0 : m1) >> (16 * (k & 1))) & pend;
          if (PROG ? nw != 0u : (m0 & m1) == ~0u) {
            pend &= ~nw;
#pragma unroll
            for (int t = 0; t < 16; ++t)
              if (nw >> t & 1u) { QLD(t); if ((t & 7) == 0) MQ(); }
          }
          if ((m0 & m1) == ~0u) break;
          __builtin_amdgcn_s_sleep(1);
        }
      }
#define QLD2() asm volatile("global_load_dwordx4 %0, %2, %3 sc1\n\tglobal_load_dwordx4 %1, %2, %4 sc1" : "=&v"(da), "=&v"(db) : "v"(qd), "s"(wsp), "s"(wsp + 262144) : "memory")
      QLD2();
      cnt_add(C_FL);
      MQ();
      if (K1AT == 1) {
        wait_bits(C_ST, 16u << k);
        ldk(1);
        cnt_addv(C_ST, 1u << (28 + k));
      }
#pragma unroll 1
      for (unsigned spin = 0; spin < SPIN_CAP; ++spin) {
        if (spin) {
#pragma unroll
          for (int t = 0; t < 16; ++t) QLD(t);
          QLD2();
        }
        MQ();
        asm volatile("s_waitcnt vmcnt(2)" : W8(qv, 0), W8(qv, 8), "+v"(f0), "+v"(f1), "+v"(f2), "+v"(tp)::"memory");
        float chk = 0.f;
        f32x4 d[16];
#pragma unroll
        for (int t = 0; t < 18; ++t) {
          if (t < 16) d[t] = mfma16(sel, qv[t], f32x4{0.f, 0.f, 0.f, 0.f});
          if (t >= 2) {
            const int u = t - 2;
            lds<v2u>((u == 15 ? qw15 : qw[u >> 1]) + (u >> 1) * 1024 + (u & 1) * 512) = v2u{pk2(d[u][0], d[u][1]), pk2(d[u][2], d[u][3])};
            chk += d[u][0];
          }
          SB0();
          if ((t & 3) == 1) MQ();
        }
        asm volatile("s_waitcnt vmcnt(0)" : "+v"(da), "+v"(db)::"memory");
        if (!__any(chk != chk || max(da[0], db[0]) == POISON)) break;
        unsigned mx = max(da[0], db[0]);
#pragma unroll
        for (int t = 0; t < 16; ++t) mx = max(mx, qv[t][0]);
        if (!__any(mx == POISON)) break;
        CFENCE();
      }
      MQ();
      cnt_addv(C_FL, 16u);
#pragma unroll
      for (int e = 0; e < 4; ++e) qf[31][e] = pk2(bf2f(da[e]) + bf2f(db[e]), bfhi(da[e]) + bfhi(db[e]));
      PIN(qf[31]);
      MQ();
      wait_bits(C_FL, 0x40u);
      MQ();
      f32x4 s0 = {-REF0, -REF0, -REF0, -REF0}, s1 = s0, a0 = {0.f, 0.f, 0.f, 0.f}, a1 = a0, t0, t1;
      auto ldq = [&](int f) AI { qf[(f >> 1) + 16 * (f & 1)] = lds128((qr ^ (((f >> 1) & 3) * 64)) + (f >> 3) * 256 + (f & 1) * 16384); };
      v4u kn[2][16], cs;
      auto ldn = [&](int r, int u) AI { kn[r & 1][u] = lds128(((aq0 + (r & 1) * 65536) ^ ((u & 3) * 64)) + (u >> 2) * 256); };
#define CRD() asm volatile("ds_read_b128 %0, %1 offset:%2" : "=v"(cs) : "v"(0u), "i"(L_CNT) : "memory")
#define CWT() asm volatile("s_waitcnt lgkmcnt(0)" : "+v"(cs)::"memory")
#pragma unroll
      for (int f = 0; f < 12; ++f) ldq(f);
#pragma unroll
      for (int u = 0; u < 16; ++u) {
        SB0();
        s0 = mfma16(kf[0][u], qf[u], s0);
        s1 = mfma16(kf[0][u], qf[16 + u], s1);
        if (MIX && (u & 1)) a1 = mfma16(kf[1][u >> 1], qf[16 + (u >> 1)], a1);
        else if (MIX) a0 = mfma16(kf[1][u >> 1], qf[u >> 1], a0);
        SB0();
        if ((u & 3) == 0) MQ();
        if (12 + 2 * u < 31) ldq(12 + 2 * u);
        if (13 + 2 * u < 31) ldq(13 + 2 * u);
        if (u == 9) cnt_addv(C_P, 0x10u);
        if (u == GR) CRD();
      }
      MQ();
      unsigned cx;
      f32x4 ma, mb;
      auto max4 = [&](const f32x4& v) AI { return fmaxf(fmaxf(v[0], v[1]), fmaxf(v[2], v[3])); };
      auto post_max = [&]() AI {
        const float mw = max32(rs_max16(max4(s0), max4(s1)));
        lds<float>(mxw) = mw;
        cnt_add(C_MX); nx += 4;
      };
#define XRD(c, w) asm volatile("ds_read_b32 %0, %3 offset:%5\n\tds_read_b128 %1, %4\n\tds_read_b128 %2, %4 offset:4096" w : "=&v"(cx), "=&v"(ma), "=&v"(mb) : "v"(0u), "v"(mxr), "i"(L_CNT + (c) * 4) : "memory")
      auto exchange = [&](int r) AI {
        asm volatile("s_waitcnt lgkmcnt(0)" : "+v"(cx), "+v"(ma), "+v"(mb)::"memory");
        if (__builtin_amdgcn_readfirstlane(cx) < nx) {
#pragma unroll 1
          for (unsigned sp = 0; sp < SPIN_CAP * 16u; ++sp) {
            XRD(C_MX, "\n\ts_waitcnt lgkmcnt(0)");
            if (__builtin_amdgcn_readfirstlane(cx) >= nx) break;
          }
        }
        float d0 = max4(ma), d1 = max4(mb);
        if (r) {
          d0 = fmaxf(d0, 0.f); d1 = fmaxf(d1, 0.f);
          const float al0 = ex2(-d0), al1 = ex2(-d1);
          lsum[0] *= al0; lsum[1] *= al1;
          lds<float>(L_AL + r * 128 + hd * 4) = (lane & 16) ? al1 : al0;
        }
#pragma unroll
        for (int e = 0; e < 4; ++e) { s0[e] -= d0; s1[e] -= d1; }
        nr0 -= d0; nr1 -= d1;
      };
      v2f ln;
      auto do_p = [&](int r, const f32x4& x0, const f32x4& x1, unsigned ov) AI {
        float e0[4], e1[4];
#pragma unroll
        for (int e = 0; e < 4; ++e) { e0[e] = ex2(x0[e]); e1[e] = ex2(x1[e]); }
        lds<v2u>(po + r * 4096) = v2u{pk2(e0[0], e0[1]), pk2(e0[2], e0[3])};
        lds<v2u>(po + r * 4096 + 256) = v2u{pk2(e1[0], e1[1]), pk2(e1[2], e1[3])};
        cnt_add2(C_P, 1u << (8 * r), ov);
        ln = lsum + ((v2f{e0[0], e1[0]} + v2f{e0[1], e1[1]}) + (v2f{e0[2], e1[2]} + v2f{e0[3], e1[3]}));
      };
      auto redo = [&](int r) AI {
        post_max();
        XRD(C_MX, "");
        exchange(r);
        do_p(r, s0, s1, 0u);
      };
      auto fin = [&](int r, const f32x4& x0, const f32x4& x1) AI {
        MQ();
#pragma unroll
        for (int e = 0; e < 4; ++e) { s0[e] = x0[e] + nr0; s1[e] = x1[e] + nr1; }
        const unsigned ov = __any(fmaxf(max4(s0), max4(s1)) > OVT) ? 1u << (8 * r) : 0u;
        do_p(r, s0, s1, ov);
      };
      auto refchk = [&](int r, unsigned pc, unsigned ov) AI {
        ov = __builtin_amdgcn_readfirstlane(ov);
        if (RARE(!(__builtin_amdgcn_readfirstlane(pc) >> (8 * r) & 12u))) { MQ(); ov = wait_p(12u << (8 * r), 0u); }
        if (RARE(ov & (7u << (8 * r)))) redo(r);
        lsum = ln;
        MQ();
      };
      v2u cr;
#pragma unroll
      for (int u = 0; u < 16; ++u) {
        SB0();
        a0 = mfma16(kf[1][u], qf[u], a0);
        a1 = mfma16(kf[1][u], qf[16 + u], a1);
        PIN(a0); PIN(a1);
        SB0();
        if ((u & 3) == 0) MQ();
        if (u == FA) {
          asm volatile("s_waitcnt lgkmcnt(%1)" : "+v"(cs) : "i"(FA) : "memory");
          if (RARE(!(__builtin_amdgcn_readfirstlane(cs[0]) & 0x40u))) wait_bits(C_P, 0x40u);
          const float mw = max32(rs_max16(max4(s0), max4(s1)));
          do_p(0, s0, s1, __any(mw > OVT || mw < -UVTV) ? 1u : 0u);
        }
        if (u == CK0 - 4) asm volatile("ds_read_b64 %0, %1 offset:%2" : "=v"(cr) : "v"(0u), "i"(L_CNT) : "memory");
        if (u == CK0) {
          asm volatile("s_waitcnt lgkmcnt(4)" : "+v"(cr)::"memory");
          refchk(0, cr[0], cr[1]);
        }
        ldn(2, u);
      }
      fin(1, a0, a1);
      MQ();
      if (RARE(!(__builtin_amdgcn_readfirstlane(cs[2]) >> (8 + k) & 1u))) {
        MQ();
        wait_bits(C_ST, 0x100u << k);
#pragma unroll
        for (int u = 0; u < 16; ++u) ldn(2, u);
        __builtin_amdgcn_s_waitcnt(0xC07F);
      }
      MQ();
      t0 = f32x4{0.f, 0.f, 0.f, 0.f}; t1 = t0;
      unsigned ak[4];
#pragma unroll
      for (int u = 0; u < 16; ++u) {
        SB0();
        t0 = mfma16(kn[0][u], qf[u], t0);
        t1 = mfma16(kn[0][u], qf[16 + u], t1);
        PIN(t0); PIN(t1);
        SB0();
        if ((u & 7) == 0) MQ();
        if (u < 4) { ak[u] = (aq0 + 65536) ^ (u * 64); PIN(ak[u]); }
        if (u == CKAT - 4) CRD();
        if (u == CKAT) { CWT(); refchk(1, cs[0], cs[1]); }
      }
      fin(2, t0, t1);
      CRD();
      t0 = f32x4{nr0, nr0, nr0, nr0}; t1 = f32x4{nr1, nr1, nr1, nr1};
      PIN(t0); PIN(t1); PIN(ln);
      MQ();
      unsigned bad;
      {
        unsigned cap = SPIN_CAP * 16u, tv;
        asm volatile("s_branch 2f\n1:\n\tds_read_b128 v[200:203], %3 offset:%4\n\ts_sub_u32 %2, %2, 1\n\ts_cselect_b64 exec, 0, exec\n2:\n\ts_waitcnt lgkmcnt(0)\n\t"
                     "v_and_b32 %1, %5, v202\n\tv_cmp_eq_u32 vcc, 0, %1\n\ts_cbranch_vccnz 1b\n\ts_mov_b64 exec, -1"
                     : "+{v[200:203]}"(cs), "=&v"(tv), "+s"(cap) : "v"(0u), "i"(L_CNT), "s"(0x1000u << k) : "memory", "vcc", "scc");
      }
#pragma unroll
      for (int u = 0; u < 16; ++u) {
        SB0();
        kn[1][u] = lds128(ak[u & 3] + (u >> 2) * 256);
        if (u == 7) MQ();
        if (u == 9) {
          asm volatile("" : "+v"(cs)::"memory");
          bad = __builtin_amdgcn_readfirstlane((cs[1] & 0x70000u) | ((cs[0] & 0xC0000u) == 0u ? 1u : 0u));
          asm volatile("" : "+s"(bad));
        }
      }
      SB0();
      if (RARE(bad)) {
        MQ();
        if (RARE(wait_p(0xC0000u, 0u) & 0x70000u)) {
          redo(2);
          t0 = f32x4{nr0, nr0, nr0, nr0}; t1 = f32x4{nr1, nr1, nr1, nr1};
        }
      }
      lsum = ln;
#pragma unroll
      for (int n = 0; n < 16 + QL; ++n) {
        SB0();
        if (n < 16) t0 = mfma16(kn[1][n], qf[n], t0);
        if (n >= QL) t1 = mfma16(kn[1][n - QL], qf[16 + n - QL], t1);
        SB0();
        if ((n & 7) == 0) MQ();
      }
      s0 = t0; s1 = t1;
      do_p(3, s0, s1, __any(fmaxf(max4(s0), max4(s1)) > OVT) ? 1u << 24 : 0u);
      MQ();
      if (RARE(wait_p(0xC000000u, 0u) & 0x7000000u)) redo(3);
      lsum = ln;
      {
        MQ();
        const float lt = add32(rs_add16(lsum[0], lsum[1]));
        lds<float>(L_LS + k * 128 + hd * 4) = lt;
        cnt_add(C_MX);
        nx += 4;
        cnt_wait(C_MX, nx);
        const LAS float* ls = (const LAS float*)(size_t)(L_LS + hd * 4);
        v4u mlv = {__float_as_uint((lane & 16) ? -nr1 : -nr0), tag, __float_as_uint((ls[0] + ls[32]) + (ls[64] + ls[96])), tag};
        unsigned gm = WS_ML + ((hd * 4 + k) * 128 + (kvi ^ (MS2 ? (hd & 7) * 16 : 0))) * 16;
        PIN(mlv); PIN(gm);
        MQ();
        if (QST) {
          unsigned rx = L_SLOT + k * 16384 + (lane >> 3) * 272 + (lane & 7) * 16;
          unsigned gx = WS_CIN + ((lane >> 3) * 4 + k) * CIN_WI + (kvi ^ (CSW ? (lane >> 3) * 16 : 0)) * CIN_ROW + (lane & 7) * 16;
          PIN(rx); PIN(gx);
          MQ();
          wait_bits(C_XS, 16u << k);
          v4u ov[4];
#pragma unroll
          for (int t = 0; t < 4; ++t) ov[t] = lds128(rx + t * 2176);
#pragma unroll
          for (int t = 0; t < 4; ++t) __builtin_amdgcn_raw_buffer_store_b128(ov[t], rs, gx, t * 32 * CIN_WI, 16);
          MQ();
        }
        wait_bits(C_XS, 1u << k);
        if (lane < 32) st_sc1(rs, gm, mlv);
      }
    }
  } else {
    const int h = 4 * xcd + (idx & 3), j = idx >> 2, wi = 4 * h + j;
    const bool ffn = wi < 86;
    const unsigned hh = lane >> 5;
    v4u xa, xb;
    asm volatile("s_cmp_gt_u32 %3, 3\n\ts_cbranch_scc1 1f\n\tglobal_load_dwordx4 %0, %2, %4\n\tglobal_load_dwordx4 %1, %2, %4 offset:512\n1:"
                 : "=&v"(xa), "=&v"(xb) : "v"((2 * w + hh) * 1024 + (lane & 31) * 16), "s"(w), "s"(p.x) : "memory", "scc");
    constexpr int NT = FR4 ? 2 : 4;
    const int hf = FMAP ? (idx & 7) + 8 * (xcd & 3) : h, jf = FMAP ? (idx >> 3) + 2 * (xcd >> 2) : j;
    const int fr = FR4 ? 1 : __builtin_amdgcn_readfirstlane((jf >> 1) ^ 1);
    const int dj = jf & 1, c0 = (FR4 ? (jf >> 1) * 256 : 0) + w * 16 * NT;
    unsigned qb = WS_QABS + dj * 262144 + (i16 * 32 + hf) * 1024 + (c0 + 8 * g4) * 2;
    auto ffn_pf = [&](unsigned& d) {
      const int u = idx * 8 + w, q5 = lane / 5, r5 = lane - 5 * q5;
      const char* pb = (const char*)((q5 & 1) ? p.wu : p.wg) + (4 * u + (min(q5, 7) >> 1)) * (FF * 2) + min(xcd * 512 + min(r5 * 128, 508), FF * 2 - 4);
      asm volatile("global_load_dword %0, %1, off" : "=&v"(d) : "v"(pb) : "memory");
    };
    unsigned dpf = 0;
    SB0();
    unsigned d0 = 0;
    if (WPF) {
      unsigned d1;
      if (WPF == 3) {
        wait_until(t0, PFT);
      } else if (WPF == 1) {
        const unsigned po = (32 * idx + 4 * w + g4) * 8192 + xcd * 1024 + i16 * 64;
        asm volatile("global_load_dword %0, %4, %5\n\tglobal_load_dword %1, %4, %6\n\ts_waitcnt vmcnt(0)" : "=&v"(d0), "=&v"(d1), "+v"(xa), "+v"(xb) : "v"(po), "s"(p.wq), "s"(p.wuk) : "memory");
      } else {
        const char* pa = (const char*)(hh ? p.wuk : p.wq) + (32 * idx + 4 * w + ((lane >> 3) & 3)) * 8192 + xcd * 1024 + (lane & 7) * 128;
        if (WPF == 6) {
          const char* pb = (const char*)((w & 4) ? p.wuk : p.wq) + ((xcd * 16 + idx) * 4 + (w & 3)) * 8192 + lane * 128;
          asm volatile("global_load_dword %0, %1, off" : "=&v"(d0) : "v"(pb) : "memory");
        } else if (WPF == 7) {
          const unsigned row = 32 * idx + 4 * w + ((lane >> 3) & 3);
          const char* pb = (const char*)(hh ? p.wuk : p.wq) + row * 8192 + (xcd ^ (row & 1)) * 1024 + (lane & 7) * 128;
          asm volatile("global_load_dword %0, %1, off" : "=&v"(d0) : "v"(pb) : "memory");
        } else if (WPF >= 4) {
          const unsigned po = (32 * idx + 8 * (w & 3) + (lane >> 3)) * 8192 + xcd * 1024 + (lane & 7) * 128;
          asm volatile("s_cmp_lt_u32 %3, 4\n\ts_cbranch_scc1 1f\n\tglobal_load_dword %0, %1, %2\n1:" : "=&v"(d0) : "v"(po), "s"(p.wq), "s"(w) : "memory", "scc");
        } else
        if (WPM == 4) asm volatile("s_mov_b32 exec_hi, 0\n\tglobal_load_dword %0, %1, off\n\ts_not_b64 exec, exec\n\tglobal_load_dword %0, %1, off sc0 nt\n\ts_mov_b64 exec, -1" : "=&v"(d0) : "v"(pa) : "memory", "scc");
        else if (WPM == 6) asm volatile("global_load_dword %0, %2, off sc0 nt\n\tglobal_load_dword %1, %2, off sc0" : "=&v"(d0), "=&v"(dpf) : "v"(pa) : "memory");
        else if (WPM == 7) asm volatile("global_load_dword %0, %2, off sc0 nt\n\ts_sleep 10\n\tglobal_load_dword %1, %2, off sc0" : "=&v"(d0), "=&v"(dpf) : "v"(pa) : "memory");
        else if (WPM == 8) asm volatile("global_load_dword %0, %2, off sc0 nt\n\ts_sleep 10\n\tglobal_load_dword %1, %2, off offset:64" : "=&v"(d0), "=&v"(dpf) : "v"(pa) : "memory");
        else if (WPM == 5) asm volatile("s_mov_b32 exec_hi, 0\n\tglobal_load_dword %0, %1, off sc0 nt\n\ts_not_b64 exec, exec\n\tglobal_load_dword %0, %1, off\n\ts_mov_b64 exec, -1" : "=&v"(d0) : "v"(pa) : "memory", "scc");
        else asm volatile("global_load_dword %0, %1, off" WPN : "=&v"(d0) : "v"(pa) : "memory");
        if (PZT) asm volatile("global_load_dword %0, %1, %2" : "=&v"(dpf) : "v"(WS_CIN + wi * CIN_WI + w * 4096), "s"(p.ws) : "memory");
        if (PZE == 1) {
#pragma unroll
          for (int t = 0; t < 4; ++t) st_sc1(rs, WS_CIN + wi * CIN_WI + w * 4096 + t * 1024 + lane * 16, v4u{POISON, POISON, POISON, POISON});
          CFENCE();
        }
        if (w >= 4) {
          wait_until(t0, WM_DLY);
          WARM(w, q, w - 4, 4);
          if (PZE == 3) VMWAIT(0);
        } else if (PZE == 3) {
          wait_until(t0, PZ_AT);
#pragma unroll
          for (int t = 0; t < 8; ++t) st_sc1(rs, WS_CIN + wi * CIN_WI + w * 8192 + t * 1024 + lane * 16, v4u{POISON, POISON, POISON, POISON});
          CFENCE();
        }
        if (SPK && F1AT < 2 && w < 4) {
          st_sc1(rs, WS_QABS + dj * 262144 + ((2 * w + hh) * 32 + hf) * 1024 + (jf >> 1) * 512 + (lane & 31) * 16, v4u{POISON, POISON, POISON, POISON});
          asm volatile("s_waitcnt vmcnt(1)" : "+v"(d0), "+v"(xa), "+v"(xb)::"memory");
        } else
        if (PFT) wait_until(t0, PFT);
        else asm volatile("s_waitcnt vmcnt(%4)" : "+v"(d0), "+v"(xa), "+v"(xb), "+v"(dpf) : "i"(PZE == 1 ? 4 : PZE == 3 ? 8 : 0) : "memory");
      }
      if (WPF_DLY) __builtin_amdgcn_s_sleep(WPF_DLY);
    }
    if (!PFT) __builtin_amdgcn_s_barrier();
    MW();
    const unsigned tag = GET_TAG();
    v4u wukf[8];
    if (fr) {
      const char* src = (const char*)p.wq + hf * 256 + dj * 128 + (size_t)(64 * w) * 8192;
#pragma unroll
      for (int t = 0; t < 8; ++t) {
        glds16s((lane >> 3) * 8192 + (((lane & 7) ^ (((g4 & 1) | ((t & 1) << 1)) << 1)) * 16), src + t * 65536, L_WQ + (64 * w + 8 * t) * 128);
        if (t == 3) MW();
      }
      MW();
    }
    {
      const unsigned vk = (8 * (i16 >> 2) + (lane & 3)) * 8192 + g4 * 16;
      const char* kb = (const char*)p.wuk + hf * 256 + dj * 128 + (size_t)c0 * 8192;
#pragma unroll
      for (int u = 0; u < NT / 2; ++u)
        asm volatile("s_cmp_eq_u32 %5, 0\n\ts_cbranch_scc1 1f\n\t"
                     "global_load_dwordx4 %0, %4, %6" NT_K "\n\tglobal_load_dwordx4 %1, %4, %6 offset:64" NT_K "\n\t"
                     "global_load_dwordx4 %2, %4, %7" NT_K "\n\tglobal_load_dwordx4 %3, %4, %7 offset:64" NT_K "\n1:"
                     : "=&v"(wukf[4 * u]), "=&v"(wukf[4 * u + 1]), "=&v"(wukf[4 * u + 2]), "=&v"(wukf[4 * u + 3])
                     : "v"(vk), "s"(fr), "s"(kb + 32 * u * 8192), "s"(kb + (32 * u + 4) * 8192) : "memory", "scc");
    }
    {
      MW();
      const v4u pz = {POISON, POISON, POISON, POISON};
      if (SPK && F1AT == 2 && w < 4) st_sc1(rs, WS_QABS + dj * 262144 + ((2 * w + hh) * 32 + hf) * 1024 + (jf >> 1) * 512 + (lane & 31) * 16, pz);
      CFENCE();
      if (!SPK && fr && i16 < 8) {
#pragma unroll
        for (int u = 0; u < NT / 2; ++u) st_sc1(rs, qb + u * 64, pz);
      }
      if (PZE == 2) {
        if (w >= 4) {
          if (PZ_DLY) __builtin_amdgcn_s_sleep(PZ_DLY);
          MW();
#pragma unroll
          for (int t = 0; t < 2; ++t) st_sc1(rs, WS_EIN + wi * 8192 + (w - 4) * 2048 + t * 1024 + lane * 16, pz);
#pragma unroll
          for (int t = 0; t < 8; ++t) st_sc1(rs, WS_CIN + wi * CIN_WI + (w - 4) * 8192 + t * 1024 + lane * 16, pz);
          VMWAIT(2 * NT + NT / 2 + 10);
        }
      } else {
      st_pz(rs, WS_EIN + wi * 8192 + w * 1024 + lane * 16, pz);
      if (!PZE) {
#pragma unroll
        for (int t = 0; t < 4; ++t) st_pz(rs, WS_CIN + wi * CIN_WI + w * 4096 + t * 1024 + lane * 16, pz);
      }
      }
    }
    if (w < 4) {
      asm volatile("s_waitcnt vmcnt(%3)\n\ts_cmp_lg_u32 %2, 0\n\ts_cbranch_scc1 1f\n\ts_waitcnt vmcnt(1)\n1:" : "+v"(xa), "+v"(xb) : "s"(fr), "i"(8 + 2 * NT + NT / 2 + 1 + (WPF == 3 || (WPF >= 4 && PFT) ? 4 : PFT ? 5 : 8)) : "memory", "scc");
      MW();
      auto flag1 = [&]() AI {
        VMWAIT(F1AT == 2 ? 5 : 8 + 2 * NT + 5);
        if (lane < 32) st_sc1_32(rs, WS_FLAG + (32 * w + lane) * 1024 + fpos<FLW>(wi >> 1) + (wi & 1) * 4, tag ^ 0x8000u);
        CFENCE();
      };
      if (SPK && F1AT == 0) flag1();
      v2f v[8];
#pragma unroll
      for (int e = 0; e < 4; ++e) { v[e] = v2f{bf2f(xa[e]), bfhi(xa[e])}; v[4 + e] = v2f{bf2f(xb[e]), bfhi(xb[e])}; }
      const unsigned p0 = __builtin_amdgcn_readlane(__float_as_uint(v[0][0]), 0), p1 = __builtin_amdgcn_readlane(__float_as_uint(v[0][0]), 32);
      const float piv = __uint_as_float(hh ? p1 : p0);
      v2f sm = {0.f, 0.f}, ss = {0.f, 0.f};
#pragma unroll
      for (int e = 0; e < 8; ++e) {
        if (e == 3) { SB0(); MW(); }
        const v2f d = v[e] - v2f{piv, piv}; sm += d; ss += d * d;
      }
      SB0();
      MW();
      float t = rowsum(rs_add16(sm[0] + sm[1], ss[0] + ss[1]));
      auto sw2 = __builtin_amdgcn_permlane16_swap(__float_as_uint(t), __float_as_uint(t), false, false);
      const float md = __uint_as_float(sw2[0]) * (1.f / DM);
      const float var = fmaxf(fmaf(-md, md, __uint_as_float(sw2[1]) * (1.f / DM)), 0.f);
      const float mb = rbf(piv + md), vb = rbf(var);
      const float rden = __builtin_amdgcn_rcpf(rbf(__builtin_amdgcn_sqrtf(rbf(vb + 1e-5f))));
      SB0();
      MW();
      v4u o4[2];
#pragma unroll
      for (int e = 0; e < 8; ++e) {
        if (e == 3 || e == 6) { SB0(); MW(); }
        const v2f d = v[e] - v2f{mb, mb};
        const unsigned u = pk2(d[0], d[1]);
        const v2f o = v2f{bf2f(u), bfhi(u)} * v2f{rden, rden};
        o4[e >> 2][e & 3] = pk2(o[0], o[1]);
      }
      const unsigned xo = L_XN + (2 * w + hh) * 1040 + (lane & 31) * 16;
      lds<v4u>(xo) = o4[0];
      lds<v4u>(xo + 512) = o4[1];
      if (tid < C_N) lds<unsigned>(L_CNT + tid * 4) = 0u;
      if (PZE == 2) VMWAIT(2 * NT + NT / 2);
      if (SPK && F1AT >= 1) flag1();
    }
    MW();
    unsigned qa = L_QH + (lane & 7) * 144 + g4 * 16;
    {
      unsigned ta = L_WQ + (8 * g4 + (i16 >> 2)) * 128 + (((2 * w + ((lane & 3) >> 1)) ^ (((i16 >> 3) | ((g4 & 1) << 1)) << 1)) * 16) + 8 * (lane & 1);
      unsigned xq = L_XN + (lane & 7) * 1040 + g4 * 16;
      unsigned qw = L_QH + (lane & 7) * 144 + (16 * w + 4 * g4) * 2;
      PIN(ta); PIN(xq); PIN(qw); PIN(qa); PIN(qb);
      if (SPK && w >= 4) VMWAIT(2 * NT + 1 + 4);
      else if (PZE != 2) VMWAIT(2 * NT + NT / 2 + 1 + (PZE ? 0 : 4));
      PIN(d0);
      wg_barrier();
      MW();
      if (fr && w < 4) {
        f32x4 c0a = {0.f, 0.f, 0.f, 0.f}, c1a = c0a;
#pragma unroll
        for (int ks = 0; ks < 16; ks += 2) {
          if (ks == 4 || ks == 10) MW();
          const v4u x0 = lds128(xq + ks * 64), x1 = lds128(xq + ks * 64 + 64);
          const v2u a0 = trread(ta + ks * 4096), a1 = trread(ta + ks * 4096 + 512), b0 = trread(ta + ks * 4096 + 4096), b1 = trread(ta + ks * 4096 + 4608);
          c0a = mfma16(v4u{a0[0], a0[1], a1[0], a1[1]}, x0, c0a);
          c1a = mfma16(v4u{b0[0], b0[1], b1[0], b1[1]}, x1, c1a);
        }
        MW();
        c0a += c1a;
        lds<v2u>(qw) = v2u{pk2(c0a[0], c0a[1]), pk2(c0a[2], c0a[3])};
      }
    }
    MW();
    wg_barrier();
    {
      const v4u q0 = lds128(qa), q1 = lds128(qa + 64);
      if (NT == 4) asm volatile("s_waitcnt vmcnt(0)" : W8(wukf, 0)::"memory");
      else asm volatile("s_waitcnt vmcnt(0)" : "+v"(wukf[0]), "+v"(wukf[1]), "+v"(wukf[2]), "+v"(wukf[3])::"memory");
      const bool last = cnt_add_rtn(C_QST) == 7u;
      if (FLAG_FIRST && last) send_flags(rs, wi, lane, tag);
      if (fr) {
        const float sc = 0.08838834764831845f * LOG2E;
#pragma unroll
        for (int u = 0; u < NT / 2; ++u) {
          f32x4 d0 = mfma16(wukf[4 * u], q0, f32x4{0.f, 0.f, 0.f, 0.f}), d1 = mfma16(wukf[4 * u + 2], q0, f32x4{0.f, 0.f, 0.f, 0.f});
          d0 = mfma16(wukf[4 * u + 1], q1, d0);
          d1 = mfma16(wukf[4 * u + 3], q1, d1);
          d0 *= sc; d1 *= sc;
          if (i16 < 8) st_sc1(rs, qb + u * 64, v4u{san(pk2(d0[0], d0[1])), pk2(d0[2], d0[3]), pk2(d1[0], d1[1]), pk2(d1[2], d1[3])});
        }
      }
      MW();
      if (!FLAG_FIRST && last) send_flags(rs, wi, lane, tag);
      if (last) cnt_add(C_SENT);
      if (KPF) {
        if (KPF_AT) wait_until(t0, KPF_AT + (w ^ 4) * KPF_ST);
        if (KPF_S) cnt_wait<1>(C_SENT, 1);
        if (KPF == 1) {
          const unsigned ko = (unsigned)(xcd * 16 + idx) * 262144u + 196608u + w * 8192 + lane * 128;
          asm volatile("global_load_dword %0, %1, %2" : "=&v"(dpf) : "v"(ko), "s"(p.ckv) : "memory");
        } else {
          const unsigned ko = min((unsigned)lane * 131072u + (w & 1) * 65536u + idx * 4096u, WS_END - 4u);
          asm volatile("s_cmp_gt_u32 %3, %4\n\ts_cbranch_scc1 1f\n\tglobal_load_dword %0, %1, %2 sc1\n1:" : "=&v"(dpf) : "v"(ko), "s"(p.ws), "s"(w), "i"(KPF - 3) : "memory", "scc");
        }
      }
      if (FPF == 3 && xcd < 6) {
        cnt_wait<1>(C_SENT, 1);
        ffn_pf(dpf);
        asm volatile("s_waitcnt vmcnt(0)" : "+v"(dpf)::"memory");
      }
    }
    if (w == 4 && (T_O == 0 || T_F == 0 || T_UV == 1)) {
      v2u f0;
      MW();
      wait_until(t0, POLL_START);
      poll_flags(p.ws, WS_FLAG + (128 + wi) * 1024 + fpos<FLW>(lane), tag, f0);
      MW();
      if (GO_DLY) wait_until(now32(), GO_DLY);
      cnt_add(C_GO);
      asm volatile("s_waitcnt vmcnt(0)" : "+v"(f0)::"memory");
    }
    MW();
    v4u wof[4][8];
    const int jr = OROT ? j : 0;
    f32x4 oc[8];
    if (w >= 4) {
      const int hk = w - 4;
      if (T_O) { wait_until(t0, T_O); cnt_wait<1>(C_SENT, 1); } else cnt_wait<2>(C_GO, 1);
      v4u wor[4][8], wdr[8], wdf[8];
      {
        const unsigned vo = 8 * g4 * 1024 + i16 * 16, vd = 8 * (g4 & 1) * 1024 + i16 * 16;
        const char* so = (const char*)p.wo + (size_t)(h * 128) * 1024 + hk * 256;
        const char* sd = (const char*)p.wd + (size_t)(ffn ? 16 * wi : 0) * 1024 + hk * 256;
        if (OPF) asm volatile("global_load_dword %0, %1, %2 sc0 nt" : "=&v"(dpf) : "v"((32 * j + 8 * hk) * 1024 + lane * 128), "s"((const char*)p.wo + (size_t)(h * 128) * 1024) : "memory");
#pragma unroll
        for (int n = 0; n < 40; ++n) {
          if (OPF && n == 8) { VMWAIT(8); if (OPD) __builtin_amdgcn_s_sleep(OPD); }
          if (n < 8) asm volatile("global_load_dwordx4 %0, %1, %2" NT_D : "=v"(wdr[n]) : "v"(vd), "s"(sd + n * 1024) : "memory");
          else if (NTO == 2 && n < 16) asm volatile("global_load_dwordx4 %0, %1, %2 nt" : "=v"(wor[0][n & 7]) : "v"(vo), "s"(so + (32 * j + (n & 7)) * 1024) : "memory");
          else asm volatile("global_load_dwordx4 %0, %1, %2" NT_O : "=v"(wor[(n - 8) >> 3][n & 7]) : "v"(vo), "s"(so + (32 * ((((n - 8) >> 3) + jr) & 3) + (n & 7)) * 1024) : "memory");
          if (n >= NFW - 1 && n < 39) VMWAIT(NFW - 1);
          if ((n & 7) == 0) MW();
        }
      }
      asm volatile("s_waitcnt vmcnt(32)" : W8(wdr, 0), "+v"(dpf)::"memory");
      transpose8(wdr, wdf);
      asm volatile("s_waitcnt vmcnt(24)" : W8(wor[0], 0)::"memory");
      transpose8(wor[0], wof[0]);
      asm volatile("s_waitcnt vmcnt(16)" : W8(wor[1], 0)::"memory");
      transpose8(wor[1], wof[1]);
      asm volatile("s_waitcnt vmcnt(8)" : W8(wor[2], 0)::"memory");
      transpose8(wor[2], wof[2]);
      asm volatile("s_waitcnt vmcnt(0)" : W8(wor[3], 0)::"memory");
      transpose8(wor[3], wof[3]);
#pragma unroll
      for (int jj = 0; jj < 8; ++jj) oc[jj] = f32x4{0.f, 0.f, 0.f, 0.f};
      if (ffn) {
        MW();
        cnt_wait<1>(C_FFN, 1);
        const v4u a = lds128(L_HF + ((lane & 7) ^ ((lane & 8) >> 2)) * 80 + g4 * 16);
#pragma unroll
        for (int jj = 0; jj < 8; ++jj) oc[jj] = mfma16(a, wdf[jj], oc[jj]);
      }
#pragma unroll
      for (int u = 0; u < 4; ++u) asm volatile("" : W8(wof[u], 0));
      asm volatile("" : W8(oc, 0));
    } else {
      MW();
      if (T_F) { wait_until(t0, T_F); cnt_wait<1>(C_SENT, 1); } else cnt_wait<2>(C_GO, 1);
      if (h < 22) {
        const char* src = (const char*)(((i16 >> 3) ^ (g4 >> 1)) ? p.wu : p.wg) + (size_t)(128 * j + 32 * w + g4) * (FF * 2) + h * 128;
#pragma unroll
        for (int t = 0; t < 8; ++t) {
          glds16<NTF>(src + (size_t)t * 4 * (FF * 2) + min((i16 ^ fswz(4 * t + g4)) & 7u, h == 21 ? 3u : 7u) * 16, L_GU + (32 * w + 4 * t) * 256);
          if (t & 1) MW();
        }
      }
      auto ld_uv = [&]() {
#pragma unroll
        for (int t = 0; t < 8; ++t) {
          const unsigned row = 32 * w + 4 * t + g4;
          if ((t & 1) == 0) MW();
          glds16<NTU>((const char*)p.wuv + (size_t)(128 * j + row) * 8192 + h * 256 + ((i16 ^ fswz(row)) * 16), L_WUV + (32 * w + 4 * t) * 256);
        }
      };
      if (!T_UV) ld_uv();
      if (h < 22) {
        if (T_UV) VMWAIT(0); else VMWAIT(8);
        MW();
        cnt_add(C_GU);
        cnt_wait(C_GU, 4);
        f32x4 ag = {0.f, 0.f, 0.f, 0.f}, au = {0.f, 0.f, 0.f, 0.f};
        const unsigned ra = 8 * g4 + (i16 >> 2), ch = 2 * w + ((lane & 3) >> 1);
        const unsigned r0 = L_GU + ra * 256 + ((ch ^ fswz(ra)) * 16) + 8 * (lane & 1), r1 = L_GU + (ra + 4) * 256 + ((ch ^ fswz(ra + 4)) * 16) + 8 * (lane & 1);
        MW();
#pragma unroll
        for (int ks = 0; ks < 4; ++ks) {
          const v4u a = lds128(L_XN + (lane & 7) * 1040 + (128 * j + 32 * ks + 8 * g4) * 2);
          const v2u b0 = trread(r0 + ks * 8192), b1 = trread(r1 + ks * 8192);
          const v2u c0 = trread((r0 ^ 128) + ks * 8192), c1 = trread((r1 ^ 128) + ks * 8192);
          if (ks == 2) MW();
          ag = mfma16(a, v4u{b0[0], b0[1], b1[0], b1[1]}, ag);
          au = mfma16(a, v4u{c0[0], c0[1], c1[0], c1[1]}, au);
        }
        MW();
        const v4u pv = __builtin_bit_cast(v4u, hh ? au : ag);
        const unsigned fo = WS_FIN + (4 * h + w) * 8192 + j * 2048 + lane * 16;
        st_sc1(rs, fo, v4u{pv[0], pv[1], pv[2], tag});
        st_sc1(rs, fo + 1024, v4u{pv[3], pv[3], pv[3], tag});
      }
      if (T_UV) {
        if (T_UV > 1) wait_until(t0, T_UV); else cnt_wait<2>(C_GO, 1);
        ld_uv();
      }
      asm volatile("s_waitcnt vmcnt(0)" : "+v"(dpf)::"memory");
      cnt_add(C_UV);
      if (ffn && w == 1) {
        v4u fv[8];
#pragma unroll 1
        for (unsigned spin = 0; spin < SPIN_CAP; ++spin) {
          MW();
          unsigned bad = 0;
#pragma unroll
          for (int t = 0; t < 8; ++t) fv[t] = ld_sc1(rs, WS_FIN + wi * 8192 + t * 1024 + lane * 16);
#pragma unroll
          for (int t = 0; t < 8; ++t) bad |= fv[t][3] ^ tag;
          if (!__any(bad != 0u)) break;
          __builtin_amdgcn_s_sleep(FX_SLP);
          CFENCE();
        }
        MW();
        float sv[4];
#pragma unroll
        for (int e = 0; e < 4; ++e) {
          const int o = e == 3, q = e % 3;
          sv[e] = (__uint_as_float(fv[o][q]) + __uint_as_float(fv[2 + o][q])) + (__uint_as_float(fv[4 + o][q]) + __uint_as_float(fv[6 + o][q]));
        }
#pragma unroll
        for (int e = 0; e < 2; ++e) {
          SB0();
          MW();
          const auto gu = __builtin_amdgcn_permlane32_swap(__float_as_uint(sv[e]), __float_as_uint(sv[e + 2]), false, false);
          const float gg = rbf(__uint_as_float(gu[0])), uu = rbf(__uint_as_float(gu[1]));
          const float inner = 0.7978845608028654f * (gg + 0.044715f * gg * gg * gg);
          const float th = 1.f - 2.f / (__expf(2.f * inner) + 1.f);
          SB0();
          MW();
          const float ge = rbf(0.5f * gg * (1.f + th));
          lds<u16>(L_HF + (4 * (g4 & 1) + 2 * hh + e) * 80 + i16 * 2) = (u16)(pk2(ge * uu, 0.f) & 0xFFFFu);
        }
        if (lane < 16) lds<v4u>(L_HF + (lane >> 1) * 80 + 32 + (lane & 1) * 16) = v4u{0u, 0u, 0u, 0u};
        MW();
        cnt_add(C_FFN);
      }
    }
    v4u uvf[4];
    MW();
    cnt_wait<2>(C_UV, 4);
#pragma unroll
    for (int ks = 0; ks < 4; ++ks) {
      const unsigned ra = 32 * (2 * (ks & 1) + (g4 >> 1)) + 16 * (ks >> 1) + 8 * (g4 & 1) + (i16 >> 2), rb = ra + 4, ch = 2 * w + ((lane & 3) >> 1);
      const v2u b0 = trread(L_WUV + ra * 256 + ((ch ^ fswz(ra)) * 16) + 8 * (lane & 1));
      const v2u b1 = trread(L_WUV + rb * 256 + ((ch ^ fswz(rb)) * 16) + 8 * (lane & 1));
      uvf[ks] = v4u{b0[0], b0[1], b1[0], b1[1]};
      PIN(uvf[ks]);
      MW();
    }
    unsigned cx = WS_CIN + wi * CIN_WI + (w ^ (CSW ? h & 7 : 0)) * 4096 + lane * 16;
    unsigned clx = L_CL + w * 272 + (((i16 >> 2) & 1) * 32 + (i16 >> 3) * 16 + (i16 & 3) * 4 + g4) * 4;
    PIN(cx); PIN(clx);
    MW();
    unsigned cla = L_CL + (lane & 7) * 272 + g4 * 16;
    unsigned cxw = L_CTX + (lane & 7) * 272 + (16 * w + 4 * g4) * 2;
    PIN(cla); PIN(cxw);
    MW();
    unsigned cxa[4];
#pragma unroll
    for (int u = 0; u < 4; ++u) { cxa[u] = L_CTX + ((lane & 7) ^ ((lane & 8) >> 2)) * 272 + (32 * ((u + jr) & 3) + 8 * g4) * 2; PIN(cxa[u]); }
    unsigned eb = w >= 4 ? WS_EIN + ((4 * (g4 & 1) + 2 * hh) * 16 + 4 * (w - 4) + (i16 >> 2)) * 8192 + (wi ^ (ES2 ? (lane >> 2) * 4 : 0)) * 64 + (i16 & 3) * 16 : WS_EIN + wi * 8192 + lane * 16;
    unsigned f2a = WS_FLAG2 + (64 * (w & 1) + lane) * 1024 + fpos<FLW2>(wi >> 1) + (wi & 1) * 4;
    PIN(f2a);
    MW();
    unsigned msk[4];
#pragma unroll
    for (int q = 0; q < 4; ++q) { msk[q] = (i16 >> 1) == 2 * q ? ((i16 & 1) ? 0xFFFF0000u : 0xFFFFu) : 0u; PIN(msk[q]); }
    MW();
    PIN(cx); PIN(clx); PIN(cla); PIN(cxw); PIN(eb);
    {
      v2u m0, m1;
      unsigned ml, cap = SPIN_CAP;
      v4u dv[4];
      const unsigned ma = WS_ML + (wi * 128 + (w ^ (MS2 ? h & 7 : 0)) * 16) * 16 + ((lane & 31) ^ (CS2 ? (h & 7) * 4 + (j & 1) * 2 : 0)) * 8;
#define MLD(r) "global_atomic_add_x2 " r ", %8, %9, %10 sc0 sc1\n\t"
#define DLD "s_mov_b64 exec, -1\n\tglobal_load_dwordx4 %0, %11, %10 sc1\n\tglobal_load_dwordx4 %1, %11, %10 offset:1024 sc1\n\t" \
            "global_load_dwordx4 %2, %11, %10 offset:2048 sc1\n\tglobal_load_dwordx4 %3, %11, %10 offset:3072 sc1"
#define MLOPS : "=&v"(dv[0]), "=&v"(dv[1]), "=&v"(dv[2]), "=&v"(dv[3]), "=&{v[248:249]}"(m0), "=&{v[250:251]}"(m1), "=&v"(ml), "+s"(cap) \
              : "v"(ma), "v"(v2u{0u, 0u}), "s"(p.ws), "v"(cx), "s"(tag), "s"(0xFFFFFFFFull) : "memory", "vcc", "scc"
      if (MLDP == 2)
        asm volatile("s_mov_b64 exec, %13\n\t" MLD("v[248:249]") "s_sleep " STR(MLG) "\n1:\n\t" MLD("v[250:251]")
                     "s_waitcnt vmcnt(1)\n\tv_cmp_ne_u32 vcc, %12, v249\n\tv_mov_b32 %6, v248\n\ts_mov_b64 exec, vcc\n\ts_cbranch_vccz 2f\n\t" MLD("v[248:249]")
                     "s_sub_u32 %7, %7, 1\n\ts_waitcnt vmcnt(1)\n\tv_cmp_ne_u32 vcc, %12, v251\n\tv_mov_b32 %6, v250\n\ts_cselect_b64 vcc, 0, vcc\n\t"
                     "s_mov_b64 exec, vcc\n\ts_cbranch_vccnz 1b\n2:\n\t" DLD MLOPS);
      else
        asm volatile("s_mov_b64 exec, %13\n1:\n\t" MLD("v[248:249]") "s_sub_u32 %7, %7, 1\n\ts_waitcnt vmcnt(0)\n\t"
                     "v_cmp_ne_u32 vcc, %12, v249\n\tv_mov_b32 %6, v248\n\ts_cselect_b64 vcc, 0, vcc\n\t" MLX "s_cbranch_vccnz 1b\n\t" DLD MLOPS);
      MW();
      v4u aw[4];
      {
        const float o = xor1(__uint_as_float(ml));
        const float ms = (lane & 1) ? o : __uint_as_float(ml), lsv = (lane & 1) ? __uint_as_float(ml) : o;
        float M = rowmax(ms);
        M = rs_max16(M, M);
        const float ee = ex2(ms - M);
        const float wsv = 2.f * ee / add16(rowsum(ee * lsv));
        unsigned wb = pk2(wsv, wsv);
        PIN(wb);
        MW();
#pragma unroll
        for (int t = 0; t < 4; ++t) {
          const unsigned u = __shfl(wb, 8 * t + 2 * g4);
          aw[t] = v4u{u & msk[0], u & msk[1], u & msk[2], u & msk[3]};
          PIN(aw[t]);
          if (t & 1) MW();
        }
      }
      asm volatile("s_waitcnt vmcnt(0)" : "+v"(dv[0]), "+v"(dv[1]), "+v"(dv[2]), "+v"(dv[3]), "+{v[248:249]}"(m0), "+{v[250:251]}"(m1)::"memory");
      if (RARE(__any(max(max(dv[0][0], dv[1][0]), max(dv[2][0], dv[3][0])) == POISON))) {
#pragma unroll 1
        for (unsigned spin = 0; spin < SPIN_CAP; ++spin) {
          MW();
#pragma unroll
          for (int t = 0; t < 4; ++t) dv[t] = ld_sc1(rs, cx + t * 1024);
          if (!__any(max(max(dv[0][0], dv[1][0]), max(dv[2][0], dv[3][0])) == POISON)) break;
          CFENCE();
        }
      }
      MW();
      f32x4 dx = {0.f, 0.f, 0.f, 0.f}, dy = dx;
#pragma unroll
      for (int t = 0; t < 2; ++t) { dx = mfma16(aw[t], dv[t], dx); dy = mfma16(aw[t + 2], dv[t + 2], dy); }
      lds<unsigned>(clx) = pk2(dx[0] + dy[0], dx[1] + dy[1]);
    }
    wg_barrier();
    MW();
    if (F2AT == 0 && (w == 2 || w == 3)) st_sc1_32(rs, f2a, tag);
    {
      v4u a[4];
#pragma unroll
      for (int ks = 0; ks < 4; ++ks) a[ks] = lds128(cla + ks * 64);
      SB0();
      f32x4 c0 = mfma16(uvf[0], a[0], f32x4{0.f, 0.f, 0.f, 0.f}), c1 = mfma16(uvf[1], a[1], f32x4{0.f, 0.f, 0.f, 0.f});
      c0 = mfma16(uvf[2], a[2], c0);
      c1 = mfma16(uvf[3], a[3], c1);
      c0 += c1;
      lds<v2u>(cxw) = v2u{pk2(c0[0], c0[1]), pk2(c0[2], c0[3])};
    }
    wg_barrier();
    MW();
    if (__builtin_expect(w >= 4, 1)) {
      v4u a[4];
#pragma unroll
      for (int u = 0; u < 4; ++u) a[u] = lds128(cxa[u]);
      SB0();
#pragma unroll
      for (int u = 0; u < 4; ++u) {
#pragma unroll
        for (int jj = 0; jj < 8; ++jj) oc[jj] = mfma16(a[u], wof[u][jj], oc[jj]);
        if (u & 1) { SB0(); MW(); }
      }
#pragma unroll
      for (int e = 0; e < 2; ++e)
        __builtin_amdgcn_raw_buffer_store_b128(v4u{san(pk2(oc[0][e], oc[1][e])), san(pk2(oc[2][e], oc[3][e])), san(pk2(oc[4][e], oc[5][e])), san(pk2(oc[6][e], oc[7][e]))},
                                               rs, eb, e * 16 * 8192, 16);
    } else if (w == 0) {
      const unsigned oo = wi * 64 + (i16 & 3) * 16 + g4 * 4;
      const unsigned xf = *(const GAS unsigned*)((const char*)p.x + oo);
      const unsigned one = (i16 & 2) ? 0u : ((i16 & 1) ? 0x3F800000u : 0x3F80u);
      v4u sel;
#pragma unroll
      for (int q = 0; q < 4; ++q) sel[q] = (i16 >> 2) == q ? one : 0u;
      v2u f0;
      unsigned eb2 = eb + 4096;
      float x0 = bf2f(xf), x1 = bfhi(xf);
      PIN(sel); PIN(eb2); PIN(x0); PIN(x1);
      MW();
      poll_flags(p.ws, WS_FLAG2 + wi * 1024 + fpos<FLW2>(lane), tag, f0);
      MW();
      if (E_DLY) __builtin_amdgcn_s_sleep(E_DLY);
      float r0, r1;
      v4u ev[8];
      auto esum = [&]() AI {
#pragma unroll
        for (int t = 0; t < 8; ++t) ev[t] = ld_sc1(rs, (t < 4 ? eb : eb2) + (t & 3) * 1024);
        SB0();
        f32x4 d[4];
#pragma unroll
        for (int t = 0; t < 8; ++t) d[t & 3] = mfma16(sel, ev[t], t < 4 ? f32x4{0.f, 0.f, 0.f, 0.f} : d[t & 3]);
        MW();
        r0 = (d[0][0] + d[1][0]) + (d[2][0] + d[3][0]); r1 = (d[0][1] + d[1][1]) + (d[2][1] + d[3][1]);
        r0 += ror16<8>(r0); r1 += ror16<8>(r1);
        r0 += ror16<4>(r0); r1 += ror16<4>(r1);
      };
      esum();
      if (RARE(__any(r0 != r0))) {
#pragma unroll 1
        for (unsigned spin = 0; spin < SPIN_CAP; ++spin) {
          MW();
          unsigned mxv = 0;
#pragma unroll
          for (int t = 0; t < 8; ++t) mxv = max(mxv, max(max(ev[t][0], ev[t][1]), max(ev[t][2], ev[t][3])));
          if (__all(mxv != POISON)) break;
          CFENCE();
          MW();
          esum();
          if (!__any(r0 != r0)) break;
        }
      }
      MW();
      asm volatile("s_mov_b64 exec, %3\n\tglobal_store_dword %0, %1, %2\n\ts_mov_b64 exec, -1" ::"v"(oo), "v"(pk2(x0 + r0, x1 + r1)), "s"(p.out), "s"(0x000F000F000F000Full) : "memory");
      asm volatile("s_waitcnt vmcnt(0)" : "+v"(f0)::"memory");
    } else if (w >= 2) {
      MW();
      if (F2AT == 1) st_sc1_32(rs, f2a, tag);
      if (wi == 0 && tid == 128) st_sc1_32(rs, WS_GEN, gen1);
    }
  }
}

extern "C" void kernel_launch(void* const* d_in, const int* in_sizes, int n_in, void* d_out, int out_size, void* d_ws,
                              size_t ws_size, hipStream_t stream) {
  Params p;
  p.x = (const u16*)d_in[0];
  p.ckv = (const u16*)d_in[1];
  p.wq = (const u16*)d_in[2];
  p.wuk = (const u16*)d_in[3];
  p.wuv = (const u16*)d_in[4];
  p.wo = (const u16*)d_in[5];
  p.wg = (const u16*)d_in[7];
  p.wu = (const u16*)d_in[8];
  p.wd = (const u16*)d_in[9];
  p.out = (u16*)d_out;
  p.ws = (unsigned char*)d_ws;
  (void)hipFuncSetAttribute((const void*)fused_kernel, hipFuncAttributeMaxDynamicSharedMemorySize, LDS_BYTES);
  fused_kernel<<<256, 512, LDS_BYTES, stream>>>(p);
}
```
